```python
import torch

def _softmax(x, axis=-1):
    return torch.softmax(x, dim=axis)

def _gelu(x):
    return torch.nn.functional.gelu(x, approximate='tanh')

def _silu(x):
    return torch.nn.functional.silu(x)

def _sigmoid(x):
    return torch.sigmoid(x)

def _layernorm(x, axis=-1, eps=1e-05):
    m = x.mean(dim=axis, keepdim=True)
    v = x.var(dim=axis, keepdim=True, unbiased=False)
    return (x - m) / torch.sqrt(v + eps)

def _scatter_add(tgt, idx, src, axis=0):
    return tgt.clone().index_add_(axis, idx, src)

def _segment_sum(src, seg, n):
    out = torch.zeros((n,) + src.shape[1:], dtype=src.dtype)
    return out.index_add_(0, seg, src)

def _pad(x, pw):
    flat = tuple((v for pair in reversed(pw) for v in pair))
    return torch.nn.functional.pad(x, flat)
kernel_name = 'adaLN_cond__compressed_kv_cla__dense_activation_mish__prompt'

def setup_inputs():

    _w = lambda a: (lambda t: t.to(torch.bfloat16) if t.is_floating_point() else t)(torch.as_tensor(a))

    def _A_setup():
        B, H, Sq, Sk, D, M = (1, 32, 2048, 2048, 128, 512)
        Dc = 4 * D
        return {'x': _w((lambda *s: torch.randn(*s, dtype=torch.bfloat16) * 0.05)(B, Sq, M)), 'c_kv': _w((lambda *s: torch.randn(*s, dtype=torch.bfloat16) * 0.4)(B, Sk, Dc)), 'w_q': _w((lambda *s: torch.randn(*s, dtype=torch.bfloat16) * 3.2)(M, H, D) * M ** (-0.5)), 'w_uk': _w((lambda *s: torch.randn(*s, dtype=torch.bfloat16) * 3.2)(Dc, H, D) * Dc ** (-0.5)), 'w_uv': _w((lambda *s: torch.randn(*s, dtype=torch.bfloat16) * 0.4)(Dc, H, D) * Dc ** (-0.5)), 'w_o': _w((lambda *s: torch.randn(*s, dtype=torch.bfloat16) * 0.4)(H, D, M) * (H * D) ** (-0.5))}
    _A_in = _A_setup()

    def _B_setup():
        B, S, D, F = (1, 2048, 512, 1792)
        return {'x': _w((lambda *s: torch.randn(*s, dtype=torch.bfloat16) * 0.05)(B, S, D)), 'w1': _w((lambda *s: torch.randn(*s, dtype=torch.bfloat16) * 0.8)(D, F) * D ** (-0.5)), 'b1': _w((lambda *s: torch.randn(*s, dtype=torch.bfloat16) * 0.2)(F)), 'w2': _w((lambda *s: torch.randn(*s, dtype=torch.bfloat16) * 0.1)(F, D) * F ** (-0.5))}
    _B_in = _B_setup()
    return {'A_x': _A_in['x'], 'A_c_kv': _A_in['c_kv'], 'A_w_q': _A_in['w_q'], 'A_w_uk': _A_in['w_uk'], 'A_w_uv': _A_in['w_uv'], 'A_w_o': _A_in['w_o'], 'B_x': _B_in['x'], 'B_w1': _B_in['w1'], 'B_b1': _B_in['b1'], 'B_w2': _B_in['w2'], 'c': _w((lambda *s: torch.randn(*s, dtype=torch.bfloat16) * 0.1)(1, 512) * 8.0), 'w_mod': _w((lambda *s: torch.randn(*s, dtype=torch.bfloat16) * 0.1)(512, 6 * 512) * 512 ** (-0.5) * 8.0)}

def reference(A_x, A_c_kv, A_w_q, A_w_uk, A_w_uv, A_w_o, B_x, B_w1, B_b1, B_w2, c, w_mod):

    def _A_ref(x, c_kv, w_q, w_uk, w_uv, w_o):
        B, Sq, M = (x.shape[0], x.shape[1], x.shape[2])
        Sk = c_kv.shape[1]
        H, D = (w_q.shape[1], w_q.shape[2])
        q = torch.einsum('bqm,mhd->bhqd', x, w_q)
        k = torch.einsum('bkc,chd->bhkd', c_kv, w_uk)
        v = torch.einsum('bkc,chd->bhkd', c_kv, w_uv)
        scores = torch.einsum('bhqd,bhkd->bhqk', q, k) * D ** (-0.5)
        off = Sk - Sq
        qi = torch.arange(Sq)[:, None]
        kj = torch.arange(Sk)[None, :]
        cmask = kj <= qi + off
        scores = torch.where(cmask[None, None], scores, -1000000000.0)
        p = _softmax(scores, axis=-1)
        ctx = torch.einsum('bhqk,bhkd->bhqd', p, v)
        return torch.einsum('bhqd,hdm->bqm', ctx, w_o)

    def _B_ref(x, w1, b1, w2):
        h = torch.einsum('bsd,df->bsf', x, w1) + b1
        sp = torch.log1p(torch.exp(-torch.abs(h))) + torch.where(h > 0, h, torch.zeros_like(h))
        act = h * torch.tanh(sp)
        return torch.einsum('bsf,fd->bsd', act, w2)
    x = A_x
    mod = (_silu(c) @ w_mod).reshape(c.shape[0], 6, 1, -1)
    sa, ca, ga, sb, cb, gb = (mod[:, 0].clone(), mod[:, 1].clone(), mod[:, 2].clone(), mod[:, 3].clone(), mod[:, 4].clone(), mod[:, 5].clone())
    h = _layernorm(x, axis=-1) * (1 + ca) + sa
    x = x + ga * _A_ref(h, A_c_kv, A_w_q, A_w_uk, A_w_uv, A_w_o)
    h = _layernorm(x, axis=-1) * (1 + cb) + sb
    x = x + gb * _B_ref(h, B_w1, B_b1, B_w2)
    return x
```

```cpp
#include <hip/hip_runtime.h>
#include <hip/hip_bf16.h>
#include <stdint.h>
typedef unsigned short u16;
typedef short bf16x8 __attribute__((ext_vector_type(8)));
typedef float f32x16 __attribute__((ext_vector_type(16)));
typedef float f32x4 __attribute__((ext_vector_type(4)));
#define DI __device__ __forceinline__
DI float bf2f(u16 x){ return __uint_as_float(((unsigned)x)<<16); }
DI u16 f2bf(float x){ unsigned u=__float_as_uint(x); u += 0x7fffu + ((u>>16)&1u); return (u16)(u>>16); }
constexpr int S=2048, M=512, H=32, D=128, F=1792;

__global__ void k_mod(const u16* c, const u16* wmod, float* mod){
  int n = blockIdx.x*64 + threadIdx.x;
  float acc=0.f;
  for(int k=0;k<512;++k){ float x=bf2f(c[k]); float s=x/(1.f+__expf(-x)); s=bf2f(f2bf(s)); acc+= s*bf2f(wmod[k*3072+n]); }
  mod[n]=bf2f(f2bf(acc));
}
__global__ void k_ln(const u16* x, const float* sh, const float* sc, u16* h){
  int row=blockIdx.x, l=threadIdx.x;
  float v[8]; float s=0;
  for(int j=0;j<8;++j){ v[j]=bf2f(x[row*512+l*8+j]); s+=v[j]; }
  for(int o=32;o;o>>=1) s+=__shfl_xor(s,o);
  float mean=s/512.f; float q=0;
  for(int j=0;j<8;++j){ float d=v[j]-mean; q+=d*d; }
  for(int o=32;o;o>>=1) q+=__shfl_xor(q,o);
  float r=rsqrtf(q/512.f+1e-5f);
  for(int j=0;j<8;++j){ int m=l*8+j; h[row*512+m]=f2bf((v[j]-mean)*r*(1.f+sc[m])+sh[m]); }
}
template<int EPI>
__global__ void k_gemm(const u16* A, const u16* B, u16* C, int K, int N, const u16* res, const float* g, const u16* bias){
  int lane=threadIdx.x&63, w=threadIdx.x>>6;
  int n0=(blockIdx.x*4+w)*32, m0=blockIdx.y*32; if(n0>=N) return;
  int r=lane&31,h=lane>>5;
  f32x16 acc={};
  for(int k=0;k<K;k+=16){
    bf16x8 a=*(const bf16x8*)(A+(size_t)(m0+r)*K+k+8*h);
    bf16x8 b; for(int j=0;j<8;++j) b[j]=B[(size_t)(k+8*h+j)*N+n0+r];
    acc=__builtin_amdgcn_mfma_f32_32x32x16_bf16(a,b,acc,0,0,0);
  }
  for(int i=0;i<16;++i){ int row=m0+(i&3)+8*(i>>2)+4*h, col=n0+r; float v=acc[i];
    if(EPI==1){ v=bf2f(res[(size_t)row*N+col])+g[col]*v; }
    if(EPI==2){ v+=bf2f(bias[col]); float sp=log1pf(__expf(-fabsf(v)))+fmaxf(v,0.f); v=v*tanhf(sp); }
    C[(size_t)row*N+col]=f2bf(v);
  }
}
__global__ __launch_bounds__(64) void k_attn(const u16* q,const u16* k,const u16* v,u16* ctx){
  int lane=threadIdx.x, r=lane&31, hh=lane>>5; int h=blockIdx.x, qb=63-blockIdx.y;
  bf16x8 qf[8]; for(int s=0;s<8;++s) qf[s]=*(const bf16x8*)(q+(size_t)(qb*32+r)*4096+h*128+16*s+8*hh);
  f32x16 o[4]={}; float m=-1e30f,l=0.f; const float C=0.08838834764831845f*1.4426950408889634f;
  for(int kb=0;kb<=qb;++kb){
    f32x16 s={};
    for(int i=0;i<8;++i){ bf16x8 kf=*(const bf16x8*)(k+(size_t)(kb*32+r)*4096+h*128+16*i+8*hh); s=__builtin_amdgcn_mfma_f32_32x32x16_bf16(kf,qf[i],s,0,0,0);}
    float mx=-1e30f;
    for(int i=0;i<16;++i){ int key=kb*32+(i&3)+8*(i>>2)+4*hh; s[i]= key<=qb*32+r ? s[i]*C : -INFINITY; mx=fmaxf(mx,s[i]); }
    mx=fmaxf(mx,__shfl_xor(mx,32)); float mn=fmaxf(m,mx), al=exp2f(m-mn); m=mn; float ps=0;
    for(int i=0;i<16;++i){ s[i]=exp2f(s[i]-mn); ps+=s[i]; }
    ps+=__shfl_xor(ps,32); l=l*al+ps;
    for(int d=0;d<4;++d) for(int i=0;i<16;++i) o[d][i]*=al;
    for(int st=0;st<2;++st){ bf16x8 p; for(int j=0;j<8;++j) p[j]=f2bf(s[8*st+j]);
      for(int d=0;d<4;++d){ bf16x8 vf; for(int j=0;j<8;++j){ int key=kb*32+16*st+8*(j>>2)+4*hh+(j&3); vf[j]=v[(size_t)key*4096+h*128+32*d+r]; }
        o[d]=__builtin_amdgcn_mfma_f32_32x32x16_bf16(vf,p,o[d],0,0,0); } }
  }
  float il=1.f/l;
  for(int d=0;d<4;++d) for(int i=0;i<16;++i){ int dd=32*d+(i&3)+8*(i>>2)+4*hh; ctx[(size_t)(qb*32+r)*4096+h*128+dd]=f2bf(o[d][i]*il); }
}
extern "C" void kernel_launch(void* const* d_in, const int* in_sizes, int n_in, void* d_out, int out_size, void* d_ws, size_t ws_size, hipStream_t stream){
  const u16* x=(const u16*)d_in[0]; const u16* ckv=(const u16*)d_in[1]; const u16* wq=(const u16*)d_in[2]; const u16* wuk=(const u16*)d_in[3];
  const u16* wuv=(const u16*)d_in[4]; const u16* wo=(const u16*)d_in[5]; const u16* w1=(const u16*)d_in[7]; const u16* b1=(const u16*)d_in[8];
  const u16* w2=(const u16*)d_in[9]; const u16* c=(const u16*)d_in[10]; const u16* wmod=(const u16*)d_in[11];
  char* ws=(char*)d_ws; float* mod=(float*)ws; u16* h=(u16*)(ws+(1<<20)); u16* x1=(u16*)(ws+(4<<20)); u16* h2=(u16*)(ws+(8<<20));
  u16* q=(u16*)(ws+(16<<20)); u16* k=(u16*)(ws+(32<<20)); u16* v=(u16*)(ws+(48<<20)); u16* ctx=(u16*)(ws+(64<<20)); u16* act=(u16*)(ws+(80<<20));
  k_mod<<<48,64,0,stream>>>(c,wmod,mod);
  k_ln<<<2048,64,0,stream>>>(x,mod,mod+512,h);
  k_gemm<0><<<dim3(32,64),256,0,stream>>>(h,wq,q,512,4096,nullptr,nullptr,nullptr);
  k_gemm<0><<<dim3(32,64),256,0,stream>>>(ckv,wuk,k,512,4096,nullptr,nullptr,nullptr);
  k_gemm<0><<<dim3(32,64),256,0,stream>>>(ckv,wuv,v,512,4096,nullptr,nullptr,nullptr);
  k_attn<<<dim3(32,64),64,0,stream>>>(q,k,v,ctx);
  k_gemm<1><<<dim3(4,64),256,0,stream>>>(ctx,wo,x1,4096,512,x,mod+1024,nullptr);
  k_ln<<<2048,64,0,stream>>>(x1,mod+1536,mod+2048,h2);
  k_gemm<2><<<dim3(14,64),256,0,stream>>>(h2,w1,act,512,1792,nullptr,nullptr,b1);
  k_gemm<1><<<dim3(4,64),256,0,stream>>>(act,w2,(u16*)d_out,1792,512,x1,mod+2560,nullptr);
}
```

```cpp
#include <hip/hip_runtime.h>

__global__ __launch_bounds__(256,1) void k_mega(const void* wuk, const void* wuv, const void* wq, void* out, const void* wo, const void* w1,
    const void* w2, const void* x, const void* b1, void* ws, const void* c, const void* wmod, const void* ckv){
  unsigned w = __builtin_amdgcn_readfirstlane(threadIdx.x>>6);
  asm volatile(
    "s_mov_b32 s13, 0\n"
    "s_load_dwordx4 s[8:11], %0, 64\n"
    "s_load_dwordx4 s[92:95], %0, 80\n"
    "s_load_dwordx8 s[48:55], %0, 0\n"
    "s_load_dwordx2 s[36:37], %0, 96\n"
    "s_load_dwordx8 s[24:31], %0, 32\n"
    "s_mov_b32 s71, %2\n"
    "s_mov_b32 s18, %1\n"
    "s_and_b32 s73, %1, 7\n"
    "s_lshl_b32 s73, s73, 2\n"
    "s_bfe_u32 s68, %1, 0x20003\n"
    "s_add_u32 s73, s73, s68\n"
    "s_lshr_b32 s72, %1, 5\n"
    "s_lshl_b32 s56, s71, 18\n"
    "s_lshl_b32 s68, s73, 8\n"
    "s_add_u32 s56, s56, s68\n"
    "s_lshl_b32 s57, s71, 12\n"
    "s_lshl_b32 s68, s72, 2\n"
    "s_add_u32 s68, s68, s71\n"
    "s_lshl_b32 s58, s68, 14\n"
    "s_lshl_b32 s60, s68, 16\n"
    "s_lshl_b32 s61, s71, 14\n"
    "s_add_u32 s61, s61, 0x10000\n"
    "s_add_u32 s4, %1, s72\n"
    "s_add_u32 s69, s4, 0\n"
    "s_and_b32 s69, s69, 3\n"
    "s_lshl_b32 s70, s69, 20\n"
    "s_add_u32 s80, s70, s56\n"
    "s_lshl_b32 s70, s69, 8\n"
    "s_add_u32 s84, s70, s60\n"
    "s_lshl_b32 s70, s69, 19\n"
    "s_add_u32 s88, s70, 0x100000\n"
    "v_mbcnt_lo_u32_b32 v127, -1, 0\n"
    "v_mbcnt_hi_u32_b32 v127, -1, v127\n"
    "v_lshlrev_b32 v190, 4, v127\n"
    "v_lshrrev_b32 v0, 3, v127\n"
    "v_bfe_u32 v1, v127, 4, 1\n"
    "v_bfe_u32 v2, v127, 1, 2\n"
    "v_xor_b32 v1, v1, v2\n"
    "v_lshlrev_b32 v1, 5, v1\n"
    "v_lshl_or_b32 v1, v0, 13, v1\n"
    "v_and_b32 v2, 1, v127\n"
    "v_lshl_or_b32 v184, v2, 4, v1\n"
    "v_xor_b32 v185, 64, v184\n"
    "v_lshrrev_b32 v1, 4, v127\n"
    "v_and_b32 v2, 7, v127\n"
    "v_xor_b32 v2, v2, v1\n"
    "v_lshlrev_b32 v2, 4, v2\n"
    "v_lshl_or_b32 v186, v0, 10, v2\n"
    "v_xor_b32 v187, 64, v186\n"
    "s_mov_b32 s78, 0x5a00000\n"
    "s_mov_b32 s79, 0x20000\n"
    "s_mov_b32 s38, 0x200000\n"
    "s_mov_b32 s39, 0x20000\n"
    "s_mov_b32 s42, 0x400000\n"
    "s_mov_b32 s43, 0x20000\n"
    "s_mov_b32 s46, 0x100000\n"
    "s_mov_b32 s47, 0x20000\n"
    "s_waitcnt lgkmcnt(0)\n"
    "s_mov_b32 s76, s10\n"
    "s_and_b32 s77, s11, 0xffff\n"
    "s_and_b32 s37, s37, 0xffff\n"
    "s_and_b32 s49, s49, 0xffff\n"
    "s_mov_b32 s40, s48\n"
    "s_mov_b32 s41, s49\n"
    "s_and_b32 s74, s73, 3\n"
    "s_lshr_b32 s75, s74, 1\n"
    "s_sub_u32 s75, s74, s75\n"
    "s_mul_i32 s33, s72, 3\n"
    "s_add_u32 s75, s75, s33\n"
    "s_add_u32 s33, s72, 24\n"
    "s_cmp_eq_u32 s74, 1\n"
    "s_cselect_b32 s33, s33, s75\n"
    "s_lshr_b32 s74, s73, 2\n"
    "s_lshl3_add_u32 s33, s33, s74\n"
    "s_andn2_b32 s59, s33, 63\n"
    "s_add_u32 s59, s59, s33\n"
    "s_lshl2_add_u32 s59, s59, s71\n"
    "s_lshl_b32 s59, s59, 2\n"
    "buffer_load_dword v240, off, s[76:79], s59 sc1\n"
    "s_and_b32 s74, s33, 3\n"
    "s_lshl_b32 s20, s74, 8\n"
    "v_lshrrev_b32 v230, 5, v190\n"
    "v_lshl_add_u32 v231, v230, 3, s20\n"
    "global_load_dwordx2 v[208:209], v231, s[92:93]\n"
    "s_getpc_b64 s[16:17]\n"
    "L_pc0:\n"
    "s_lshr_b32 s69, s18, 3\n"
    "s_lshl2_add_u32 s69, s69, s71\n"
    "s_lshl_b32 s69, s69, 10\n"
    "v_and_b32 v253, 0x70, v190\n"
    "v_lshl_add_u32 v253, v253, 3, s69\n"
    "v_min_u32 v253, L_end-L_pc0-4, v253\n"
    "global_load_dword v253, v253, s[16:17]\n"
    "s_and_b32 s74, s33, 3\n"
    "v_mul_u32_u24 v230, 24576, v230\n"
    "v_and_b32 v231, 16, v190\n"
    "s_mul_i32 s74, s74, 786432\n"
    "s_andn2_b32 s75, s33, 3\n"
    "s_add_u32 s75, s75, s71\n"
    "s_lshl_b32 s75, s75, 5\n"
    "s_add_u32 s74, s74, s75\n"
    "v_add3_u32 v230, v230, v231, s74\n"
    "s_cmp_ge_u32 s33, 192\n"
    "s_cbranch_scc1 L_s1_a\n"
    "global_load_dwordx4 v[192:195], v230, s[94:95]\n"
    "v_add_u32 v230, 6144, v230\n"
    "global_load_dwordx4 v[196:199], v230, s[94:95]\n"
    "v_add_u32 v230, 6144, v230\n"
    "global_load_dwordx4 v[200:203], v230, s[94:95]\n"
    "v_add_u32 v230, 6144, v230\n"
    "global_load_dwordx4 v[204:207], v230, s[94:95]\n"
    "L_s1_a:\n"
    "s_and_b32 s51, s51, 0xffff\n"
    "s_and_b32 s53, s53, 0xffff\n"
    "s_mov_b64 s[34:35], s[30:31]\n"
    "s_lshl_b32 s19, s71, 13\n"
    "s_add_u32 s19, s19, 0x20000\n"
    "s_add_u32 s69, s4, 1\n"
    "s_and_b32 s69, s69, 3\n"
    "s_lshl_b32 s70, s69, 20\n"
    "s_add_u32 s81, s70, s56\n"
    "s_lshl_b32 s70, s69, 8\n"
    "s_add_u32 s85, s70, s60\n"
    "s_lshl_b32 s70, s69, 19\n"
    "s_add_u32 s89, s70, 0x100000\n"
    "s_add_u32 s69, s4, 2\n"
    "s_and_b32 s69, s69, 3\n"
    "s_lshl_b32 s70, s69, 20\n"
    "s_add_u32 s82, s70, s56\n"
    "s_lshl_b32 s70, s69, 8\n"
    "s_add_u32 s86, s70, s60\n"
    "s_lshl_b32 s70, s69, 19\n"
    "s_add_u32 s90, s70, 0x100000\n"
    "s_add_u32 s69, s4, 3\n"
    "s_and_b32 s69, s69, 3\n"
    "s_lshl_b32 s70, s69, 20\n"
    "s_add_u32 s83, s70, s56\n"
    "s_lshl_b32 s70, s69, 8\n"
    "s_add_u32 s87, s70, s60\n"
    "s_lshl_b32 s70, s69, 19\n"
    "s_add_u32 s91, s70, 0x100000\n"
    "s_lshl_b32 s4, s72, 3\n"
    "s_add_u32 s69, s4, s71\n"
    "s_sub_u32 s4, 120, s4\n"
    "s_add_u32 s70, s4, s71\n"
    "s_lshl_b32 s64, s69, 12\n"
    "s_add_u32 s65, s64, 0x4000\n"
    "s_lshl_b32 s66, s70, 12\n"
    "s_add_u32 s67, s66, 0x4000\n"
    "s_lshl_b32 s70, s73, 14\n"
    "s_add_u32 s70, s70, s57\n"
    "s_add_u32 s70, s70, 0x2400000\n"
    "s_lshl_b32 s69, s72, 20\n"
    "s_add_u32 s62, s70, s69\n"
    "s_sub_u32 s69, 0xf00000, s69\n"
    "s_add_u32 s63, s70, s69\n"
    "s_lshr_b32 s15, %1, 3\n"
    "s_lshl_b32 s15, s15, 2\n"
    "s_add_u32 s15, s15, s71\n"
    "s_and_b32 s69, %1, 7\n"
    "s_lshl_b32 s30, s15, 3\n"
    "s_add_u32 s30, s30, s69\n"
    "s_lshl_b32 s69, s73, 20\n"
    "s_add_u32 s69, s69, 0x400000\n"
    "s_add_u32 s44, s10, s69\n"
    "s_addc_u32 s45, s11, 0\n"
    "s_and_b32 s45, s45, 0xffff\n"
    "s_lshl_b32 s12, s73, 7\n"
    "s_lshl_b32 s12, s12, 5\n"
    "s_add_u32 s12, s12, 0x5800000\n"
    "v_bfe_u32 v2, v127, 1, 3\n"
    "v_xor_b32 v2, v2, v1\n"
    "v_lshlrev_b32 v2, 4, v2\n"
    "v_and_b32 v3, 15, v127\n"
    "v_lshl_or_b32 v2, v3, 7, v2\n"
    "v_xor_b32 v3, 64, v2\n"
    "v_add_u32 v188, s61, v2\n"
    "v_add_u32 v189, s61, v3\n"
    "v_add_u32 v254, s19, v2\n"
    "v_add_u32 v255, s19, v3\n"
    "v_bfe_u32 v2, v127, 2, 2\n"
    "v_lshl_or_b32 v3, v1, 3, v2\n"
    "v_lshlrev_b32 v3, 7, v3\n"
    "v_and_b32 v4, 3, v127\n"
    "v_lshl_or_b32 v3, v4, 3, v3\n"
    "v_bfe_u32 v4, v127, 3, 2\n"
    "v_lshl_or_b32 v176, v4, 5, v3\n"
    "v_xor_b32 v177, 32, v176\n"
    "v_xor_b32 v178, 64, v176\n"
    "v_xor_b32 v179, 96, v176\n"
    "v_add_u32 v180, 0x10000, v176\n"
    "v_add_u32 v181, 0x10000, v177\n"
    "v_add_u32 v182, 0x10000, v178\n"
    "v_add_u32 v183, 0x10000, v179\n"
    "v_min_u32 v248, 1, v190\n"
    "v_lshlrev_b32 v248, 30, v248\n"
    "v_mov_b32 v249, 1\n"
    "v_mov_b32 v242, 0\n"
    "v_mov_b32 v243, 0\n"
    "v_mov_b32 v244, 0\n"
    "v_mov_b32 v245, 0\n"
    "s_lshl_b32 s21, s33, 2\n"
    "s_add_u32 s21, s21, s71\n"
    "s_lshl_b32 s21, s21, 2\n"
    "s_lshl_b32 s22, s71, 11\n"
    "v_lshlrev_b32 v230, 0, v190\n"
    "v_lshlrev_b32 v252, 3, v190\n"
    "s_lshl_b32 s7, s71, 10\n"
    "s_add_u32 s7, s7, 0x18000\n"
    "s_cmp_lg_u32 s33, 240\n"
    "s_cbranch_scc1 L_ld_a\n"
    "buffer_store_dwordx4 v[242:245], v190, s[76:79], s7 offen sc1\n"
    "L_ld_a:\n"
    "s_cmp_ge_u32 s33, 192\n"
    "s_cbranch_scc0 L_rq_a\n"
    "s_waitcnt vmcnt(0)\n"
    "L_rq_a:\n"
    "s_waitcnt vmcnt(6)\n"
    "v_add_u32 v240, 1, v240\n"
    "s_add_u32 s74, s59, 0x400\n"
    "buffer_store_dword v240, v248, s[76:79], s74 offen sc1\n"
    "s_lshl_b32 s74, s21, 9\n"
    "s_add_u32 s74, s74, 0x5800000\n"
    "s_lshl_b32 s75, s21, 5\n"
    "s_add_u32 s75, s75, 0x8000\n"
    "v_and_b32 v241, 0x70, v190\n"
    "s_cmp_lt_u32 s33, 64\n"
    "s_cbranch_scc0 L_zf\n"
    "buffer_store_dwordx4 v[242:245], v190, s[76:79], s74 offen offset:0 sc1\n"
    "buffer_store_dwordx4 v[242:245], v190, s[76:79], s74 offen offset:1024 sc1\n"
    "s_mov_b64 exec, 0xff\n"
    "buffer_store_dwordx4 v[242:245], v241, s[76:79], s75 offen sc1\n"
    "s_mov_b64 exec, -1\n"
    "L_zf:\n"
    "s_lshr_b32 s5, s72, 2\n"
    "s_xor_b32 s5, s5, 0\n"
    "s_lshl_b32 s68, s5, 14\n"
    "s_add_u32 m0, s68, s57\n"
    "s_lshl_b32 s5, s5, 7\n"
    "s_add_u32 s5, s5, s80\n"
    "s_add_u32 s68, s5, 0\n"
    "buffer_load_dwordx4 v184, s[40:43], s68 offen offset:0 lds\n"
    "s_add_u32 s68, s5, 64512\n"
    "buffer_load_dwordx4 v185, s[40:43], s68 offen offset:1024 lds\n"
    "s_add_u32 s68, s5, 129024\n"
    "buffer_load_dwordx4 v184, s[40:43], s68 offen offset:2048 lds\n"
    "s_add_u32 s68, s5, 193536\n"
    "buffer_load_dwordx4 v185, s[40:43], s68 offen offset:3072 lds\n"
    "s_add_u32 s5, s73, 0\n"
    "s_and_b32 s5, s5, 3\n"
    "s_lshl_b32 s68, s5, 12\n"
    "s_add_u32 m0, s68, s61\n"
    "s_bfe_u32 s68, s5, 0x10001\n"
    "s_and_b32 s5, s5, 1\n"
    "s_lshl_b32 s5, s5, 8\n"
    "s_add_u32 s5, s5, s68\n"
    "s_lshl_b32 s5, s5, 7\n"
    "s_add_u32 s5, s5, s84\n"
    "s_add_u32 s68, s5, 0\n"
    "buffer_load_dwordx4 v186, s[36:39], s68 offen offset:0 lds\n"
    "s_add_u32 s68, s5, 7168\n"
    "buffer_load_dwordx4 v187, s[36:39], s68 offen offset:1024 lds\n"
    "s_add_u32 s68, s5, 14336\n"
    "buffer_load_dwordx4 v186, s[36:39], s68 offen offset:2048 lds\n"
    "s_add_u32 s68, s5, 21504\n"
    "buffer_load_dwordx4 v187, s[36:39], s68 offen offset:3072 lds\n"
    "s_cmp_lg_u32 s33, 240\n"
    "s_cbranch_scc0 L_e8\n"
    "L_e8_r:\n"
    "s_mov_b64 exec, 0\n"
    "s_cmp_eq_u32 s71, 0\n"
    "s_cbranch_scc1 L_pad0\n"
    "s_cmp_eq_u32 s71, 1\n"
    "s_cbranch_scc1 L_pad1\n"
    "s_cmp_eq_u32 s71, 2\n"
    "s_cbranch_scc1 L_pad2\n"
    "s_branch L_pad3\n"
    "L_walked:\n"
    "s_mov_b64 exec, -1\n"
    "s_lshr_b32 s5, s72, 2\n"
    "s_xor_b32 s5, s5, 1\n"
    "s_lshl_b32 s68, s5, 14\n"
    "s_add_u32 m0, s68, s57\n"
    "s_lshl_b32 s5, s5, 7\n"
    "s_add_u32 s5, s5, s80\n"
    "s_add_u32 s68, s5, 0\n"
    "buffer_load_dwordx4 v184, s[40:43], s68 offen offset:0 lds\n"
    "s_add_u32 s68, s5, 64512\n"
    "buffer_load_dwordx4 v185, s[40:43], s68 offen offset:1024 lds\n"
    "s_add_u32 s68, s5, 129024\n"
    "buffer_load_dwordx4 v184, s[40:43], s68 offen offset:2048 lds\n"
    "s_add_u32 s68, s5, 193536\n"
    "buffer_load_dwordx4 v185, s[40:43], s68 offen offset:3072 lds\n"
    "s_waitcnt vmcnt(18)\n"
    "v_lshlrev_b32 v210, 16, v208\n"
    "v_and_b32 v211, 0xffff0000, v208\n"
    "v_lshlrev_b32 v212, 16, v209\n"
    "v_and_b32 v213, 0xffff0000, v209\n"
    "v_mul_f32 v222, 0xbfb8aa3b, v210\n"
    "v_mul_f32 v223, 0xbfb8aa3b, v211\n"
    "v_mul_f32 v224, 0xbfb8aa3b, v212\n"
    "v_mul_f32 v225, 0xbfb8aa3b, v213\n"
    "v_exp_f32 v222, v222\n"
    "v_exp_f32 v223, v223\n"
    "v_exp_f32 v224, v224\n"
    "v_exp_f32 v225, v225\n"
    "v_add_f32 v222, 1.0, v222\n"
    "v_add_f32 v223, 1.0, v223\n"
    "v_add_f32 v224, 1.0, v224\n"
    "v_add_f32 v225, 1.0, v225\n"
    "v_rcp_f32 v222, v222\n"
    "v_rcp_f32 v223, v223\n"
    "v_rcp_f32 v224, v224\n"
    "v_rcp_f32 v225, v225\n"
    "v_mul_f32 v210, v210, v222\n"
    "v_mul_f32 v211, v211, v223\n"
    "v_mul_f32 v212, v212, v224\n"
    "v_mul_f32 v213, v213, v225\n"
    "v_cvt_pk_bf16_f32 v208, v210, v211\n"
    "v_cvt_pk_bf16_f32 v209, v212, v213\n"
    "v_lshlrev_b32 v210, 16, v208\n"
    "v_and_b32 v211, 0xffff0000, v208\n"
    "v_lshlrev_b32 v212, 16, v209\n"
    "v_and_b32 v213, 0xffff0000, v209\n"
    "s_waitcnt vmcnt(13)\n"
    "v_lshlrev_b32 v222, 16, v192\n"
    "v_and_b32 v223, 0xffff0000, v192\n"
    "v_lshlrev_b32 v224, 16, v193\n"
    "v_and_b32 v225, 0xffff0000, v193\n"
    "v_lshlrev_b32 v226, 16, v194\n"
    "v_and_b32 v227, 0xffff0000, v194\n"
    "v_lshlrev_b32 v228, 16, v195\n"
    "v_and_b32 v229, 0xffff0000, v195\n"
    "v_pk_mul_f32 v[214:215], v[222:223], v[210:211] op_sel_hi:[1,0]\n"
    "v_pk_mul_f32 v[216:217], v[224:225], v[210:211] op_sel_hi:[1,0]\n"
    "v_pk_mul_f32 v[218:219], v[226:227], v[210:211] op_sel_hi:[1,0]\n"
    "v_pk_mul_f32 v[220:221], v[228:229], v[210:211] op_sel_hi:[1,0]\n"
    "v_lshlrev_b32 v222, 16, v196\n"
    "v_and_b32 v223, 0xffff0000, v196\n"
    "v_lshlrev_b32 v224, 16, v197\n"
    "v_and_b32 v225, 0xffff0000, v197\n"
    "v_lshlrev_b32 v226, 16, v198\n"
    "v_and_b32 v227, 0xffff0000, v198\n"
    "v_lshlrev_b32 v228, 16, v199\n"
    "v_and_b32 v229, 0xffff0000, v199\n"
    "v_pk_fma_f32 v[214:215], v[222:223], v[210:211], v[214:215] op_sel:[0,1,0]\n"
    "v_pk_fma_f32 v[216:217], v[224:225], v[210:211], v[216:217] op_sel:[0,1,0]\n"
    "v_pk_fma_f32 v[218:219], v[226:227], v[210:211], v[218:219] op_sel:[0,1,0]\n"
    "v_pk_fma_f32 v[220:221], v[228:229], v[210:211], v[220:221] op_sel:[0,1,0]\n"
    "v_lshlrev_b32 v222, 16, v200\n"
    "v_and_b32 v223, 0xffff0000, v200\n"
    "v_lshlrev_b32 v224, 16, v201\n"
    "v_and_b32 v225, 0xffff0000, v201\n"
    "v_lshlrev_b32 v226, 16, v202\n"
    "v_and_b32 v227, 0xffff0000, v202\n"
    "v_lshlrev_b32 v228, 16, v203\n"
    "v_and_b32 v229, 0xffff0000, v203\n"
    "v_pk_fma_f32 v[214:215], v[222:223], v[212:213], v[214:215] op_sel_hi:[1,0,1]\n"
    "v_pk_fma_f32 v[216:217], v[224:225], v[212:213], v[216:217] op_sel_hi:[1,0,1]\n"
    "v_pk_fma_f32 v[218:219], v[226:227], v[212:213], v[218:219] op_sel_hi:[1,0,1]\n"
    "v_pk_fma_f32 v[220:221], v[228:229], v[212:213], v[220:221] op_sel_hi:[1,0,1]\n"
    "v_lshlrev_b32 v222, 16, v204\n"
    "v_and_b32 v223, 0xffff0000, v204\n"
    "v_lshlrev_b32 v224, 16, v205\n"
    "v_and_b32 v225, 0xffff0000, v205\n"
    "v_lshlrev_b32 v226, 16, v206\n"
    "v_and_b32 v227, 0xffff0000, v206\n"
    "v_lshlrev_b32 v228, 16, v207\n"
    "v_and_b32 v229, 0xffff0000, v207\n"
    "v_pk_fma_f32 v[214:215], v[222:223], v[212:213], v[214:215] op_sel:[0,1,0]\n"
    "v_pk_fma_f32 v[216:217], v[224:225], v[212:213], v[216:217] op_sel:[0,1,0]\n"
    "v_pk_fma_f32 v[218:219], v[226:227], v[212:213], v[218:219] op_sel:[0,1,0]\n"
    "v_pk_fma_f32 v[220:221], v[228:229], v[212:213], v[220:221] op_sel:[0,1,0]\n"
    "v_and_b32 v193, 0xe0, v190\n"
    "v_min_u32 v193, 1, v193\n"
    "v_permlane32_swap_b32 v214, v218\n"
    "v_permlane32_swap_b32 v215, v219\n"
    "v_permlane32_swap_b32 v216, v220\n"
    "v_permlane32_swap_b32 v217, v221\n"
    "v_add_f32 v214, v214, v218\n"
    "v_add_f32 v215, v215, v219\n"
    "v_add_f32 v216, v216, v220\n"
    "v_add_f32 v217, v217, v221\n"
    "v_lshlrev_b32 v193, 30, v193\n"
    "v_lshl_or_b32 v193, v231, 1, v193\n"
    "v_permlane16_swap_b32 v214, v216\n"
    "v_permlane16_swap_b32 v215, v217\n"
    "v_add_f32 v214, v214, v216\n"
    "v_add_f32 v215, v215, v217\n"
    "v_lshrrev_b32 v194, 8, v190\n"
    "v_lshl_or_b32 v193, v194, 3, v193\n"
    "v_add_f32_dpp v214, v214, v214 row_ror:8 row_mask:0xf bank_mask:0xf\n"
    "v_add_f32_dpp v215, v215, v215 row_ror:8 row_mask:0xf bank_mask:0xf\n"
    "s_nop 0\n"
    "v_add_f32_dpp v214, v214, v214 row_ror:4 row_mask:0xf bank_mask:0xf\n"
    "v_add_f32_dpp v215, v215, v215 row_ror:4 row_mask:0xf bank_mask:0xf\n"
    "s_nop 0\n"
    "v_add_f32_dpp v214, v214, v214 row_ror:2 row_mask:0xf bank_mask:0xf\n"
    "v_add_f32_dpp v215, v215, v215 row_ror:2 row_mask:0xf bank_mask:0xf\n"
    "s_nop 0\n"
    "s_cmp_lg_u32 s33, 240\n"
    "s_cbranch_scc0 L_es0\n"
    "L_es0_r:\n"
    "s_add_u32 s5, s73, 1\n"
    "s_and_b32 s5, s5, 3\n"
    "s_lshl_b32 s68, s5, 12\n"
    "s_add_u32 m0, s68, s61\n"
    "s_bfe_u32 s68, s5, 0x10001\n"
    "s_and_b32 s5, s5, 1\n"
    "s_lshl_b32 s5, s5, 8\n"
    "s_add_u32 s5, s5, s68\n"
    "s_lshl_b32 s5, s5, 7\n"
    "s_add_u32 s5, s5, s84\n"
    "s_add_u32 s68, s5, 0\n"
    "buffer_load_dwordx4 v186, s[36:39], s68 offen offset:0 lds\n"
    "s_add_u32 s68, s5, 7168\n"
    "buffer_load_dwordx4 v187, s[36:39], s68 offen offset:1024 lds\n"
    "s_add_u32 s68, s5, 14336\n"
    "buffer_load_dwordx4 v186, s[36:39], s68 offen offset:2048 lds\n"
    "s_add_u32 s68, s5, 21504\n"
    "buffer_load_dwordx4 v187, s[36:39], s68 offen offset:3072 lds\n"
    "s_add_u32 s5, s73, 2\n"
    "s_and_b32 s5, s5, 3\n"
    "s_lshl_b32 s68, s5, 12\n"
    "s_add_u32 m0, s68, s61\n"
    "s_bfe_u32 s68, s5, 0x10001\n"
    "s_and_b32 s5, s5, 1\n"
    "s_lshl_b32 s5, s5, 8\n"
    "s_add_u32 s5, s5, s68\n"
    "s_lshl_b32 s5, s5, 7\n"
    "s_add_u32 s5, s5, s84\n"
    "s_add_u32 s68, s5, 0\n"
    "buffer_load_dwordx4 v186, s[36:39], s68 offen offset:0 lds\n"
    "s_add_u32 s68, s5, 7168\n"
    "buffer_load_dwordx4 v187, s[36:39], s68 offen offset:1024 lds\n"
    "s_add_u32 s68, s5, 14336\n"
    "buffer_load_dwordx4 v186, s[36:39], s68 offen offset:2048 lds\n"
    "s_add_u32 s68, s5, 21504\n"
    "buffer_load_dwordx4 v187, s[36:39], s68 offen offset:3072 lds\n"
    "s_add_u32 s5, s73, 3\n"
    "s_and_b32 s5, s5, 3\n"
    "s_lshl_b32 s68, s5, 12\n"
    "s_add_u32 m0, s68, s61\n"
    "s_bfe_u32 s68, s5, 0x10001\n"
    "s_and_b32 s5, s5, 1\n"
    "s_lshl_b32 s5, s5, 8\n"
    "s_add_u32 s5, s5, s68\n"
    "s_lshl_b32 s5, s5, 7\n"
    "s_add_u32 s5, s5, s84\n"
    "s_add_u32 s68, s5, 0\n"
    "buffer_load_dwordx4 v186, s[36:39], s68 offen offset:0 lds\n"
    "s_add_u32 s68, s5, 7168\n"
    "buffer_load_dwordx4 v187, s[36:39], s68 offen offset:1024 lds\n"
    "s_add_u32 s68, s5, 14336\n"
    "buffer_load_dwordx4 v186, s[36:39], s68 offen offset:2048 lds\n"
    "s_add_u32 s68, s5, 21504\n"
    "buffer_load_dwordx4 v187, s[36:39], s68 offen offset:3072 lds\n"
    "s_waitcnt vmcnt(12)\n"
    "s_barrier\n"
    "s_cmp_lg_u32 s33, 240\n"
    "s_cbranch_scc0 L_el7\n"
    "L_el7_r:\n"
    "ds_read_b64_tr_b16 v[128:129], v176 offset:0\n"
    "ds_read_b64_tr_b16 v[130:131], v176 offset:512\n"
    "ds_read_b64_tr_b16 v[132:133], v176 offset:4096\n"
    "ds_read_b64_tr_b16 v[134:135], v176 offset:4608\n"
    "ds_read_b64_tr_b16 v[136:137], v176 offset:8192\n"
    "ds_read_b64_tr_b16 v[138:139], v176 offset:8704\n"
    "ds_read_b64_tr_b16 v[140:141], v176 offset:12288\n"
    "ds_read_b64_tr_b16 v[142:143], v176 offset:12800\n"
    "s_add_u32 m0, s57, 32768\n"
    "s_add_u32 s68, s81, 0\n"
    "buffer_load_dwordx4 v184, s[40:43], s68 offen offset:0 lds\n"
    "s_add_u32 s68, s81, 64512\n"
    "buffer_load_dwordx4 v185, s[40:43], s68 offen offset:1024 lds\n"
    "s_add_u32 s68, s81, 129024\n"
    "buffer_load_dwordx4 v184, s[40:43], s68 offen offset:2048 lds\n"
    "s_add_u32 s68, s81, 193536\n"
    "buffer_load_dwordx4 v185, s[40:43], s68 offen offset:3072 lds\n"
    "s_add_u32 m0, s57, 49152\n"
    "s_add_u32 s68, s81, 128\n"
    "buffer_load_dwordx4 v184, s[40:43], s68 offen offset:0 lds\n"
    "s_add_u32 s68, s81, 64640\n"
    "buffer_load_dwordx4 v185, s[40:43], s68 offen offset:1024 lds\n"
    "s_add_u32 s68, s81, 129152\n"
    "buffer_load_dwordx4 v184, s[40:43], s68 offen offset:2048 lds\n"
    "s_add_u32 s68, s81, 193664\n"
    "buffer_load_dwordx4 v185, s[40:43], s68 offen offset:3072 lds\n"
    "s_cmp_lg_u32 s33, 240\n"
    "s_cbranch_scc0 L_es7\n"
    "L_es7_r:\n"
    "s_and_b32 s74, s33, 3\n"
    "s_mul_i32 s74, s74, 12288\n"
    "s_andn2_b32 s75, s33, 3\n"
    "s_add_u32 s75, s75, s71\n"
    "s_lshl_b32 s75, s75, 6\n"
    "s_add_u32 s74, s74, s75\n"
    "s_add_u32 s74, s74, 0x60000\n"
    "s_add_u32 s75, s21, 0x18000\n"
    "s_add_u32 s20, s59, 0x0\n"
    "s_cmp_lt_u32 s33, 192\n"
    "s_cselect_b32 s74, s74, 0x40000000\n"
    "buffer_store_dwordx2 v[214:215], v193, s[76:79], s74 offen sc1\n"
    "s_waitcnt vmcnt(9)\n"
    "ds_read_b128 a[0:3], v188 offset:0\n"
    "ds_read_b128 a[16:19], v189 offset:0\n"
    "ds_read_b128 a[32:35], v188 offset:8192\n"
    "ds_read_b128 a[48:51], v189 offset:8192\n"
    "ds_read_b128 a[4:7], v188 offset:2048\n"
    "ds_read_b128 a[20:23], v189 offset:2048\n"
    "ds_read_b128 a[36:39], v188 offset:10240\n"
    "ds_read_b128 a[52:55], v189 offset:10240\n"
    "ds_read_b128 a[8:11], v188 offset:4096\n"
    "ds_read_b128 a[24:27], v189 offset:4096\n"
    "ds_read_b128 a[40:43], v188 offset:12288\n"
    "ds_read_b128 a[56:59], v189 offset:12288\n"
    "ds_read_b128 a[12:15], v188 offset:6144\n"
    "ds_read_b128 a[28:31], v189 offset:6144\n"
    "ds_read_b128 a[44:47], v188 offset:14336\n"
    "ds_read_b128 a[60:63], v189 offset:14336\n"
    "s_waitcnt lgkmcnt(12)\n"
    "v_mfma_f32_16x16x32_bf16 v[0:3], v[128:131], a[0:3], 0\n"
    "ds_read_b64_tr_b16 v[144:145], v177 offset:0\n"
    "ds_read_b64_tr_b16 v[146:147], v177 offset:512\n"
    "v_mfma_f32_16x16x32_bf16 v[0:3], v[132:135], a[16:19], v[0:3]\n"
    "ds_read_b64_tr_b16 v[160:161], v178 offset:0\n"
    "v_mfma_f32_16x16x32_bf16 v[0:3], v[136:139], a[32:35], v[0:3]\n"
    "ds_read_b64_tr_b16 v[148:149], v177 offset:4096\n"
    "ds_read_b64_tr_b16 v[150:151], v177 offset:4608\n"
    "v_mfma_f32_16x16x32_bf16 v[0:3], v[140:143], a[48:51], v[0:3]\n"
    "s_lshl_b32 s16, s15, 15\n"
    "ds_read_b64_tr_b16 v[162:163], v178 offset:512\n"
    "s_waitcnt lgkmcnt(14)\n"
    "v_mfma_f32_16x16x32_bf16 v[16:19], v[128:131], a[4:7], 0\n"
    "ds_read_b64_tr_b16 v[152:153], v177 offset:8192\n"
    "ds_read_b64_tr_b16 v[154:155], v177 offset:8704\n"
    "s_cmp_lg_u32 s33, 240\n"
    "s_cbranch_scc0 L_el1\n"
    "L_el1_r:\n"
    "v_mfma_f32_16x16x32_bf16 v[16:19], v[132:135], a[20:23], v[16:19]\n"
    "ds_read_b64_tr_b16 v[164:165], v178 offset:4096\n"
    "v_mfma_f32_16x16x32_bf16 v[16:19], v[136:139], a[36:39], v[16:19]\n"
    "ds_read_b64_tr_b16 v[156:157], v177 offset:12288\n"
    "ds_read_b64_tr_b16 v[158:159], v177 offset:12800\n"
    "v_mfma_f32_16x16x32_bf16 v[16:19], v[140:143], a[52:55], v[16:19]\n"
    "s_add_u32 m0, s19, 0\n"
    "s_lshr_b32 s17, s73, 2\n"
    "ds_read_b64_tr_b16 v[166:167], v178 offset:4608\n"
    "s_waitcnt lgkmcnt(15)\n"
    "v_mfma_f32_16x16x32_bf16 v[32:35], v[128:131], a[8:11], 0\n"
    "s_add_u32 s68, s85, 0\n"
    "buffer_load_dwordx4 v186, s[36:39], s68 offen offset:0 lds\n"
    "v_mfma_f32_16x16x32_bf16 v[32:35], v[132:135], a[24:27], v[32:35]\n"
    "ds_read_b64_tr_b16 v[168:169], v178 offset:8192\n"
    "v_mfma_f32_16x16x32_bf16 v[32:35], v[136:139], a[40:43], v[32:35]\n"
    "v_mfma_f32_16x16x32_bf16 v[32:35], v[140:143], a[56:59], v[32:35]\n"
    "s_lshl_b32 s17, s17, 10\n"
    "ds_read_b64_tr_b16 v[170:171], v178 offset:8704\n"
    "s_waitcnt lgkmcnt(14)\n"
    "v_mfma_f32_16x16x32_bf16 v[48:51], v[128:131], a[12:15], 0\n"
    "s_add_u32 s68, s85, 7168\n"
    "buffer_load_dwordx4 v187, s[36:39], s68 offen offset:1024 lds\n"
    "v_mfma_f32_16x16x32_bf16 v[48:51], v[132:135], a[28:31], v[48:51]\n"
    "ds_read_b64_tr_b16 v[172:173], v178 offset:12288\n"
    "v_mfma_f32_16x16x32_bf16 v[48:51], v[136:139], a[44:47], v[48:51]\n"
    "v_mfma_f32_16x16x32_bf16 v[48:51], v[140:143], a[60:63], v[48:51]\n"
    "s_add_u32 s16, s16, s17\n"
    "ds_read_b64_tr_b16 v[174:175], v178 offset:12800\n"
    "s_waitcnt lgkmcnt(5)\n"
    "v_mfma_f32_16x16x32_bf16 v[4:7], v[144:147], a[0:3], 0\n"
    "s_add_u32 s68, s85, 14336\n"
    "s_lshl_b32 s23, s33, 5\n"
    "buffer_load_dwordx4 v186, s[36:39], s68 offen offset:2048 lds\n"
    "v_mfma_f32_16x16x32_bf16 v[4:7], v[148:151], a[16:19], v[4:7]\n"
    "s_add_u32 s23, s23, s71\n"
    "ds_read_b64_tr_b16 v[128:129], v179 offset:0\n"
    "v_mfma_f32_16x16x32_bf16 v[4:7], v[152:155], a[32:35], v[4:7]\n"
    "s_lshl_b32 s23, s23, 8\n"
    "v_mfma_f32_16x16x32_bf16 v[4:7], v[156:159], a[48:51], v[4:7]\n"
    "ds_read_b64_tr_b16 v[130:131], v179 offset:512\n"
    "v_lshrrev_b32 v247, 7, v190\n"
    "v_mfma_f32_16x16x32_bf16 v[20:23], v[144:147], a[4:7], 0\n"
    "s_add_u32 s68, s85, 21504\n"
    "buffer_load_dwordx4 v187, s[36:39], s68 offen offset:3072 lds\n"
    "v_mfma_f32_16x16x32_bf16 v[20:23], v[148:151], a[20:23], v[20:23]\n"
    "ds_read_b64_tr_b16 v[132:133], v179 offset:4096\n"
    "v_and_b32 v246, 0x70, v190\n"
    "v_mfma_f32_16x16x32_bf16 v[20:23], v[152:155], a[36:39], v[20:23]\n"
    "buffer_load_dword v241, off, s[76:79], s20 sc1\n"
    "v_mfma_f32_16x16x32_bf16 v[20:23], v[156:159], a[52:55], v[20:23]\n"
    "s_add_u32 m0, s19, 4096\n"
    "ds_read_b64_tr_b16 v[134:135], v179 offset:4608\n"
    "v_lshlrev_b32 v229, 2, v247\n"
    "v_mfma_f32_16x16x32_bf16 v[36:39], v[144:147], a[8:11], 0\n"
    "s_add_u32 s68, s85, 32768\n"
    "buffer_load_dwordx4 v186, s[36:39], s68 offen offset:0 lds\n"
    "v_mfma_f32_16x16x32_bf16 v[36:39], v[148:151], a[24:27], v[36:39]\n"
    "ds_read_b64_tr_b16 v[136:137], v179 offset:8192\n"
    "v_lshl_or_b32 v247, v247, 10, v246\n"
    "v_mfma_f32_16x16x32_bf16 v[36:39], v[152:155], a[40:43], v[36:39]\n"
    "v_add_u32 v247, s23, v247\n"
    "v_and_b32 v214, 0x70, v190\n"
    "v_mfma_f32_16x16x32_bf16 v[36:39], v[156:159], a[56:59], v[36:39]\n"
    "ds_read_b64_tr_b16 v[138:139], v179 offset:8704\n"
    "v_lshl_or_b32 v246, v246, 1, v229\n"
    "v_mfma_f32_16x16x32_bf16 v[52:55], v[144:147], a[12:15], 0\n"
    "s_add_u32 s68, s85, 39936\n"
    "buffer_load_dwordx4 v187, s[36:39], s68 offen offset:1024 lds\n"
    "v_mfma_f32_16x16x32_bf16 v[52:55], v[148:151], a[28:31], v[52:55]\n"
    "ds_read_b64_tr_b16 v[140:141], v179 offset:12288\n"
    "v_lshrrev_b32 v214, 1, v214\n"
    "v_mfma_f32_16x16x32_bf16 v[52:55], v[152:155], a[44:47], v[52:55]\n"
    "v_lshrrev_b32 v193, 7, v190\n"
    "v_lshrrev_b32 v250, 8, v190\n"
    "v_mfma_f32_16x16x32_bf16 v[52:55], v[156:159], a[60:63], v[52:55]\n"
    "ds_read_b64_tr_b16 v[142:143], v179 offset:12800\n"
    "v_lshl_add_u32 v193, v193, 3, s19\n"
    "s_waitcnt lgkmcnt(8)\n"
    "v_mfma_f32_16x16x32_bf16 v[8:11], v[160:163], a[0:3], 0\n"
    "s_add_u32 s68, s85, 47104\n"
    "buffer_load_dwordx4 v186, s[36:39], s68 offen offset:2048 lds\n"
    "v_mfma_f32_16x16x32_bf16 v[8:11], v[164:167], a[16:19], v[8:11]\n"
    "ds_read_b64_tr_b16 v[144:145], v176 offset:16384\n"
    "v_add_u32 v214, 0x20000, v214\n"
    "v_mfma_f32_16x16x32_bf16 v[8:11], v[168:171], a[32:35], v[8:11]\n"
    "v_lshrrev_b32 v195, 9, v190\n"
    "v_lshlrev_b32 v250, 13, v250\n"
    "v_mfma_f32_16x16x32_bf16 v[8:11], v[172:175], a[48:51], v[8:11]\n"
    "ds_read_b64_tr_b16 v[146:147], v176 offset:16896\n"
    "v_mul_u32_u24 v215, 1536, v195\n"
    "v_mfma_f32_16x16x32_bf16 v[24:27], v[160:163], a[4:7], 0\n"
    "s_add_u32 s68, s85, 54272\n"
    "buffer_load_dwordx4 v187, s[36:39], s68 offen offset:3072 lds\n"
    "s_cmp_lg_u32 s33, 240\n"
    "s_cbranch_scc0 L_es1\n"
    "L_es1_r:\n"
    "v_mfma_f32_16x16x32_bf16 v[24:27], v[164:167], a[20:23], v[24:27]\n"
    "ds_read_b64_tr_b16 v[148:149], v176 offset:20480\n"
    "v_add_u32 v215, v215, v190\n"
    "v_mfma_f32_16x16x32_bf16 v[24:27], v[168:171], a[36:39], v[24:27]\n"
    "v_cvt_f32_u32 v195, v195\n"
    "v_and_b32 v251, 0xf0, v190\n"
    "v_mfma_f32_16x16x32_bf16 v[24:27], v[172:175], a[52:55], v[24:27]\n"
    "s_add_u32 m0, s61, 0\n"
    "ds_read_b64_tr_b16 v[150:151], v176 offset:20992\n"
    "v_add_u32 v194, s19, v190\n"
    "v_mfma_f32_16x16x32_bf16 v[40:43], v[160:163], a[8:11], 0\n"
    "s_add_u32 s68, s85, 128\n"
    "buffer_load_dwordx4 v186, s[36:39], s68 offen offset:0 lds\n"
    "s_cmp_lg_u32 s33, 240\n"
    "s_cbranch_scc0 L_el2\n"
    "L_el2_r:\n"
    "v_mfma_f32_16x16x32_bf16 v[40:43], v[164:167], a[24:27], v[40:43]\n"
    "ds_read_b64_tr_b16 v[152:153], v176 offset:24576\n"
    "v_lshl_or_b32 v250, v251, 2, v250\n"
    "v_mfma_f32_16x16x32_bf16 v[40:43], v[168:171], a[40:43], v[40:43]\n"
    "v_add_u32 v250, s16, v250\n"
    "v_mfma_f32_16x16x32_bf16 v[40:43], v[172:175], a[56:59], v[40:43]\n"
    "ds_read_b64_tr_b16 v[154:155], v176 offset:25088\n"
    "v_mfma_f32_16x16x32_bf16 v[56:59], v[160:163], a[12:15], 0\n"
    "s_add_u32 s68, s85, 7296\n"
    "buffer_load_dwordx4 v187, s[36:39], s68 offen offset:1024 lds\n"
    "v_mfma_f32_16x16x32_bf16 v[56:59], v[164:167], a[28:31], v[56:59]\n"
    "ds_read_b64_tr_b16 v[156:157], v176 offset:28672\n"
    "v_mfma_f32_16x16x32_bf16 v[56:59], v[168:171], a[44:47], v[56:59]\n"
    "v_mfma_f32_16x16x32_bf16 v[56:59], v[172:175], a[60:63], v[56:59]\n"
    "ds_read_b64_tr_b16 v[158:159], v176 offset:29184\n"
    "s_waitcnt lgkmcnt(8)\n"
    "v_mfma_f32_16x16x32_bf16 v[12:15], v[128:131], a[0:3], 0\n"
    "s_add_u32 s68, s85, 14464\n"
    "buffer_load_dwordx4 v186, s[36:39], s68 offen offset:2048 lds\n"
    "v_mfma_f32_16x16x32_bf16 v[12:15], v[132:135], a[16:19], v[12:15]\n"
    "ds_read_b64_tr_b16 v[160:161], v177 offset:16384\n"
    "v_mfma_f32_16x16x32_bf16 v[12:15], v[136:139], a[32:35], v[12:15]\n"
    "v_mfma_f32_16x16x32_bf16 v[12:15], v[140:143], a[48:51], v[12:15]\n"
    "ds_read_b64_tr_b16 v[162:163], v177 offset:16896\n"
    "v_mfma_f32_16x16x32_bf16 v[28:31], v[128:131], a[4:7], 0\n"
    "s_add_u32 s68, s85, 21632\n"
    "buffer_load_dwordx4 v187, s[36:39], s68 offen offset:3072 lds\n"
    "s_branch L_pad0\n"
    ".p2align 12\n"
    "L_pad0:\n"
    "s_cbranch_execz L_pad4\n"
    "v_mfma_f32_16x16x32_bf16 v[28:31], v[132:135], a[20:23], v[28:31]\n"
    "ds_read_b64_tr_b16 v[164:165], v177 offset:20480\n"
    "v_mfma_f32_16x16x32_bf16 v[28:31], v[136:139], a[36:39], v[28:31]\n"
    "v_mfma_f32_16x16x32_bf16 v[28:31], v[140:143], a[52:55], v[28:31]\n"
    "s_add_u32 m0, s61, 4096\n"
    "ds_read_b64_tr_b16 v[166:167], v177 offset:20992\n"
    "v_mfma_f32_16x16x32_bf16 v[44:47], v[128:131], a[8:11], 0\n"
    "s_add_u32 s68, s85, 32896\n"
    "buffer_load_dwordx4 v186, s[36:39], s68 offen offset:0 lds\n"
    "v_mfma_f32_16x16x32_bf16 v[44:47], v[132:135], a[24:27], v[44:47]\n"
    "ds_read_b64_tr_b16 v[168:169], v177 offset:24576\n"
    "v_mfma_f32_16x16x32_bf16 v[44:47], v[136:139], a[40:43], v[44:47]\n"
    "s_waitcnt vmcnt(9)\n"
    "v_cmp_ne_u32 vcc, v240, v241\n"
    "s_cmp_eq_u32 s33, 240\n"
    "s_cselect_b64 vcc, 0, vcc\n"
    "s_cbranch_vccz L_go_ok\n"
    "s_mov_b32 s14, 0x40000\n"
    "L_go_spin:\n"
    "buffer_load_dword v241, off, s[76:79], s20 sc1\n"
    "s_sub_u32 s14, s14, 1\n"
    "s_waitcnt vmcnt(0)\n"
    "s_cbranch_scc1 L_go_ok\n"
    "v_cmp_ne_u32 vcc, v240, v241\n"
    "s_cmp_eq_u32 s33, 240\n"
    "s_cselect_b64 vcc, 0, vcc\n"
    "s_cbranch_vccz L_go_ok\n"
    "s_sleep 3\n"
    "s_branch L_go_spin\n"
    "L_go_ok:\n"
    "buffer_store_dword v249, v248, s[76:79], s75 offen sc1\n"
    "v_mfma_f32_16x16x32_bf16 v[44:47], v[140:143], a[56:59], v[44:47]\n"
    "ds_read_b64_tr_b16 v[170:171], v177 offset:25088\n"
    "v_mfma_f32_16x16x32_bf16 v[60:63], v[128:131], a[12:15], 0\n"
    "s_add_u32 s68, s85, 40064\n"
    "buffer_load_dwordx4 v187, s[36:39], s68 offen offset:1024 lds\n"
    "v_mfma_f32_16x16x32_bf16 v[60:63], v[132:135], a[28:31], v[60:63]\n"
    "ds_read_b64_tr_b16 v[172:173], v177 offset:28672\n"
    "v_mfma_f32_16x16x32_bf16 v[60:63], v[136:139], a[44:47], v[60:63]\n"
    "v_mfma_f32_16x16x32_bf16 v[60:63], v[140:143], a[60:63], v[60:63]\n"
    "ds_read_b64_tr_b16 v[174:175], v177 offset:29184\n"
    "s_waitcnt lgkmcnt(8)\n"
    "v_mfma_f32_16x16x32_bf16 v[64:67], v[144:147], a[0:3], 0\n"
    "s_waitcnt vmcnt(7)\n"
    "s_add_u32 s68, s85, 47232\n"
    "buffer_load_dwordx4 v186, s[36:39], s68 offen offset:2048 lds\n"
    "v_mfma_f32_16x16x32_bf16 v[64:67], v[148:151], a[16:19], v[64:67]\n"
    "ds_read_b64_tr_b16 v[128:129], v178 offset:16384\n"
    "ds_read_b128 a[64:67], v254 offset:0\n"
    "v_mfma_f32_16x16x32_bf16 v[64:67], v[152:155], a[32:35], v[64:67]\n"
    "v_mfma_f32_16x16x32_bf16 v[64:67], v[156:159], a[48:51], v[64:67]\n"
    "ds_read_b64_tr_b16 v[130:131], v178 offset:16896\n"
    "ds_read_b128 a[68:71], v254 offset:2048\n"
    "v_mfma_f32_16x16x32_bf16 v[80:83], v[144:147], a[4:7], 0\n"
    "s_add_u32 s68, s85, 54400\n"
    "buffer_load_dwordx4 v187, s[36:39], s68 offen offset:3072 lds\n"
    "v_mfma_f32_16x16x32_bf16 v[80:83], v[148:151], a[20:23], v[80:83]\n"
    "ds_read_b64_tr_b16 v[132:133], v178 offset:20480\n"
    "ds_read_b128 a[72:75], v254 offset:4096\n"
    "v_mfma_f32_16x16x32_bf16 v[80:83], v[152:155], a[36:39], v[80:83]\n"
    "v_mfma_f32_16x16x32_bf16 v[80:83], v[156:159], a[52:55], v[80:83]\n"
    "s_add_u32 m0, s61, 8192\n"
    "ds_read_b64_tr_b16 v[134:135], v178 offset:20992\n"
    "ds_read_b128 a[76:79], v254 offset:6144\n"
    "v_mfma_f32_16x16x32_bf16 v[96:99], v[144:147], a[8:11], 0\n"
    "s_add_u32 s68, s86, 0\n"
    "buffer_load_dwordx4 v186, s[36:39], s68 offen offset:0 lds\n"
    "v_mfma_f32_16x16x32_bf16 v[96:99], v[148:151], a[24:27], v[96:99]\n"
    "ds_read_b64_tr_b16 v[136:137], v178 offset:24576\n"
    "ds_read_b128 a[80:83], v255 offset:0\n"
    "v_mfma_f32_16x16x32_bf16 v[96:99], v[152:155], a[40:43], v[96:99]\n"
    "v_mfma_f32_16x16x32_bf16 v[96:99], v[156:159], a[56:59], v[96:99]\n"
    "ds_read_b64_tr_b16 v[138:139], v178 offset:25088\n"
    "ds_read_b128 a[84:87], v255 offset:2048\n"
    "v_mfma_f32_16x16x32_bf16 v[112:115], v[144:147], a[12:15], 0\n"
    "s_add_u32 s68, s86, 7168\n"
    "buffer_load_dwordx4 v187, s[36:39], s68 offen offset:1024 lds\n"
    "s_cmp_lg_u32 s33, 240\n"
    "s_cbranch_scc0 L_es2\n"
    "L_es2_r:\n"
    "v_mfma_f32_16x16x32_bf16 v[112:115], v[148:151], a[28:31], v[112:115]\n"
    "ds_read_b64_tr_b16 v[140:141], v178 offset:28672\n"
    "ds_read_b128 a[88:91], v255 offset:4096\n"
    "v_mfma_f32_16x16x32_bf16 v[112:115], v[152:155], a[44:47], v[112:115]\n"
    "s_cmp_lg_u32 s33, 240\n"
    "s_cbranch_scc0 L_el3\n"
    "L_el3_r:\n"
    "v_mfma_f32_16x16x32_bf16 v[112:115], v[156:159], a[60:63], v[112:115]\n"
    "ds_read_b64_tr_b16 v[142:143], v178 offset:29184\n"
    "ds_read_b128 a[92:95], v255 offset:6144\n"
    "s_waitcnt lgkmcnt(15)\n"
    "v_mfma_f32_16x16x32_bf16 v[68:71], v[160:163], a[0:3], 0\n"
    "ds_read_b64_tr_b16 v[144:145], v179 offset:16384\n"
    "v_mfma_f32_16x16x32_bf16 v[68:71], v[164:167], a[16:19], v[68:71]\n"
    "ds_read_b64_tr_b16 v[146:147], v179 offset:16896\n"
    "v_mfma_f32_16x16x32_bf16 v[68:71], v[168:171], a[32:35], v[68:71]\n"
    "ds_read_b64_tr_b16 v[148:149], v179 offset:20480\n"
    "v_mfma_f32_16x16x32_bf16 v[68:71], v[172:175], a[48:51], v[68:71]\n"
    "ds_read_b64_tr_b16 v[150:151], v179 offset:20992\n"
    "v_mfma_f32_16x16x32_bf16 v[84:87], v[160:163], a[4:7], 0\n"
    "ds_read_b64_tr_b16 v[152:153], v179 offset:24576\n"
    "v_mfma_f32_16x16x32_bf16 v[84:87], v[164:167], a[20:23], v[84:87]\n"
    "ds_read_b64_tr_b16 v[154:155], v179 offset:25088\n"
    "v_mfma_f32_16x16x32_bf16 v[84:87], v[168:171], a[36:39], v[84:87]\n"
    "ds_read_b64_tr_b16 v[156:157], v179 offset:28672\n"
    "v_mfma_f32_16x16x32_bf16 v[84:87], v[172:175], a[52:55], v[84:87]\n"
    "ds_read_b64_tr_b16 v[158:159], v179 offset:29184\n"
    "v_mfma_f32_16x16x32_bf16 v[100:103], v[160:163], a[8:11], 0\n"
    "s_add_u32 s68, s86, 14336\n"
    "buffer_load_dwordx4 v186, s[36:39], s68 offen offset:2048 lds\n"
    "v_mfma_f32_16x16x32_bf16 v[100:103], v[164:167], a[24:27], v[100:103]\n"
    "v_mfma_f32_16x16x32_bf16 v[100:103], v[168:171], a[40:43], v[100:103]\n"
    "s_add_u32 s68, s86, 21504\n"
    "buffer_load_dwordx4 v187, s[36:39], s68 offen offset:3072 lds\n"
    "v_mfma_f32_16x16x32_bf16 v[100:103], v[172:175], a[56:59], v[100:103]\n"
    "s_add_u32 m0, s61, 12288\n"
    "v_mfma_f32_16x16x32_bf16 v[116:119], v[160:163], a[12:15], 0\n"
    "s_add_u32 s68, s86, 32768\n"
    "buffer_load_dwordx4 v186, s[36:39], s68 offen offset:0 lds\n"
    "v_mfma_f32_16x16x32_bf16 v[116:119], v[164:167], a[28:31], v[116:119]\n"
    "v_mfma_f32_16x16x32_bf16 v[116:119], v[168:171], a[44:47], v[116:119]\n"
    "s_add_u32 s68, s86, 39936\n"
    "buffer_load_dwordx4 v187, s[36:39], s68 offen offset:1024 lds\n"
    "v_mfma_f32_16x16x32_bf16 v[116:119], v[172:175], a[60:63], v[116:119]\n"
    "s_waitcnt lgkmcnt(9)\n"
    "s_waitcnt lgkmcnt(0)\n"
    "s_barrier\n"
    "v_mfma_f32_16x16x32_bf16 v[72:75], v[128:131], a[0:3], 0\n"
    "s_add_u32 s68, s86, 47104\n"
    "buffer_load_dwordx4 v186, s[36:39], s68 offen offset:2048 lds\n"
    "v_mfma_f32_16x16x32_bf16 v[72:75], v[132:135], a[16:19], v[72:75]\n"
    "ds_read_b64_tr_b16 v[160:161], v176 offset:32768\n"
    "v_mfma_f32_16x16x32_bf16 v[72:75], v[136:139], a[32:35], v[72:75]\n"
    "v_mfma_f32_16x16x32_bf16 v[72:75], v[140:143], a[48:51], v[72:75]\n"
    "ds_read_b64_tr_b16 v[162:163], v176 offset:33280\n"
    "v_mfma_f32_16x16x32_bf16 v[88:91], v[128:131], a[4:7], 0\n"
    "s_add_u32 s68, s86, 54272\n"
    "buffer_load_dwordx4 v187, s[36:39], s68 offen offset:3072 lds\n"
    "v_mfma_f32_16x16x32_bf16 v[88:91], v[132:135], a[20:23], v[88:91]\n"
    "ds_read_b64_tr_b16 v[164:165], v176 offset:36864\n"
    "v_mfma_f32_16x16x32_bf16 v[88:91], v[136:139], a[36:39], v[88:91]\n"
    "v_mfma_f32_16x16x32_bf16 v[88:91], v[140:143], a[52:55], v[88:91]\n"
    "ds_read_b64_tr_b16 v[166:167], v176 offset:37376\n"
    "v_mfma_f32_16x16x32_bf16 v[104:107], v[128:131], a[8:11], 0\n"
    "v_mfma_f32_16x16x32_bf16 v[104:107], v[132:135], a[24:27], v[104:107]\n"
    "ds_read_b64_tr_b16 v[168:169], v176 offset:40960\n"
    "v_mfma_f32_16x16x32_bf16 v[104:107], v[136:139], a[40:43], v[104:107]\n"
    "s_cmp_lg_u32 s33, 240\n"
    "s_cbranch_scc0 L_es3\n"
    "L_es3_r:\n"
    "v_mfma_f32_16x16x32_bf16 v[104:107], v[140:143], a[56:59], v[104:107]\n"
    "ds_read_b64_tr_b16 v[170:171], v176 offset:41472\n"
    "v_mfma_f32_16x16x32_bf16 v[120:123], v[128:131], a[12:15], 0\n"
    "s_waitcnt vmcnt(8)\n"
    "v_mfma_f32_16x16x32_bf16 v[120:123], v[132:135], a[28:31], v[120:123]\n"
    "s_add_u32 m0, s57, 0\n"
    "ds_read_b64_tr_b16 v[172:173], v176 offset:45056\n"
    "ds_read_b128 a[96:99], v188 offset:0\n"
    "v_mfma_f32_16x16x32_bf16 v[120:123], v[136:139], a[44:47], v[120:123]\n"
    "s_add_u32 s68, s82, 0\n"
    "buffer_load_dwordx4 v184, s[40:43], s68 offen offset:0 lds\n"
    "v_mfma_f32_16x16x32_bf16 v[120:123], v[140:143], a[60:63], v[120:123]\n"
    "ds_read_b64_tr_b16 v[174:175], v176 offset:45568\n"
    "ds_read_b128 a[100:103], v188 offset:2048\n"
    "v_mfma_f32_16x16x32_bf16 v[76:79], v[144:147], a[0:3], 0\n"
    "v_mfma_f32_16x16x32_bf16 v[76:79], v[148:151], a[16:19], v[76:79]\n"
    "ds_read_b64_tr_b16 v[128:129], v177 offset:32768\n"
    "ds_read_b128 a[104:107], v188 offset:4096\n"
    "v_mfma_f32_16x16x32_bf16 v[76:79], v[152:155], a[32:35], v[76:79]\n"
    "s_add_u32 s68, s82, 64512\n"
    "buffer_load_dwordx4 v185, s[40:43], s68 offen offset:1024 lds\n"
    "v_mfma_f32_16x16x32_bf16 v[76:79], v[156:159], a[48:51], v[76:79]\n"
    "ds_read_b64_tr_b16 v[130:131], v177 offset:33280\n"
    "ds_read_b128 a[108:111], v188 offset:6144\n"
    "v_mfma_f32_16x16x32_bf16 v[92:95], v[144:147], a[4:7], 0\n"
    "v_mfma_f32_16x16x32_bf16 v[92:95], v[148:151], a[20:23], v[92:95]\n"
    "ds_read_b64_tr_b16 v[132:133], v177 offset:36864\n"
    "ds_read_b128 a[112:115], v189 offset:0\n"
    "v_mfma_f32_16x16x32_bf16 v[92:95], v[152:155], a[36:39], v[92:95]\n"
    "s_add_u32 s68, s82, 129024\n"
    "buffer_load_dwordx4 v184, s[40:43], s68 offen offset:2048 lds\n"
    "v_mfma_f32_16x16x32_bf16 v[92:95], v[156:159], a[52:55], v[92:95]\n"
    "ds_read_b64_tr_b16 v[134:135], v177 offset:37376\n"
    "ds_read_b128 a[116:119], v189 offset:2048\n"
    "v_mfma_f32_16x16x32_bf16 v[108:111], v[144:147], a[8:11], 0\n"
    "v_mfma_f32_16x16x32_bf16 v[108:111], v[148:151], a[24:27], v[108:111]\n"
    "ds_read_b64_tr_b16 v[136:137], v177 offset:40960\n"
    "ds_read_b128 a[120:123], v189 offset:4096\n"
    "v_mfma_f32_16x16x32_bf16 v[108:111], v[152:155], a[40:43], v[108:111]\n"
    "s_add_u32 s68, s82, 193536\n"
    "buffer_load_dwordx4 v185, s[40:43], s68 offen offset:3072 lds\n"
    "v_mfma_f32_16x16x32_bf16 v[108:111], v[156:159], a[56:59], v[108:111]\n"
    "ds_read_b64_tr_b16 v[138:139], v177 offset:41472\n"
    "ds_read_b128 a[124:127], v189 offset:6144\n"
    "v_mfma_f32_16x16x32_bf16 v[124:127], v[144:147], a[12:15], 0\n"
    "v_mfma_f32_16x16x32_bf16 v[124:127], v[148:151], a[28:31], v[124:127]\n"
    "s_add_u32 m0, s57, 16384\n"
    "ds_read_b64_tr_b16 v[140:141], v177 offset:45056\n"
    "v_mfma_f32_16x16x32_bf16 v[124:127], v[152:155], a[44:47], v[124:127]\n"
    "s_add_u32 s68, s82, 128\n"
    "buffer_load_dwordx4 v184, s[40:43], s68 offen offset:0 lds\n"
    "v_mfma_f32_16x16x32_bf16 v[124:127], v[156:159], a[60:63], v[124:127]\n"
    "ds_read_b64_tr_b16 v[142:143], v177 offset:45568\n"
    "s_waitcnt lgkmcnt(2)\n"
    "v_mfma_f32_16x16x32_bf16 v[0:3], v[160:163], a[64:67], v[0:3]\n"
    "v_mfma_f32_16x16x32_bf16 v[0:3], v[164:167], a[80:83], v[0:3]\n"
    "ds_read_b64_tr_b16 v[144:145], v178 offset:32768\n"
    "v_mfma_f32_16x16x32_bf16 v[0:3], v[168:171], a[96:99], v[0:3]\n"
    "s_add_u32 s68, s82, 64640\n"
    "buffer_load_dwordx4 v185, s[40:43], s68 offen offset:1024 lds\n"
    "v_mfma_f32_16x16x32_bf16 v[0:3], v[172:175], a[112:115], v[0:3]\n"
    "ds_read_b64_tr_b16 v[146:147], v178 offset:33280\n"
    "v_mfma_f32_16x16x32_bf16 v[16:19], v[160:163], a[68:71], v[16:19]\n"
    "global_load_dwordx4 v[232:235], v247, s[34:35] offset:0\n"
    "v_mfma_f32_16x16x32_bf16 v[16:19], v[164:167], a[84:87], v[16:19]\n"
    "ds_read_b64_tr_b16 v[148:149], v178 offset:36864\n"
    "v_mfma_f32_16x16x32_bf16 v[16:19], v[168:171], a[100:103], v[16:19]\n"
    "s_add_u32 s68, s82, 129152\n"
    "buffer_load_dwordx4 v184, s[40:43], s68 offen offset:2048 lds\n"
    "v_mfma_f32_16x16x32_bf16 v[16:19], v[172:175], a[116:119], v[16:19]\n"
    "ds_read_b64_tr_b16 v[150:151], v178 offset:37376\n"
    "v_mfma_f32_16x16x32_bf16 v[32:35], v[160:163], a[72:75], v[32:35]\n"
    "global_load_dwordx4 v[236:239], v247, s[34:35] offset:128\n"
    "v_mfma_f32_16x16x32_bf16 v[32:35], v[164:167], a[88:91], v[32:35]\n"
    "ds_read_b64_tr_b16 v[152:153], v178 offset:40960\n"
    "v_mfma_f32_16x16x32_bf16 v[32:35], v[168:171], a[104:107], v[32:35]\n"
    "s_add_u32 s68, s82, 193664\n"
    "buffer_load_dwordx4 v185, s[40:43], s68 offen offset:3072 lds\n"
    "v_mfma_f32_16x16x32_bf16 v[32:35], v[172:175], a[120:123], v[32:35]\n"
    "ds_read_b64_tr_b16 v[154:155], v178 offset:41472\n"
    "v_mfma_f32_16x16x32_bf16 v[48:51], v[160:163], a[76:79], v[48:51]\n"
    "v_mfma_f32_16x16x32_bf16 v[48:51], v[164:167], a[92:95], v[48:51]\n"
    "s_add_u32 m0, s19, 0\n"
    "ds_read_b64_tr_b16 v[156:157], v178 offset:45056\n"
    "v_mfma_f32_16x16x32_bf16 v[48:51], v[168:171], a[108:111], v[48:51]\n"
    "s_add_u32 s68, s86, 128\n"
    "buffer_load_dwordx4 v186, s[36:39], s68 offen offset:0 lds\n"
    "v_mfma_f32_16x16x32_bf16 v[48:51], v[172:175], a[124:127], v[48:51]\n"
    "ds_read_b64_tr_b16 v[158:159], v178 offset:45568\n"
    "s_waitcnt lgkmcnt(8)\n"
    "v_mfma_f32_16x16x32_bf16 v[4:7], v[128:131], a[64:67], v[4:7]\n"
    "s_waitcnt vmcnt(11)\n"
    "v_mfma_f32_16x16x32_bf16 v[4:7], v[132:135], a[80:83], v[4:7]\n"
    "ds_read_b64_tr_b16 v[160:161], v179 offset:32768\n"
    "ds_read_b128 a[128:131], v188 offset:8192\n"
    "v_mfma_f32_16x16x32_bf16 v[4:7], v[136:139], a[96:99], v[4:7]\n"
    "s_add_u32 s68, s86, 7296\n"
    "buffer_load_dwordx4 v187, s[36:39], s68 offen offset:1024 lds\n"
    "v_mfma_f32_16x16x32_bf16 v[4:7], v[140:143], a[112:115], v[4:7]\n"
    "ds_read_b64_tr_b16 v[162:163], v179 offset:33280\n"
    "ds_read_b128 a[132:135], v188 offset:10240\n"
    "v_mfma_f32_16x16x32_bf16 v[20:23], v[128:131], a[68:71], v[20:23]\n"
    "v_mfma_f32_16x16x32_bf16 v[20:23], v[132:135], a[84:87], v[20:23]\n"
    "ds_read_b64_tr_b16 v[164:165], v179 offset:36864\n"
    "ds_read_b128 a[136:139], v188 offset:12288\n"
    "v_mfma_f32_16x16x32_bf16 v[20:23], v[136:139], a[100:103], v[20:23]\n"
    "s_add_u32 s68, s86, 14464\n"
    "buffer_load_dwordx4 v186, s[36:39], s68 offen offset:2048 lds\n"
    "v_mfma_f32_16x16x32_bf16 v[20:23], v[140:143], a[116:119], v[20:23]\n"
    "ds_read_b64_tr_b16 v[166:167], v179 offset:37376\n"
    "ds_read_b128 a[140:143], v188 offset:14336\n"
    "v_mfma_f32_16x16x32_bf16 v[36:39], v[128:131], a[72:75], v[36:39]\n"
    "v_mfma_f32_16x16x32_bf16 v[36:39], v[132:135], a[88:91], v[36:39]\n"
    "ds_read_b64_tr_b16 v[168:169], v179 offset:40960\n"
    "ds_read_b128 a[144:147], v189 offset:8192\n"
    "v_mfma_f32_16x16x32_bf16 v[36:39], v[136:139], a[104:107], v[36:39]\n"
    "s_add_u32 s68, s86, 21632\n"
    "buffer_load_dwordx4 v187, s[36:39], s68 offen offset:3072 lds\n"
    "v_mfma_f32_16x16x32_bf16 v[36:39], v[140:143], a[120:123], v[36:39]\n"
    "ds_read_b64_tr_b16 v[170:171], v179 offset:41472\n"
    "ds_read_b128 a[148:151], v189 offset:10240\n"
    "v_mfma_f32_16x16x32_bf16 v[52:55], v[128:131], a[76:79], v[52:55]\n"
    "v_mfma_f32_16x16x32_bf16 v[52:55], v[132:135], a[92:95], v[52:55]\n"
    "s_add_u32 m0, s19, 4096\n"
    "ds_read_b64_tr_b16 v[172:173], v179 offset:45056\n"
    "ds_read_b128 a[152:155], v189 offset:12288\n"
    "v_mfma_f32_16x16x32_bf16 v[52:55], v[136:139], a[108:111], v[52:55]\n"
    "s_add_u32 s68, s86, 32896\n"
    "buffer_load_dwordx4 v186, s[36:39], s68 offen offset:0 lds\n"
    "v_mfma_f32_16x16x32_bf16 v[52:55], v[140:143], a[124:127], v[52:55]\n"
    "ds_read_b64_tr_b16 v[174:175], v179 offset:45568\n"
    "ds_read_b128 a[156:159], v189 offset:14336\n"
    "s_waitcnt lgkmcnt(15)\n"
    "v_mfma_f32_16x16x32_bf16 v[8:11], v[144:147], a[64:67], v[8:11]\n"
    "v_mfma_f32_16x16x32_bf16 v[8:11], v[148:151], a[80:83], v[8:11]\n"
    "ds_read_b64_tr_b16 v[128:129], v176 offset:49152\n"
    "v_mfma_f32_16x16x32_bf16 v[8:11], v[152:155], a[96:99], v[8:11]\n"
    "s_add_u32 s68, s86, 40064\n"
    "buffer_load_dwordx4 v187, s[36:39], s68 offen offset:1024 lds\n"
    "v_mfma_f32_16x16x32_bf16 v[8:11], v[156:159], a[112:115], v[8:11]\n"
    "ds_read_b64_tr_b16 v[130:131], v176 offset:49664\n"
    "v_mfma_f32_16x16x32_bf16 v[24:27], v[144:147], a[68:71], v[24:27]\n"
    "v_mfma_f32_16x16x32_bf16 v[24:27], v[148:151], a[84:87], v[24:27]\n"
    "ds_read_b64_tr_b16 v[132:133], v176 offset:53248\n"
    "v_mfma_f32_16x16x32_bf16 v[24:27], v[152:155], a[100:103], v[24:27]\n"
    "s_add_u32 s68, s86, 47232\n"
    "buffer_load_dwordx4 v186, s[36:39], s68 offen offset:2048 lds\n"
    "v_mfma_f32_16x16x32_bf16 v[24:27], v[156:159], a[116:119], v[24:27]\n"
    "ds_read_b64_tr_b16 v[134:135], v176 offset:53760\n"
    "v_mfma_f32_16x16x32_bf16 v[40:43], v[144:147], a[72:75], v[40:43]\n"
    "v_mfma_f32_16x16x32_bf16 v[40:43], v[148:151], a[88:91], v[40:43]\n"
    "ds_read_b64_tr_b16 v[136:137], v176 offset:57344\n"
    "v_mfma_f32_16x16x32_bf16 v[40:43], v[152:155], a[104:107], v[40:43]\n"
    "s_add_u32 s68, s86, 54400\n"
    "buffer_load_dwordx4 v187, s[36:39], s68 offen offset:3072 lds\n"
    "v_mfma_f32_16x16x32_bf16 v[40:43], v[156:159], a[120:123], v[40:43]\n"
    "ds_read_b64_tr_b16 v[138:139], v176 offset:57856\n"
    "v_mfma_f32_16x16x32_bf16 v[56:59], v[144:147], a[76:79], v[56:59]\n"
    "v_mfma_f32_16x16x32_bf16 v[56:59], v[148:151], a[92:95], v[56:59]\n"
    "s_add_u32 m0, s61, 0\n"
    "ds_read_b64_tr_b16 v[140:141], v176 offset:61440\n"
    "v_mfma_f32_16x16x32_bf16 v[56:59], v[152:155], a[108:111], v[56:59]\n"
    "s_add_u32 s68, s87, 0\n"
    "buffer_load_dwordx4 v186, s[36:39], s68 offen offset:0 lds\n"
    "v_mfma_f32_16x16x32_bf16 v[56:59], v[156:159], a[124:127], v[56:59]\n"
    "ds_read_b64_tr_b16 v[142:143], v176 offset:61952\n"
    "s_waitcnt lgkmcnt(9)\n"
    "v_mfma_f32_16x16x32_bf16 v[12:15], v[160:163], a[64:67], v[12:15]\n"
    "v_mfma_f32_16x16x32_bf16 v[12:15], v[164:167], a[80:83], v[12:15]\n"
    "ds_read_b64_tr_b16 v[144:145], v177 offset:49152\n"
    "v_mfma_f32_16x16x32_bf16 v[12:15], v[168:171], a[96:99], v[12:15]\n"
    "s_add_u32 s68, s87, 7168\n"
    "buffer_load_dwordx4 v187, s[36:39], s68 offen offset:1024 lds\n"
    "v_mfma_f32_16x16x32_bf16 v[12:15], v[172:175], a[112:115], v[12:15]\n"
    "ds_read_b64_tr_b16 v[146:147], v177 offset:49664\n"
    "v_mfma_f32_16x16x32_bf16 v[28:31], v[160:163], a[68:71], v[28:31]\n"
    "v_mfma_f32_16x16x32_bf16 v[28:31], v[164:167], a[84:87], v[28:31]\n"
    "ds_read_b64_tr_b16 v[148:149], v177 offset:53248\n"
    "v_mfma_f32_16x16x32_bf16 v[28:31], v[168:171], a[100:103], v[28:31]\n"
    "s_add_u32 s68, s87, 14336\n"
    "buffer_load_dwordx4 v186, s[36:39], s68 offen offset:2048 lds\n"
    "v_mfma_f32_16x16x32_bf16 v[28:31], v[172:175], a[116:119], v[28:31]\n"
    "ds_read_b64_tr_b16 v[150:151], v177 offset:53760\n"
    "v_mfma_f32_16x16x32_bf16 v[44:47], v[160:163], a[72:75], v[44:47]\n"
    "v_mfma_f32_16x16x32_bf16 v[44:47], v[164:167], a[88:91], v[44:47]\n"
    "ds_read_b64_tr_b16 v[152:153], v177 offset:57344\n"
    "v_mfma_f32_16x16x32_bf16 v[44:47], v[168:171], a[104:107], v[44:47]\n"
    "s_add_u32 s68, s87, 21504\n"
    "buffer_load_dwordx4 v187, s[36:39], s68 offen offset:3072 lds\n"
    "v_mfma_f32_16x16x32_bf16 v[44:47], v[172:175], a[120:123], v[44:47]\n"
    "ds_read_b64_tr_b16 v[154:155], v177 offset:57856\n"
    "v_mfma_f32_16x16x32_bf16 v[60:63], v[160:163], a[76:79], v[60:63]\n"
    "v_mfma_f32_16x16x32_bf16 v[60:63], v[164:167], a[92:95], v[60:63]\n"
    "s_add_u32 m0, s61, 4096\n"
    "ds_read_b64_tr_b16 v[156:157], v177 offset:61440\n"
    "v_mfma_f32_16x16x32_bf16 v[60:63], v[168:171], a[108:111], v[60:63]\n"
    "s_add_u32 s68, s87, 32768\n"
    "buffer_load_dwordx4 v186, s[36:39], s68 offen offset:0 lds\n"
    "v_mfma_f32_16x16x32_bf16 v[60:63], v[172:175], a[124:127], v[60:63]\n"
    "ds_read_b64_tr_b16 v[158:159], v177 offset:61952\n"
    "s_waitcnt lgkmcnt(8)\n"
    "v_mfma_f32_16x16x32_bf16 v[64:67], v[128:131], a[64:67], v[64:67]\n"
    "v_mfma_f32_16x16x32_bf16 v[64:67], v[132:135], a[80:83], v[64:67]\n"
    "ds_read_b64_tr_b16 v[160:161], v178 offset:49152\n"
    "v_mfma_f32_16x16x32_bf16 v[64:67], v[136:139], a[96:99], v[64:67]\n"
    "s_add_u32 s68, s87, 39936\n"
    "buffer_load_dwordx4 v187, s[36:39], s68 offen offset:1024 lds\n"
    "v_mfma_f32_16x16x32_bf16 v[64:67], v[140:143], a[112:115], v[64:67]\n"
    "ds_read_b64_tr_b16 v[162:163], v178 offset:49664\n"
    "v_mfma_f32_16x16x32_bf16 v[80:83], v[128:131], a[68:71], v[80:83]\n"
    "v_mfma_f32_16x16x32_bf16 v[80:83], v[132:135], a[84:87], v[80:83]\n"
    "ds_read_b64_tr_b16 v[164:165], v178 offset:53248\n"
    "v_mfma_f32_16x16x32_bf16 v[80:83], v[136:139], a[100:103], v[80:83]\n"
    "s_add_u32 s68, s87, 47104\n"
    "buffer_load_dwordx4 v186, s[36:39], s68 offen offset:2048 lds\n"
    "v_mfma_f32_16x16x32_bf16 v[80:83], v[140:143], a[116:119], v[80:83]\n"
    "ds_read_b64_tr_b16 v[166:167], v178 offset:53760\n"
    "v_mfma_f32_16x16x32_bf16 v[96:99], v[128:131], a[72:75], v[96:99]\n"
    "v_mfma_f32_16x16x32_bf16 v[96:99], v[132:135], a[88:91], v[96:99]\n"
    "ds_read_b64_tr_b16 v[168:169], v178 offset:57344\n"
    "v_mfma_f32_16x16x32_bf16 v[96:99], v[136:139], a[104:107], v[96:99]\n"
    "s_add_u32 s68, s87, 54272\n"
    "buffer_load_dwordx4 v187, s[36:39], s68 offen offset:3072 lds\n"
    "v_mfma_f32_16x16x32_bf16 v[96:99], v[140:143], a[120:123], v[96:99]\n"
    "ds_read_b64_tr_b16 v[170:171], v178 offset:57856\n"
    "v_mfma_f32_16x16x32_bf16 v[112:115], v[128:131], a[76:79], v[112:115]\n"
    "v_mfma_f32_16x16x32_bf16 v[112:115], v[132:135], a[92:95], v[112:115]\n"
    "s_add_u32 m0, s61, 8192\n"
    "ds_read_b64_tr_b16 v[172:173], v178 offset:61440\n"
    "v_mfma_f32_16x16x32_bf16 v[112:115], v[136:139], a[108:111], v[112:115]\n"
    "s_add_u32 s68, s87, 128\n"
    "buffer_load_dwordx4 v186, s[36:39], s68 offen offset:0 lds\n"
    "v_mfma_f32_16x16x32_bf16 v[112:115], v[140:143], a[124:127], v[112:115]\n"
    "ds_read_b64_tr_b16 v[174:175], v178 offset:61952\n"
    "s_waitcnt vmcnt(18)\n"
    "v_lshlrev_b32 v196, 16, v232\n"
    "s_waitcnt lgkmcnt(8)\n"
    "v_mfma_f32_16x16x32_bf16 v[68:71], v[144:147], a[64:67], v[68:71]\n"
    "ds_read_b64_tr_b16 v[128:129], v179 offset:49152\n"
    "v_and_b32 v197, 0xffff0000, v232\n"
    "v_mfma_f32_16x16x32_bf16 v[68:71], v[148:151], a[80:83], v[68:71]\n"
    "ds_read_b64_tr_b16 v[130:131], v179 offset:49664\n"
    "v_lshlrev_b32 v198, 16, v233\n"
    "v_mfma_f32_16x16x32_bf16 v[68:71], v[152:155], a[96:99], v[68:71]\n"
    "ds_read_b64_tr_b16 v[132:133], v179 offset:53248\n"
    "v_and_b32 v199, 0xffff0000, v233\n"
    "v_mfma_f32_16x16x32_bf16 v[68:71], v[156:159], a[112:115], v[68:71]\n"
    "ds_read_b64_tr_b16 v[134:135], v179 offset:53760\n"
    "v_lshlrev_b32 v200, 16, v234\n"
    "v_mfma_f32_16x16x32_bf16 v[84:87], v[144:147], a[68:71], v[84:87]\n"
    "ds_read_b64_tr_b16 v[136:137], v179 offset:57344\n"
    "v_and_b32 v201, 0xffff0000, v234\n"
    "v_mfma_f32_16x16x32_bf16 v[84:87], v[148:151], a[84:87], v[84:87]\n"
    "ds_read_b64_tr_b16 v[138:139], v179 offset:57856\n"
    "v_lshlrev_b32 v202, 16, v235\n"
    "v_mfma_f32_16x16x32_bf16 v[84:87], v[152:155], a[100:103], v[84:87]\n"
    "ds_read_b64_tr_b16 v[140:141], v179 offset:61440\n"
    "v_and_b32 v203, 0xffff0000, v235\n"
    "v_mfma_f32_16x16x32_bf16 v[84:87], v[156:159], a[116:119], v[84:87]\n"
    "ds_read_b64_tr_b16 v[142:143], v179 offset:61952\n"
    "v_lshlrev_b32 v204, 16, v236\n"
    "v_mfma_f32_16x16x32_bf16 v[100:103], v[144:147], a[72:75], v[100:103]\n"
    "s_add_u32 s68, s87, 7296\n"
    "buffer_load_dwordx4 v187, s[36:39], s68 offen offset:1024 lds\n"
    "v_mfma_f32_16x16x32_bf16 v[100:103], v[148:151], a[88:91], v[100:103]\n"
    "v_and_b32 v205, 0xffff0000, v236\n"
    "v_lshlrev_b32 v206, 16, v237\n"
    "v_mfma_f32_16x16x32_bf16 v[100:103], v[152:155], a[104:107], v[100:103]\n"
    "s_add_u32 s68, s87, 14464\n"
    "buffer_load_dwordx4 v186, s[36:39], s68 offen offset:2048 lds\n"
    "v_mfma_f32_16x16x32_bf16 v[100:103], v[156:159], a[120:123], v[100:103]\n"
    "v_and_b32 v207, 0xffff0000, v237\n"
    "v_lshlrev_b32 v208, 16, v238\n"
    "v_mfma_f32_16x16x32_bf16 v[116:119], v[144:147], a[76:79], v[116:119]\n"
    "s_add_u32 s68, s87, 21632\n"
    "buffer_load_dwordx4 v187, s[36:39], s68 offen offset:3072 lds\n"
    "v_mfma_f32_16x16x32_bf16 v[116:119], v[148:151], a[92:95], v[116:119]\n"
    "s_add_u32 m0, s61, 12288\n"
    "v_and_b32 v209, 0xffff0000, v238\n"
    "v_lshlrev_b32 v210, 16, v239\n"
    "v_mfma_f32_16x16x32_bf16 v[116:119], v[152:155], a[108:111], v[116:119]\n"
    "s_add_u32 s68, s87, 32896\n"
    "buffer_load_dwordx4 v186, s[36:39], s68 offen offset:0 lds\n"
    "v_mfma_f32_16x16x32_bf16 v[116:119], v[156:159], a[124:127], v[116:119]\n"
    "v_and_b32 v211, 0xffff0000, v239\n"
    "v_add_f32 v212, v196, v197\n"
    "s_waitcnt lgkmcnt(8)\n"
    "s_waitcnt vmcnt(21) lgkmcnt(0)\n"
    "s_barrier\n"
    "v_mfma_f32_16x16x32_bf16 v[72:75], v[160:163], a[64:67], v[72:75]\n"
    "v_add_f32 v212, v212, v198\n"
    "v_add_f32 v212, v212, v199\n"
    "v_mfma_f32_16x16x32_bf16 v[72:75], v[164:167], a[80:83], v[72:75]\n"
    "ds_read_b64_tr_b16 v[144:145], v176 offset:0\n"
    "v_add_f32 v212, v212, v200\n"
    "v_mfma_f32_16x16x32_bf16 v[72:75], v[168:171], a[96:99], v[72:75]\n"
    "s_waitcnt vmcnt(13)\n"
    "s_add_u32 s68, s87, 40064\n"
    "buffer_load_dwordx4 v187, s[36:39], s68 offen offset:1024 lds\n"
    "s_cmp_lg_u32 s33, 240\n"
    "s_cbranch_scc0 L_el9\n"
    "L_el9_r:\n"
    "v_mfma_f32_16x16x32_bf16 v[72:75], v[172:175], a[112:115], v[72:75]\n"
    "ds_read_b64_tr_b16 v[146:147], v176 offset:512\n"
    "ds_read_b128 a[160:163], v254 offset:0\n"
    "v_mfma_f32_16x16x32_bf16 v[88:91], v[160:163], a[68:71], v[88:91]\n"
    "s_mov_b32 s74, 0x18000\n"
    "v_add_f32 v212, v212, v201\n"
    "v_add_f32 v212, v212, v202\n"
    "v_mfma_f32_16x16x32_bf16 v[88:91], v[164:167], a[84:87], v[88:91]\n"
    "ds_read_b64_tr_b16 v[148:149], v176 offset:4096\n"
    "ds_read_b128 a[164:167], v254 offset:2048\n"
    "v_mfma_f32_16x16x32_bf16 v[88:91], v[168:171], a[100:103], v[88:91]\n"
    "s_add_u32 s68, s87, 47232\n"
    "buffer_load_dwordx4 v186, s[36:39], s68 offen offset:2048 lds\n"
    "v_mfma_f32_16x16x32_bf16 v[88:91], v[172:175], a[116:119], v[88:91]\n"
    "ds_read_b64_tr_b16 v[150:151], v176 offset:4608\n"
    "ds_read_b128 a[168:171], v254 offset:4096\n"
    "v_mfma_f32_16x16x32_bf16 v[104:107], v[160:163], a[72:75], v[104:107]\n"
    "buffer_load_dwordx4 v[216:219], v190, s[76:79], s74 offen sc1\n"
    "v_mfma_f32_16x16x32_bf16 v[104:107], v[164:167], a[88:91], v[104:107]\n"
    "ds_read_b64_tr_b16 v[152:153], v176 offset:8192\n"
    "ds_read_b128 a[172:175], v254 offset:6144\n"
    "v_mfma_f32_16x16x32_bf16 v[104:107], v[168:171], a[104:107], v[104:107]\n"
    "s_add_u32 s68, s87, 54400\n"
    "buffer_load_dwordx4 v187, s[36:39], s68 offen offset:3072 lds\n"
    "v_mfma_f32_16x16x32_bf16 v[104:107], v[172:175], a[120:123], v[104:107]\n"
    "ds_read_b64_tr_b16 v[154:155], v176 offset:8704\n"
    "ds_read_b128 a[176:179], v255 offset:0\n"
    "v_mfma_f32_16x16x32_bf16 v[120:123], v[160:163], a[76:79], v[120:123]\n"
    "v_add_f32 v212, v212, v203\n"
    "v_add_f32 v212, v212, v204\n"
    "v_mfma_f32_16x16x32_bf16 v[120:123], v[164:167], a[92:95], v[120:123]\n"
    "s_add_u32 m0, s57, 32768\n"
    "ds_read_b64_tr_b16 v[156:157], v176 offset:12288\n"
    "ds_read_b128 a[180:183], v255 offset:2048\n"
    "v_mfma_f32_16x16x32_bf16 v[120:123], v[168:171], a[108:111], v[120:123]\n"
    "s_add_u32 s68, s83, 0\n"
    "buffer_load_dwordx4 v184, s[40:43], s68 offen offset:0 lds\n"
    "v_mfma_f32_16x16x32_bf16 v[120:123], v[172:175], a[124:127], v[120:123]\n"
    "ds_read_b64_tr_b16 v[158:159], v176 offset:12800\n"
    "ds_read_b128 a[184:187], v255 offset:4096\n"
    "v_mfma_f32_16x16x32_bf16 v[76:79], v[128:131], a[64:67], v[76:79]\n"
    "s_lshl_b32 s75, s71, 9\n"
    "v_add_f32 v212, v212, v205\n"
    "v_add_f32 v212, v212, v206\n"
    "v_mfma_f32_16x16x32_bf16 v[76:79], v[132:135], a[80:83], v[76:79]\n"
    "ds_read_b64_tr_b16 v[160:161], v177 offset:0\n"
    "ds_read_b128 a[188:191], v255 offset:6144\n"
    "v_mfma_f32_16x16x32_bf16 v[76:79], v[136:139], a[96:99], v[76:79]\n"
    "s_add_u32 s68, s83, 64512\n"
    "buffer_load_dwordx4 v185, s[40:43], s68 offen offset:1024 lds\n"
    "v_mfma_f32_16x16x32_bf16 v[76:79], v[140:143], a[112:115], v[76:79]\n"
    "ds_read_b64_tr_b16 v[162:163], v177 offset:512\n"
    "v_add_f32 v212, v212, v207\n"
    "v_mfma_f32_16x16x32_bf16 v[92:95], v[128:131], a[68:71], v[92:95]\n"
    "v_add_f32 v212, v212, v208\n"
    "v_add_f32 v212, v212, v209\n"
    "v_mfma_f32_16x16x32_bf16 v[92:95], v[132:135], a[84:87], v[92:95]\n"
    "ds_read_b64_tr_b16 v[164:165], v177 offset:4096\n"
    "v_add_f32 v212, v212, v210\n"
    "v_mfma_f32_16x16x32_bf16 v[92:95], v[136:139], a[100:103], v[92:95]\n"
    "s_add_u32 s68, s83, 129024\n"
    "buffer_load_dwordx4 v184, s[40:43], s68 offen offset:2048 lds\n"
    "v_mfma_f32_16x16x32_bf16 v[92:95], v[140:143], a[116:119], v[92:95]\n"
    "ds_read_b64_tr_b16 v[166:167], v177 offset:4608\n"
    "v_add_f32 v212, v212, v211\n"
    "v_mfma_f32_16x16x32_bf16 v[108:111], v[128:131], a[72:75], v[108:111]\n"
    "s_nop 1\n"
    "v_add_f32_dpp v212, v212, v212 quad_perm:[1,0,3,2] row_mask:0xf bank_mask:0xf\n"
    "v_mfma_f32_16x16x32_bf16 v[108:111], v[132:135], a[88:91], v[108:111]\n"
    "ds_read_b64_tr_b16 v[168:169], v177 offset:8192\n"
    "s_nop 1\n"
    "v_mfma_f32_16x16x32_bf16 v[108:111], v[136:139], a[104:107], v[108:111]\n"
    "s_add_u32 s68, s83, 193536\n"
    "buffer_load_dwordx4 v185, s[40:43], s68 offen offset:3072 lds\n"
    "v_mfma_f32_16x16x32_bf16 v[108:111], v[140:143], a[120:123], v[108:111]\n"
    "ds_read_b64_tr_b16 v[170:171], v177 offset:8704\n"
    "v_add_f32_dpp v212, v212, v212 quad_perm:[2,3,0,1] row_mask:0xf bank_mask:0xf\n"
    "v_mfma_f32_16x16x32_bf16 v[124:127], v[128:131], a[76:79], v[124:127]\n"
    "s_nop 1\n"
    "v_add_f32_dpp v212, v212, v212 row_half_mirror row_mask:0xf bank_mask:0xf\n"
    "v_mfma_f32_16x16x32_bf16 v[124:127], v[132:135], a[92:95], v[124:127]\n"
    "s_add_u32 m0, s57, 49152\n"
    "ds_read_b64_tr_b16 v[172:173], v177 offset:12288\n"
    "v_mul_f32 v212, 0x3c000000, v212\n"
    "v_mfma_f32_16x16x32_bf16 v[124:127], v[136:139], a[108:111], v[124:127]\n"
    "s_add_u32 s68, s83, 128\n"
    "buffer_load_dwordx4 v184, s[40:43], s68 offen offset:0 lds\n"
    "v_mfma_f32_16x16x32_bf16 v[124:127], v[140:143], a[124:127], v[124:127]\n"
    "ds_read_b64_tr_b16 v[174:175], v177 offset:12800\n"
    "v_sub_f32 v196, v196, v212\n"
    "s_waitcnt lgkmcnt(7)\n"
    "v_mfma_f32_16x16x32_bf16 v[0:3], v[144:147], a[128:131], v[0:3]\n"
    "s_waitcnt vmcnt(14)\n"
    "v_sub_f32 v197, v197, v212\n"
    "v_sub_f32 v198, v198, v212\n"
    "v_mfma_f32_16x16x32_bf16 v[0:3], v[148:151], a[144:147], v[0:3]\n"
    "ds_read_b64_tr_b16 v[128:129], v178 offset:0\n"
    "ds_read_b128 a[192:195], v188 offset:0\n"
    "v_mfma_f32_16x16x32_bf16 v[0:3], v[152:155], a[160:163], v[0:3]\n"
    "s_add_u32 s68, s83, 64640\n"
    "buffer_load_dwordx4 v185, s[40:43], s68 offen offset:1024 lds\n"
    "v_mfma_f32_16x16x32_bf16 v[0:3], v[156:159], a[176:179], v[0:3]\n"
    ".p2align 12\n"
    "L_pad1:\n"
    "s_cbranch_execz L_pad5\n"
    "ds_read_b64_tr_b16 v[130:131], v178 offset:512\n"
    "ds_read_b128 a[196:199], v188 offset:2048\n"
    "v_mfma_f32_16x16x32_bf16 v[16:19], v[144:147], a[132:135], v[16:19]\n"
    "v_sub_f32 v199, v199, v212\n"
    "v_sub_f32 v200, v200, v212\n"
    "v_mfma_f32_16x16x32_bf16 v[16:19], v[148:151], a[148:151], v[16:19]\n"
    "ds_read_b64_tr_b16 v[132:133], v178 offset:4096\n"
    "ds_read_b128 a[200:203], v188 offset:4096\n"
    "v_mfma_f32_16x16x32_bf16 v[16:19], v[152:155], a[164:167], v[16:19]\n"
    "s_add_u32 s68, s83, 129152\n"
    "buffer_load_dwordx4 v184, s[40:43], s68 offen offset:2048 lds\n"
    "v_mfma_f32_16x16x32_bf16 v[16:19], v[156:159], a[180:183], v[16:19]\n"
    "ds_read_b64_tr_b16 v[134:135], v178 offset:4608\n"
    "ds_read_b128 a[204:207], v188 offset:6144\n"
    "v_mfma_f32_16x16x32_bf16 v[32:35], v[144:147], a[136:139], v[32:35]\n"
    "v_sub_f32 v201, v201, v212\n"
    "v_sub_f32 v202, v202, v212\n"
    "v_mfma_f32_16x16x32_bf16 v[32:35], v[148:151], a[152:155], v[32:35]\n"
    "ds_read_b64_tr_b16 v[136:137], v178 offset:8192\n"
    "ds_read_b128 a[208:211], v189 offset:0\n"
    "v_mfma_f32_16x16x32_bf16 v[32:35], v[152:155], a[168:171], v[32:35]\n"
    "s_add_u32 s68, s83, 193664\n"
    "buffer_load_dwordx4 v185, s[40:43], s68 offen offset:3072 lds\n"
    "s_cmp_lg_u32 s33, 240\n"
    "s_cbranch_scc0 L_eo\n"
    "L_eo_r:\n"
    "v_mfma_f32_16x16x32_bf16 v[32:35], v[156:159], a[184:187], v[32:35]\n"
    "ds_read_b64_tr_b16 v[138:139], v178 offset:8704\n"
    "ds_read_b128 a[212:215], v189 offset:2048\n"
    "v_mfma_f32_16x16x32_bf16 v[48:51], v[144:147], a[140:143], v[48:51]\n"
    "s_waitcnt vmcnt(9)\n"
    "v_xor_b32 v216, 1, v216\n"
    "v_xor_b32 v217, 1, v217\n"
    "v_xor_b32 v218, 1, v218\n"
    "v_xor_b32 v219, 1, v219\n"
    "v_or3_b32 v216, v216, v217, v218\n"
    "v_or_b32 v216, v216, v219\n"
    "v_cmp_ne_u32 vcc, 0, v216\n"
    "s_cbranch_vccz L_fm_ok\n"
    "s_mov_b32 s14, 0x40000\n"
    "L_fm_spin:\n"
    "buffer_load_dwordx4 v[216:219], v190, s[76:79], s74 offen sc1\n"
    "s_sub_u32 s14, s14, 1\n"
    "s_waitcnt vmcnt(0)\n"
    "s_cbranch_scc1 L_fm_ok\n"
    "v_xor_b32 v216, 1, v216\n"
    "v_xor_b32 v217, 1, v217\n"
    "v_xor_b32 v218, 1, v218\n"
    "v_xor_b32 v219, 1, v219\n"
    "v_or3_b32 v216, v216, v217, v218\n"
    "v_or_b32 v216, v216, v219\n"
    "v_cmp_ne_u32 vcc, 0, v216\n"
    "s_cbranch_vccz L_fm_ok\n"
    "s_sleep 3\n"
    "s_branch L_fm_spin\n"
    "L_fm_ok:\n"
    "v_mfma_f32_16x16x32_bf16 v[48:51], v[148:151], a[156:159], v[48:51]\n"
    "ds_read_b64_tr_b16 v[140:141], v178 offset:12288\n"
    "ds_read_b128 a[216:219], v189 offset:4096\n"
    "v_mfma_f32_16x16x32_bf16 v[48:51], v[152:155], a[172:175], v[48:51]\n"
    "s_add_u32 s74, s75, 0x60000\n"
    "buffer_load_dwordx4 v[216:219], v215, s[76:79], s74 offen sc1\n"
    "v_mfma_f32_16x16x32_bf16 v[48:51], v[156:159], a[188:191], v[48:51]\n"
    "ds_read_b64_tr_b16 v[142:143], v178 offset:12800\n"
    "ds_read_b128 a[220:223], v189 offset:6144\n"
    "s_waitcnt lgkmcnt(15)\n"
    "v_mfma_f32_16x16x32_bf16 v[4:7], v[160:163], a[128:131], v[4:7]\n"
    "s_add_u32 s74, s75, 0x63000\n"
    "buffer_load_dwordx4 v[220:223], v215, s[76:79], s74 offen sc1\n"
    "v_mfma_f32_16x16x32_bf16 v[4:7], v[164:167], a[144:147], v[4:7]\n"
    "ds_read_b64_tr_b16 v[144:145], v179 offset:0\n"
    "v_sub_f32 v203, v203, v212\n"
    "v_mfma_f32_16x16x32_bf16 v[4:7], v[168:171], a[160:163], v[4:7]\n"
    "s_add_u32 s74, s75, 0x66000\n"
    "buffer_load_dwordx4 v[224:227], v215, s[76:79], s74 offen sc1\n"
    "v_mfma_f32_16x16x32_bf16 v[4:7], v[172:175], a[176:179], v[4:7]\n"
    "ds_read_b64_tr_b16 v[146:147], v179 offset:512\n"
    "v_sub_f32 v204, v204, v212\n"
    "v_mfma_f32_16x16x32_bf16 v[20:23], v[160:163], a[132:135], v[20:23]\n"
    "s_add_u32 s74, s75, 0x69000\n"
    "buffer_load_dwordx4 v[228:231], v215, s[76:79], s74 offen sc1\n"
    "v_mfma_f32_16x16x32_bf16 v[20:23], v[164:167], a[148:151], v[20:23]\n"
    "ds_read_b64_tr_b16 v[148:149], v179 offset:4096\n"
    "v_sub_f32 v205, v205, v212\n"
    "v_mfma_f32_16x16x32_bf16 v[20:23], v[168:171], a[164:167], v[20:23]\n"
    "v_sub_f32 v206, v206, v212\n"
    "v_sub_f32 v207, v207, v212\n"
    "v_mfma_f32_16x16x32_bf16 v[20:23], v[172:175], a[180:183], v[20:23]\n"
    "ds_read_b64_tr_b16 v[150:151], v179 offset:4608\n"
    "v_sub_f32 v208, v208, v212\n"
    "v_mfma_f32_16x16x32_bf16 v[36:39], v[160:163], a[136:139], v[36:39]\n"
    "v_sub_f32 v209, v209, v212\n"
    "v_sub_f32 v210, v210, v212\n"
    "v_mfma_f32_16x16x32_bf16 v[36:39], v[164:167], a[152:155], v[36:39]\n"
    "ds_read_b64_tr_b16 v[152:153], v179 offset:8192\n"
    "v_sub_f32 v211, v211, v212\n"
    "v_mfma_f32_16x16x32_bf16 v[36:39], v[168:171], a[168:171], v[36:39]\n"
    "v_mul_f32 v213, v196, v196\n"
    "v_fmac_f32 v213, v197, v197\n"
    "v_mfma_f32_16x16x32_bf16 v[36:39], v[172:175], a[184:187], v[36:39]\n"
    "ds_read_b64_tr_b16 v[154:155], v179 offset:8704\n"
    "v_fmac_f32 v213, v198, v198\n"
    "v_mfma_f32_16x16x32_bf16 v[52:55], v[160:163], a[140:143], v[52:55]\n"
    "v_fmac_f32 v213, v199, v199\n"
    "v_fmac_f32 v213, v200, v200\n"
    "v_mfma_f32_16x16x32_bf16 v[52:55], v[164:167], a[156:159], v[52:55]\n"
    "ds_read_b64_tr_b16 v[156:157], v179 offset:12288\n"
    "v_fmac_f32 v213, v201, v201\n"
    "v_mfma_f32_16x16x32_bf16 v[52:55], v[168:171], a[172:175], v[52:55]\n"
    "v_fmac_f32 v213, v202, v202\n"
    "v_fmac_f32 v213, v203, v203\n"
    "v_mfma_f32_16x16x32_bf16 v[52:55], v[172:175], a[188:191], v[52:55]\n"
    "ds_read_b64_tr_b16 v[158:159], v179 offset:12800\n"
    "v_fmac_f32 v213, v204, v204\n"
    "s_waitcnt lgkmcnt(9)\n"
    "v_mfma_f32_16x16x32_bf16 v[8:11], v[128:131], a[128:131], v[8:11]\n"
    "s_waitcnt vmcnt(12)\n"
    "v_fmac_f32 v213, v205, v205\n"
    "v_fmac_f32 v213, v206, v206\n"
    "v_mfma_f32_16x16x32_bf16 v[8:11], v[132:135], a[144:147], v[8:11]\n"
    "ds_read_b64_tr_b16 v[160:161], v176 offset:16384\n"
    "ds_read_b128 a[224:227], v188 offset:8192\n"
    "v_mfma_f32_16x16x32_bf16 v[8:11], v[136:139], a[160:163], v[8:11]\n"
    "global_load_dword v251, v250, s[50:51]\n"
    "v_mfma_f32_16x16x32_bf16 v[8:11], v[140:143], a[176:179], v[8:11]\n"
    "ds_read_b64_tr_b16 v[162:163], v176 offset:16896\n"
    "ds_read_b128 a[228:231], v188 offset:10240\n"
    "v_mfma_f32_16x16x32_bf16 v[24:27], v[128:131], a[132:135], v[24:27]\n"
    "v_fmac_f32 v213, v207, v207\n"
    "v_fmac_f32 v213, v208, v208\n"
    "v_mfma_f32_16x16x32_bf16 v[24:27], v[132:135], a[148:151], v[24:27]\n"
    "ds_read_b64_tr_b16 v[164:165], v176 offset:20480\n"
    "ds_read_b128 a[232:235], v188 offset:12288\n"
    "v_mfma_f32_16x16x32_bf16 v[24:27], v[136:139], a[164:167], v[24:27]\n"
    "v_fmac_f32 v213, v209, v209\n"
    "v_fmac_f32 v213, v210, v210\n"
    "v_mfma_f32_16x16x32_bf16 v[24:27], v[140:143], a[180:183], v[24:27]\n"
    "ds_read_b64_tr_b16 v[166:167], v176 offset:20992\n"
    "ds_read_b128 a[236:239], v188 offset:14336\n"
    "v_mfma_f32_16x16x32_bf16 v[40:43], v[128:131], a[136:139], v[40:43]\n"
    "v_fmac_f32 v213, v211, v211\n"
    "s_nop 1\n"
    "v_mfma_f32_16x16x32_bf16 v[40:43], v[132:135], a[152:155], v[40:43]\n"
    "ds_read_b64_tr_b16 v[168:169], v176 offset:24576\n"
    "ds_read_b128 a[240:243], v189 offset:8192\n"
    "v_mfma_f32_16x16x32_bf16 v[40:43], v[136:139], a[168:171], v[40:43]\n"
    "v_add_f32_dpp v213, v213, v213 quad_perm:[1,0,3,2] row_mask:0xf bank_mask:0xf\n"
    "s_nop 1\n"
    "v_mfma_f32_16x16x32_bf16 v[40:43], v[140:143], a[184:187], v[40:43]\n"
    "ds_read_b64_tr_b16 v[170:171], v176 offset:25088\n"
    "ds_read_b128 a[244:247], v189 offset:10240\n"
    "v_mfma_f32_16x16x32_bf16 v[56:59], v[128:131], a[140:143], v[56:59]\n"
    "v_add_f32_dpp v213, v213, v213 quad_perm:[2,3,0,1] row_mask:0xf bank_mask:0xf\n"
    "s_nop 1\n"
    "v_mfma_f32_16x16x32_bf16 v[56:59], v[132:135], a[156:159], v[56:59]\n"
    "ds_read_b64_tr_b16 v[172:173], v176 offset:28672\n"
    "ds_read_b128 a[248:251], v189 offset:12288\n"
    "v_mfma_f32_16x16x32_bf16 v[56:59], v[136:139], a[172:175], v[56:59]\n"
    "v_add_f32_dpp v213, v213, v213 row_half_mirror row_mask:0xf bank_mask:0xf\n"
    "v_mfma_f32_16x16x32_bf16 v[56:59], v[140:143], a[188:191], v[56:59]\n"
    "ds_read_b64_tr_b16 v[174:175], v176 offset:29184\n"
    "ds_read_b128 a[252:255], v189 offset:14336\n"
    "s_waitcnt lgkmcnt(15)\n"
    "v_mfma_f32_16x16x32_bf16 v[12:15], v[144:147], a[128:131], v[12:15]\n"
    "v_mfma_f32_16x16x32_bf16 v[12:15], v[148:151], a[144:147], v[12:15]\n"
    "ds_read_b64_tr_b16 v[128:129], v177 offset:16384\n"
    "v_mfma_f32_16x16x32_bf16 v[12:15], v[152:155], a[160:163], v[12:15]\n"
    "v_mfma_f32_16x16x32_bf16 v[12:15], v[156:159], a[176:179], v[12:15]\n"
    "ds_read_b64_tr_b16 v[130:131], v177 offset:16896\n"
    "v_mfma_f32_16x16x32_bf16 v[28:31], v[144:147], a[132:135], v[28:31]\n"
    "v_mfma_f32_16x16x32_bf16 v[28:31], v[148:151], a[148:151], v[28:31]\n"
    "ds_read_b64_tr_b16 v[132:133], v177 offset:20480\n"
    "v_mfma_f32_16x16x32_bf16 v[28:31], v[152:155], a[164:167], v[28:31]\n"
    "v_mfma_f32_16x16x32_bf16 v[28:31], v[156:159], a[180:183], v[28:31]\n"
    "ds_read_b64_tr_b16 v[134:135], v177 offset:20992\n"
    "v_mfma_f32_16x16x32_bf16 v[44:47], v[144:147], a[136:139], v[44:47]\n"
    "s_waitcnt vmcnt(1)\n"
    "v_mfma_f32_16x16x32_bf16 v[44:47], v[148:151], a[152:155], v[44:47]\n"
    "ds_read_b64_tr_b16 v[136:137], v177 offset:24576\n"
    "v_add_f32 v216, v216, v220\n"
    "v_mfma_f32_16x16x32_bf16 v[44:47], v[152:155], a[168:171], v[44:47]\n"
    "v_add_f32 v224, v224, v228\n"
    "v_add_f32 v217, v217, v221\n"
    "v_mfma_f32_16x16x32_bf16 v[44:47], v[156:159], a[184:187], v[44:47]\n"
    "ds_read_b64_tr_b16 v[138:139], v177 offset:25088\n"
    "v_add_f32 v225, v225, v229\n"
    "v_mfma_f32_16x16x32_bf16 v[60:63], v[144:147], a[140:143], v[60:63]\n"
    "v_add_f32 v218, v218, v222\n"
    "v_add_f32 v226, v226, v230\n"
    "v_mfma_f32_16x16x32_bf16 v[60:63], v[148:151], a[156:159], v[60:63]\n"
    "ds_read_b64_tr_b16 v[140:141], v177 offset:28672\n"
    "v_add_f32 v219, v219, v223\n"
    "v_mfma_f32_16x16x32_bf16 v[60:63], v[152:155], a[172:175], v[60:63]\n"
    "v_add_f32 v227, v227, v231\n"
    "v_add_f32 v216, v216, v224\n"
    "v_mfma_f32_16x16x32_bf16 v[60:63], v[156:159], a[188:191], v[60:63]\n"
    "ds_read_b64_tr_b16 v[142:143], v177 offset:29184\n"
    "v_add_f32 v217, v217, v225\n"
    "s_waitcnt lgkmcnt(9)\n"
    "v_mfma_f32_16x16x32_bf16 v[64:67], v[160:163], a[128:131], v[64:67]\n"
    "s_lshr_b32 s74, s33, 1\n"
    "v_add_f32 v218, v218, v226\n"
    "v_add_f32 v219, v219, v227\n"
    "v_mfma_f32_16x16x32_bf16 v[64:67], v[164:167], a[144:147], v[64:67]\n"
    "s_lshl_b32 s74, s74, 12\n"
    "ds_read_b64_tr_b16 v[144:145], v178 offset:16384\n"
    "v_cvt_pk_bf16_f32 v220, v216, v217\n"
    "v_mfma_f32_16x16x32_bf16 v[64:67], v[168:171], a[160:163], v[64:67]\n"
    "s_and_b32 s75, s33, 1\n"
    "v_cvt_pk_bf16_f32 v221, v218, v219\n"
    "v_lshlrev_b32 v216, 16, v220\n"
    "v_mfma_f32_16x16x32_bf16 v[64:67], v[172:175], a[176:179], v[64:67]\n"
    "s_lshl_b32 s75, s75, 7\n"
    "ds_read_b64_tr_b16 v[146:147], v178 offset:16896\n"
    "v_and_b32 v217, 0xffff0000, v220\n"
    "v_mfma_f32_16x16x32_bf16 v[80:83], v[160:163], a[132:135], v[80:83]\n"
    "s_add_u32 s74, s74, s75\n"
    "v_lshlrev_b32 v218, 16, v221\n"
    "v_and_b32 v219, 0xffff0000, v221\n"
    "v_mfma_f32_16x16x32_bf16 v[80:83], v[164:167], a[148:151], v[80:83]\n"
    "s_lshl_b32 s75, s71, 19\n"
    "ds_read_b64_tr_b16 v[148:149], v178 offset:20480\n"
    "v_add_f32 v216, v195, v216\n"
    "v_mfma_f32_16x16x32_bf16 v[80:83], v[168:171], a[164:167], v[80:83]\n"
    "s_add_u32 s74, s74, s75\n"
    "v_add_f32 v217, v195, v217\n"
    "v_add_f32 v218, v195, v218\n"
    "v_mfma_f32_16x16x32_bf16 v[80:83], v[172:175], a[180:183], v[80:83]\n"
    "s_add_u32 s74, s74, 0x100000\n"
    "ds_read_b64_tr_b16 v[150:151], v178 offset:20992\n"
    "v_add_f32 v219, v195, v219\n"
    "v_mfma_f32_16x16x32_bf16 v[96:99], v[160:163], a[136:139], v[96:99]\n"
    "s_add_u32 s75, s21, 0x1000\n"
    "v_cvt_pk_bf16_f32 v220, v216, v217\n"
    "v_cvt_pk_bf16_f32 v221, v218, v219\n"
    "v_mfma_f32_16x16x32_bf16 v[96:99], v[164:167], a[152:155], v[96:99]\n"
    "s_lshl_b32 s75, s75, 5\n"
    "ds_read_b64_tr_b16 v[152:153], v178 offset:24576\n"
    "v_lshlrev_b32 v216, 16, v220\n"
    "v_mfma_f32_16x16x32_bf16 v[96:99], v[168:171], a[168:171], v[96:99]\n"
    "s_add_u32 s75, s75, 0x5800000\n"
    "global_load_dword v251, v250, s[52:53]\n"
    "v_mfma_f32_16x16x32_bf16 v[96:99], v[172:175], a[184:187], v[96:99]\n"
    "ds_read_b64_tr_b16 v[154:155], v178 offset:25088\n"
    "v_and_b32 v217, 0xffff0000, v220\n"
    "v_mfma_f32_16x16x32_bf16 v[112:115], v[160:163], a[140:143], v[112:115]\n"
    "v_lshlrev_b32 v218, 16, v221\n"
    "v_and_b32 v219, 0xffff0000, v221\n"
    "v_mfma_f32_16x16x32_bf16 v[112:115], v[164:167], a[156:159], v[112:115]\n"
    "ds_read_b64_tr_b16 v[156:157], v178 offset:28672\n"
    "v_mul_f32 v216, 0x3e0293ee, v216\n"
    "v_mfma_f32_16x16x32_bf16 v[112:115], v[168:171], a[172:175], v[112:115]\n"
    "v_mul_f32 v217, 0x3e0293ee, v217\n"
    "v_mul_f32 v218, 0x3e0293ee, v218\n"
    "v_mfma_f32_16x16x32_bf16 v[112:115], v[172:175], a[188:191], v[112:115]\n"
    "ds_read_b64_tr_b16 v[158:159], v178 offset:29184\n"
    "v_mul_f32 v219, 0x3e0293ee, v219\n"
    "s_waitcnt lgkmcnt(8)\n"
    "v_mfma_f32_16x16x32_bf16 v[68:71], v[128:131], a[128:131], v[68:71]\n"
    "ds_read_b64_tr_b16 v[160:161], v179 offset:16384\n"
    "ds_write_b64 v193, v[212:213]\n"
    "v_mfma_f32_16x16x32_bf16 v[68:71], v[132:135], a[144:147], v[68:71]\n"
    "ds_read_b64_tr_b16 v[162:163], v179 offset:16896\n"
    "ds_write_b128 v194, v[216:219] offset:1024\n"
    "v_mfma_f32_16x16x32_bf16 v[68:71], v[136:139], a[160:163], v[68:71]\n"
    "ds_read_b64_tr_b16 v[164:165], v179 offset:20480\n"
    "ds_bpermute_b32 v204, v246, v232\n"
    "v_mfma_f32_16x16x32_bf16 v[68:71], v[140:143], a[176:179], v[68:71]\n"
    "ds_read_b64_tr_b16 v[166:167], v179 offset:20992\n"
    "ds_bpermute_b32 v205, v246, v233\n"
    "v_mfma_f32_16x16x32_bf16 v[84:87], v[128:131], a[132:135], v[84:87]\n"
    "ds_read_b64_tr_b16 v[168:169], v179 offset:24576\n"
    "ds_bpermute_b32 v206, v246, v234\n"
    "v_mfma_f32_16x16x32_bf16 v[84:87], v[132:135], a[148:151], v[84:87]\n"
    "ds_read_b64_tr_b16 v[170:171], v179 offset:25088\n"
    "ds_bpermute_b32 v207, v246, v235\n"
    "v_mfma_f32_16x16x32_bf16 v[84:87], v[136:139], a[164:167], v[84:87]\n"
    "ds_read_b64_tr_b16 v[172:173], v179 offset:28672\n"
    "ds_bpermute_b32 v208, v246, v236\n"
    "v_mfma_f32_16x16x32_bf16 v[84:87], v[140:143], a[180:183], v[84:87]\n"
    "ds_read_b64_tr_b16 v[174:175], v179 offset:29184\n"
    "ds_bpermute_b32 v209, v246, v237\n"
    "v_mfma_f32_16x16x32_bf16 v[100:103], v[128:131], a[136:139], v[100:103]\n"
    "ds_bpermute_b32 v210, v246, v238\n"
    "ds_bpermute_b32 v211, v246, v239\n"
    "v_mfma_f32_16x16x32_bf16 v[100:103], v[132:135], a[152:155], v[100:103]\n"
    "v_lshrrev_b32 v193, 7, v190\n"
    "v_lshl_add_u32 v193, v193, 5, s19\n"
    "v_mfma_f32_16x16x32_bf16 v[100:103], v[136:139], a[168:171], v[100:103]\n"
    "v_mfma_f32_16x16x32_bf16 v[100:103], v[140:143], a[184:187], v[100:103]\n"
    "v_mfma_f32_16x16x32_bf16 v[116:119], v[128:131], a[140:143], v[116:119]\n"
    "v_mfma_f32_16x16x32_bf16 v[116:119], v[132:135], a[156:159], v[116:119]\n"
    "v_mfma_f32_16x16x32_bf16 v[116:119], v[136:139], a[172:175], v[116:119]\n"
    "v_mfma_f32_16x16x32_bf16 v[116:119], v[140:143], a[188:191], v[116:119]\n"
    "s_mov_b32 s40, s50\n"
    "s_waitcnt lgkmcnt(15)\n"
    "s_waitcnt lgkmcnt(0)\n"
    "s_barrier\n"
    "v_mfma_f32_16x16x32_bf16 v[72:75], v[144:147], a[128:131], v[72:75]\n"
    "s_mov_b32 s41, s51\n"
    "ds_read_b64 v[232:233], v214 offset:0\n"
    "v_mfma_f32_16x16x32_bf16 v[72:75], v[148:151], a[144:147], v[72:75]\n"
    "s_add_u32 m0, s57, 65536\n"
    "ds_read_b64_tr_b16 v[128:129], v176 offset:32768\n"
    "ds_read_b64 v[234:235], v214 offset:8192\n"
    "v_mfma_f32_16x16x32_bf16 v[72:75], v[152:155], a[160:163], v[72:75]\n"
    "s_add_u32 s68, s80, 0\n"
    "buffer_load_dwordx4 v184, s[40:43], s68 offen offset:0 lds\n"
    "v_mfma_f32_16x16x32_bf16 v[72:75], v[156:159], a[176:179], v[72:75]\n"
    "ds_read_b64_tr_b16 v[130:131], v176 offset:33280\n"
    "ds_read_b64 v[236:237], v214 offset:16384\n"
    "v_mfma_f32_16x16x32_bf16 v[88:91], v[144:147], a[132:135], v[88:91]\n"
    "ds_read_b64 v[238:239], v214 offset:24576\n"
    "v_mfma_f32_16x16x32_bf16 v[88:91], v[148:151], a[148:151], v[88:91]\n"
    "ds_read_b64_tr_b16 v[132:133], v176 offset:36864\n"
    "v_lshlrev_b32 v196, 16, v204\n"
    "v_mfma_f32_16x16x32_bf16 v[88:91], v[152:155], a[164:167], v[88:91]\n"
    "s_add_u32 s68, s80, 64512\n"
    "buffer_load_dwordx4 v185, s[40:43], s68 offen offset:1024 lds\n"
    "v_mfma_f32_16x16x32_bf16 v[88:91], v[156:159], a[180:183], v[88:91]\n"
    "ds_read_b64_tr_b16 v[134:135], v176 offset:37376\n"
    "v_and_b32 v197, 0xffff0000, v204\n"
    "v_mfma_f32_16x16x32_bf16 v[104:107], v[144:147], a[136:139], v[104:107]\n"
    "v_lshlrev_b32 v198, 16, v205\n"
    "v_mfma_f32_16x16x32_bf16 v[104:107], v[148:151], a[152:155], v[104:107]\n"
    "ds_read_b64_tr_b16 v[136:137], v176 offset:40960\n"
    "v_and_b32 v199, 0xffff0000, v205\n"
    "v_mfma_f32_16x16x32_bf16 v[104:107], v[152:155], a[168:171], v[104:107]\n"
    "s_add_u32 s68, s80, 129024\n"
    "buffer_load_dwordx4 v184, s[40:43], s68 offen offset:2048 lds\n"
    "v_mfma_f32_16x16x32_bf16 v[104:107], v[156:159], a[184:187], v[104:107]\n"
    "ds_read_b64_tr_b16 v[138:139], v176 offset:41472\n"
    "v_lshlrev_b32 v200, 16, v206\n"
    "v_mfma_f32_16x16x32_bf16 v[120:123], v[144:147], a[140:143], v[120:123]\n"
    "v_and_b32 v201, 0xffff0000, v206\n"
    "v_mfma_f32_16x16x32_bf16 v[120:123], v[148:151], a[156:159], v[120:123]\n"
    "ds_read_b64_tr_b16 v[140:141], v176 offset:45056\n"
    "v_lshlrev_b32 v202, 16, v207\n"
    "v_mfma_f32_16x16x32_bf16 v[120:123], v[152:155], a[172:175], v[120:123]\n"
    "s_add_u32 s68, s80, 193536\n"
    "buffer_load_dwordx4 v185, s[40:43], s68 offen offset:3072 lds\n"
    "v_mfma_f32_16x16x32_bf16 v[120:123], v[156:159], a[188:191], v[120:123]\n"
    "ds_read_b64_tr_b16 v[142:143], v176 offset:45568\n"
    "v_and_b32 v203, 0xffff0000, v207\n"
    "v_mfma_f32_16x16x32_bf16 v[76:79], v[160:163], a[128:131], v[76:79]\n"
    "v_lshlrev_b32 v204, 16, v208\n"
    "v_mfma_f32_16x16x32_bf16 v[76:79], v[164:167], a[144:147], v[76:79]\n"
    "s_add_u32 m0, s57, 81920\n"
    "ds_read_b64_tr_b16 v[144:145], v177 offset:32768\n"
    "v_and_b32 v205, 0xffff0000, v208\n"
    "v_mfma_f32_16x16x32_bf16 v[76:79], v[168:171], a[160:163], v[76:79]\n"
    "s_add_u32 s68, s81, 0\n"
    "buffer_load_dwordx4 v184, s[40:43], s68 offen offset:0 lds\n"
    "v_mfma_f32_16x16x32_bf16 v[76:79], v[172:175], a[176:179], v[76:79]\n"
    "ds_read_b64_tr_b16 v[146:147], v177 offset:33280\n"
    "v_lshlrev_b32 v206, 16, v209\n"
    "v_mfma_f32_16x16x32_bf16 v[92:95], v[160:163], a[132:135], v[92:95]\n"
    "v_and_b32 v207, 0xffff0000, v209\n"
    "v_mfma_f32_16x16x32_bf16 v[92:95], v[164:167], a[148:151], v[92:95]\n"
    "ds_read_b64_tr_b16 v[148:149], v177 offset:36864\n"
    "v_lshlrev_b32 v208, 16, v210\n"
    "v_mfma_f32_16x16x32_bf16 v[92:95], v[168:171], a[164:167], v[92:95]\n"
    "s_add_u32 s68, s81, 64512\n"
    "buffer_load_dwordx4 v185, s[40:43], s68 offen offset:1024 lds\n"
    "v_mfma_f32_16x16x32_bf16 v[92:95], v[172:175], a[180:183], v[92:95]\n"
    "ds_read_b64_tr_b16 v[150:151], v177 offset:37376\n"
    "v_and_b32 v209, 0xffff0000, v210\n"
    "v_mfma_f32_16x16x32_bf16 v[108:111], v[160:163], a[136:139], v[108:111]\n"
    "v_lshlrev_b32 v210, 16, v211\n"
    "v_mfma_f32_16x16x32_bf16 v[108:111], v[164:167], a[152:155], v[108:111]\n"
    "ds_read_b64_tr_b16 v[152:153], v177 offset:40960\n"
    "v_and_b32 v211, 0xffff0000, v211\n"
    "v_mfma_f32_16x16x32_bf16 v[108:111], v[168:171], a[168:171], v[108:111]\n"
    "s_add_u32 s68, s81, 129024\n"
    "s_waitcnt lgkmcnt(11)\n"
    "buffer_load_dwordx4 v184, s[40:43], s68 offen offset:2048 lds\n"
    "v_mfma_f32_16x16x32_bf16 v[108:111], v[172:175], a[184:187], v[108:111]\n"
    "ds_read_b64_tr_b16 v[154:155], v177 offset:41472\n"
    "v_add_f32 v214, v232, v234\n"
    "v_mfma_f32_16x16x32_bf16 v[124:127], v[160:163], a[140:143], v[124:127]\n"
    "v_add_f32 v215, v236, v238\n"
    "v_mfma_f32_16x16x32_bf16 v[124:127], v[164:167], a[156:159], v[124:127]\n"
    "ds_read_b64_tr_b16 v[156:157], v177 offset:45056\n"
    "v_add_f32 v214, v214, v215\n"
    "v_mfma_f32_16x16x32_bf16 v[124:127], v[168:171], a[172:175], v[124:127]\n"
    "s_add_u32 s68, s81, 193536\n"
    "buffer_load_dwordx4 v185, s[40:43], s68 offen offset:3072 lds\n"
    "v_mfma_f32_16x16x32_bf16 v[124:127], v[172:175], a[188:191], v[124:127]\n"
    "ds_read_b64_tr_b16 v[158:159], v177 offset:45568\n"
    "v_mul_f32 v214, 0x3e800000, v214\n"
    "s_waitcnt lgkmcnt(8)\n"
    "v_mfma_f32_16x16x32_bf16 v[0:3], v[128:131], a[192:195], v[0:3]\n"
    "v_add_f32 v215, v233, v235\n"
    "v_mfma_f32_16x16x32_bf16 v[0:3], v[132:135], a[208:211], v[0:3]\n"
    "s_add_u32 m0, s57, 98304\n"
    "ds_read_b64_tr_b16 v[160:161], v178 offset:32768\n"
    "v_add_f32 v215, v215, v237\n"
    "v_mfma_f32_16x16x32_bf16 v[0:3], v[136:139], a[224:227], v[0:3]\n"
    "s_add_u32 s68, s82, 0\n"
    "buffer_load_dwordx4 v184, s[40:43], s68 offen offset:0 lds\n"
    "v_mfma_f32_16x16x32_bf16 v[0:3], v[140:143], a[240:243], v[0:3]\n"
    "ds_read_b64_tr_b16 v[162:163], v178 offset:33280\n"
    "v_add_f32 v215, v215, v239\n"
    "v_mfma_f32_16x16x32_bf16 v[16:19], v[128:131], a[196:199], v[16:19]\n"
    "v_sub_f32 v232, v232, v214\n"
    "v_mfma_f32_16x16x32_bf16 v[16:19], v[132:135], a[212:215], v[16:19]\n"
    "ds_read_b64_tr_b16 v[164:165], v178 offset:36864\n"
    "v_sub_f32 v234, v234, v214\n"
    "v_mfma_f32_16x16x32_bf16 v[16:19], v[136:139], a[228:231], v[16:19]\n"
    "s_add_u32 s68, s82, 64512\n"
    "buffer_load_dwordx4 v185, s[40:43], s68 offen offset:1024 lds\n"
    "v_mfma_f32_16x16x32_bf16 v[16:19], v[140:143], a[244:247], v[16:19]\n"
    "ds_read_b64_tr_b16 v[166:167], v178 offset:37376\n"
    "v_sub_f32 v236, v236, v214\n"
    "v_mfma_f32_16x16x32_bf16 v[32:35], v[128:131], a[200:203], v[32:35]\n"
    "v_sub_f32 v238, v238, v214\n"
    "v_mfma_f32_16x16x32_bf16 v[32:35], v[132:135], a[216:219], v[32:35]\n"
    "ds_read_b64_tr_b16 v[168:169], v178 offset:40960\n"
    "v_mul_f32 v233, v232, v232\n"
    "v_mfma_f32_16x16x32_bf16 v[32:35], v[136:139], a[232:235], v[32:35]\n"
    "s_add_u32 s68, s82, 129024\n"
    "buffer_load_dwordx4 v184, s[40:43], s68 offen offset:2048 lds\n"
    "v_mfma_f32_16x16x32_bf16 v[32:35], v[140:143], a[248:251], v[32:35]\n"
    "ds_read_b64_tr_b16 v[170:171], v178 offset:41472\n"
    "v_fmac_f32 v233, v234, v234\n"
    "v_mfma_f32_16x16x32_bf16 v[48:51], v[128:131], a[204:207], v[48:51]\n"
    "v_fmac_f32 v233, v236, v236\n"
    "v_mfma_f32_16x16x32_bf16 v[48:51], v[132:135], a[220:223], v[48:51]\n"
    "ds_read_b64_tr_b16 v[172:173], v178 offset:45056\n"
    "v_fmac_f32 v233, v238, v238\n"
    "v_mfma_f32_16x16x32_bf16 v[48:51], v[136:139], a[236:239], v[48:51]\n"
    "s_add_u32 s68, s82, 193536\n"
    "buffer_load_dwordx4 v185, s[40:43], s68 offen offset:3072 lds\n"
    "v_mfma_f32_16x16x32_bf16 v[48:51], v[140:143], a[252:255], v[48:51]\n"
    "ds_read_b64_tr_b16 v[174:175], v178 offset:45568\n"
    "v_fmamk_f32 v215, v233, 0x43000000, v215\n"
    "s_waitcnt lgkmcnt(8)\n"
    "v_mfma_f32_16x16x32_bf16 v[4:7], v[144:147], a[192:195], v[4:7]\n"
    "v_mov_b32 v192, 0x3727c5ac\n"
    "v_mfma_f32_16x16x32_bf16 v[4:7], v[148:151], a[208:211], v[4:7]\n"
    "s_add_u32 m0, s57, 114688\n"
    "ds_read_b64_tr_b16 v[128:129], v179 offset:32768\n"
    "ds_read_b128 v[216:219], v193 offset:1024\n"
    "v_mfma_f32_16x16x32_bf16 v[4:7], v[152:155], a[224:227], v[4:7]\n"
    "s_add_u32 s68, s83, 0\n"
    "buffer_load_dwordx4 v184, s[40:43], s68 offen offset:0 lds\n"
    "v_mfma_f32_16x16x32_bf16 v[4:7], v[156:159], a[240:243], v[4:7]\n"
    "ds_read_b64_tr_b16 v[130:131], v179 offset:33280\n"
    "ds_read_b128 v[220:223], v193 offset:1040\n"
    "v_mfma_f32_16x16x32_bf16 v[20:23], v[144:147], a[196:199], v[20:23]\n"
    "ds_read_b128 v[224:227], v193 offset:1280\n"
    "v_mfma_f32_16x16x32_bf16 v[20:23], v[148:151], a[212:215], v[20:23]\n"
    "ds_read_b64_tr_b16 v[132:133], v179 offset:36864\n"
    "ds_read_b128 v[228:231], v193 offset:1296\n"
    "v_mfma_f32_16x16x32_bf16 v[20:23], v[152:155], a[228:231], v[20:23]\n"
    "s_add_u32 s68, s83, 64512\n"
    "buffer_load_dwordx4 v185, s[40:43], s68 offen offset:1024 lds\n"
    "v_mfma_f32_16x16x32_bf16 v[20:23], v[156:159], a[244:247], v[20:23]\n"
    "ds_read_b64_tr_b16 v[134:135], v179 offset:37376\n"
    "ds_read_b128 v[232:235], v193 offset:1536\n"
    "v_mfma_f32_16x16x32_bf16 v[36:39], v[144:147], a[200:203], v[36:39]\n"
    "ds_read_b128 v[236:239], v193 offset:1552\n"
    "v_mfma_f32_16x16x32_bf16 v[36:39], v[148:151], a[216:219], v[36:39]\n"
    "ds_read_b64_tr_b16 v[136:137], v179 offset:40960\n"
    "ds_read_b128 v[240:243], v193 offset:1792\n"
    "v_mfma_f32_16x16x32_bf16 v[36:39], v[152:155], a[232:235], v[36:39]\n"
    "s_add_u32 s68, s83, 129024\n"
    "buffer_load_dwordx4 v184, s[40:43], s68 offen offset:2048 lds\n"
    "v_mfma_f32_16x16x32_bf16 v[36:39], v[156:159], a[248:251], v[36:39]\n"
    "ds_read_b64_tr_b16 v[138:139], v179 offset:41472\n"
    "ds_read_b128 v[244:247], v193 offset:1808\n"
    "v_mfma_f32_16x16x32_bf16 v[52:55], v[144:147], a[204:207], v[52:55]\n"
    "v_fmamk_f32 v215, v215, 0x3b000000, v192\n"
    "v_mfma_f32_16x16x32_bf16 v[52:55], v[148:151], a[220:223], v[52:55]\n"
    "ds_read_b64_tr_b16 v[140:141], v179 offset:45056\n"
    "v_mfma_f32_16x16x32_bf16 v[52:55], v[152:155], a[236:239], v[52:55]\n"
    "s_add_u32 s68, s83, 193536\n"
    "buffer_load_dwordx4 v185, s[40:43], s68 offen offset:3072 lds\n"
    "v_mfma_f32_16x16x32_bf16 v[52:55], v[156:159], a[252:255], v[52:55]\n"
    "ds_read_b64_tr_b16 v[142:143], v179 offset:45568\n"
    "s_waitcnt lgkmcnt(15)\n"
    "v_mfma_f32_16x16x32_bf16 v[8:11], v[160:163], a[192:195], v[8:11]\n"
    "v_rsq_f32 v215, v215\n"
    "v_mfma_f32_16x16x32_bf16 v[8:11], v[164:167], a[208:211], v[8:11]\n"
    "ds_read_b64_tr_b16 v[144:145], v176 offset:49152\n"
    "v_sub_f32 v212, 0, v214\n"
    "v_mfma_f32_16x16x32_bf16 v[8:11], v[168:171], a[224:227], v[8:11]\n"
    "v_mul_f32 v212, v212, v215\n"
    "v_mfma_f32_16x16x32_bf16 v[8:11], v[172:175], a[240:243], v[8:11]\n"
    "s_add_u32 m0, s57, 0\n"
    "ds_read_b64_tr_b16 v[146:147], v176 offset:49664\n"
    "v_fma_f32 v196, v196, v215, v212\n"
    "v_mfma_f32_16x16x32_bf16 v[24:27], v[160:163], a[196:199], v[24:27]\n"
    "s_add_u32 s68, s80, 128\n"
    "buffer_load_dwordx4 v184, s[40:43], s68 offen offset:0 lds\n"
    "v_mfma_f32_16x16x32_bf16 v[24:27], v[164:167], a[212:215], v[24:27]\n"
    "ds_read_b64_tr_b16 v[148:149], v176 offset:53248\n"
    "v_fma_f32 v197, v197, v215, v212\n"
    "v_mfma_f32_16x16x32_bf16 v[24:27], v[168:171], a[228:231], v[24:27]\n"
    "v_fma_f32 v198, v198, v215, v212\n"
    "v_mfma_f32_16x16x32_bf16 v[24:27], v[172:175], a[244:247], v[24:27]\n"
    "ds_read_b64_tr_b16 v[150:151], v176 offset:53760\n"
    "v_fma_f32 v199, v199, v215, v212\n"
    "v_mfma_f32_16x16x32_bf16 v[40:43], v[160:163], a[200:203], v[40:43]\n"
    "v_fma_f32 v200, v200, v215, v212\n"
    "v_mfma_f32_16x16x32_bf16 v[40:43], v[164:167], a[216:219], v[40:43]\n"
    "ds_read_b64_tr_b16 v[152:153], v176 offset:57344\n"
    "v_fma_f32 v201, v201, v215, v212\n"
    "v_mfma_f32_16x16x32_bf16 v[40:43], v[168:171], a[232:235], v[40:43]\n"
    "s_add_u32 s68, s80, 64640\n"
    "buffer_load_dwordx4 v185, s[40:43], s68 offen offset:1024 lds\n"
    "v_mfma_f32_16x16x32_bf16 v[40:43], v[172:175], a[248:251], v[40:43]\n"
    "ds_read_b64_tr_b16 v[154:155], v176 offset:57856\n"
    "v_fma_f32 v202, v202, v215, v212\n"
    "v_mfma_f32_16x16x32_bf16 v[56:59], v[160:163], a[204:207], v[56:59]\n"
    "v_fma_f32 v203, v203, v215, v212\n"
    "v_mfma_f32_16x16x32_bf16 v[56:59], v[164:167], a[220:223], v[56:59]\n"
    "ds_read_b64_tr_b16 v[156:157], v176 offset:61440\n"
    "v_fma_f32 v204, v204, v215, v212\n"
    "v_mfma_f32_16x16x32_bf16 v[56:59], v[168:171], a[236:239], v[56:59]\n"
    "v_fma_f32 v205, v205, v215, v212\n"
    "v_mfma_f32_16x16x32_bf16 v[56:59], v[172:175], a[252:255], v[56:59]\n"
    "ds_read_b64_tr_b16 v[158:159], v176 offset:61952\n"
    "v_fma_f32 v206, v206, v215, v212\n"
    "s_waitcnt lgkmcnt(8)\n"
    "v_mfma_f32_16x16x32_bf16 v[12:15], v[128:131], a[192:195], v[12:15]\n"
    "s_add_u32 s68, s80, 129152\n"
    "buffer_load_dwordx4 v184, s[40:43], s68 offen offset:2048 lds\n"
    "v_mfma_f32_16x16x32_bf16 v[12:15], v[132:135], a[208:211], v[12:15]\n"
    "ds_read_b64_tr_b16 v[160:161], v177 offset:49152\n"
    "v_fma_f32 v207, v207, v215, v212\n"
    "v_mfma_f32_16x16x32_bf16 v[12:15], v[136:139], a[224:227], v[12:15]\n"
    "v_fma_f32 v208, v208, v215, v212\n"
    "v_mfma_f32_16x16x32_bf16 v[12:15], v[140:143], a[240:243], v[12:15]\n"
    "ds_read_b64_tr_b16 v[162:163], v177 offset:49664\n"
    "v_fma_f32 v209, v209, v215, v212\n"
    "v_mfma_f32_16x16x32_bf16 v[28:31], v[128:131], a[196:199], v[28:31]\n"
    "v_fma_f32 v210, v210, v215, v212\n"
    "v_mfma_f32_16x16x32_bf16 v[28:31], v[132:135], a[212:215], v[28:31]\n"
    "ds_read_b64_tr_b16 v[164:165], v177 offset:53248\n"
    "v_fma_f32 v211, v211, v215, v212\n"
    "v_mfma_f32_16x16x32_bf16 v[28:31], v[136:139], a[228:231], v[28:31]\n"
    "s_add_u32 s68, s80, 193664\n"
    "buffer_load_dwordx4 v185, s[40:43], s68 offen offset:3072 lds\n"
    "v_mfma_f32_16x16x32_bf16 v[28:31], v[140:143], a[244:247], v[28:31]\n"
    "ds_read_b64_tr_b16 v[166:167], v177 offset:53760\n"
    "v_fma_f32 v196, v196, v232, v216\n"
    "v_mfma_f32_16x16x32_bf16 v[44:47], v[128:131], a[200:203], v[44:47]\n"
    "v_fma_f32 v197, v197, v233, v217\n"
    "v_mfma_f32_16x16x32_bf16 v[44:47], v[132:135], a[216:219], v[44:47]\n"
    "ds_read_b64_tr_b16 v[168:169], v177 offset:57344\n"
    "v_fma_f32 v198, v198, v234, v218\n"
    "v_mfma_f32_16x16x32_bf16 v[44:47], v[136:139], a[232:235], v[44:47]\n"
    "v_fma_f32 v199, v199, v235, v219\n"
    "v_mfma_f32_16x16x32_bf16 v[44:47], v[140:143], a[248:251], v[44:47]\n"
    "s_add_u32 m0, s57, 16384\n"
    "ds_read_b64_tr_b16 v[170:171], v177 offset:57856\n"
    "v_fma_f32 v200, v200, v236, v220\n"
    "v_mfma_f32_16x16x32_bf16 v[60:63], v[128:131], a[204:207], v[60:63]\n"
    "s_add_u32 s68, s81, 128\n"
    "buffer_load_dwordx4 v184, s[40:43], s68 offen offset:0 lds\n"
    "v_mfma_f32_16x16x32_bf16 v[60:63], v[132:135], a[220:223], v[60:63]\n"
    "ds_read_b64_tr_b16 v[172:173], v177 offset:61440\n"
    "v_fma_f32 v201, v201, v237, v221\n"
    "v_mfma_f32_16x16x32_bf16 v[60:63], v[136:139], a[236:239], v[60:63]\n"
    "v_fma_f32 v202, v202, v238, v222\n"
    "v_mfma_f32_16x16x32_bf16 v[60:63], v[140:143], a[252:255], v[60:63]\n"
    "ds_read_b64_tr_b16 v[174:175], v177 offset:61952\n"
    "v_fma_f32 v203, v203, v239, v223\n"
    "s_waitcnt lgkmcnt(8)\n"
    "v_mfma_f32_16x16x32_bf16 v[64:67], v[144:147], a[192:195], v[64:67]\n"
    "v_fma_f32 v204, v204, v240, v224\n"
    "v_mfma_f32_16x16x32_bf16 v[64:67], v[148:151], a[208:211], v[64:67]\n"
    "ds_read_b64_tr_b16 v[128:129], v178 offset:49152\n"
    "v_fma_f32 v205, v205, v241, v225\n"
    "v_mfma_f32_16x16x32_bf16 v[64:67], v[152:155], a[224:227], v[64:67]\n"
    "s_add_u32 s68, s81, 64640\n"
    "buffer_load_dwordx4 v185, s[40:43], s68 offen offset:1024 lds\n"
    "v_mfma_f32_16x16x32_bf16 v[64:67], v[156:159], a[240:243], v[64:67]\n"
    ".p2align 12\n"
    "L_pad2:\n"
    "s_cbranch_execz L_pad6\n"
    "ds_read_b64_tr_b16 v[130:131], v178 offset:49664\n"
    "v_fma_f32 v206, v206, v242, v226\n"
    "v_mfma_f32_16x16x32_bf16 v[80:83], v[144:147], a[196:199], v[80:83]\n"
    "v_fma_f32 v207, v207, v243, v227\n"
    "v_mfma_f32_16x16x32_bf16 v[80:83], v[148:151], a[212:215], v[80:83]\n"
    "ds_read_b64_tr_b16 v[132:133], v178 offset:53248\n"
    "v_fma_f32 v208, v208, v244, v228\n"
    "v_mfma_f32_16x16x32_bf16 v[80:83], v[152:155], a[228:231], v[80:83]\n"
    "v_fma_f32 v209, v209, v245, v229\n"
    "v_mfma_f32_16x16x32_bf16 v[80:83], v[156:159], a[244:247], v[80:83]\n"
    "ds_read_b64_tr_b16 v[134:135], v178 offset:53760\n"
    "v_fma_f32 v210, v210, v246, v230\n"
    "v_mfma_f32_16x16x32_bf16 v[96:99], v[144:147], a[200:203], v[96:99]\n"
    "s_add_u32 s68, s81, 129152\n"
    "buffer_load_dwordx4 v184, s[40:43], s68 offen offset:2048 lds\n"
    "v_mfma_f32_16x16x32_bf16 v[96:99], v[148:151], a[216:219], v[96:99]\n"
    "ds_read_b64_tr_b16 v[136:137], v178 offset:57344\n"
    "v_fma_f32 v211, v211, v247, v231\n"
    "v_mfma_f32_16x16x32_bf16 v[96:99], v[152:155], a[232:235], v[96:99]\n"
    "v_cvt_pk_bf16_f32 v196, v196, v197\n"
    "v_mfma_f32_16x16x32_bf16 v[96:99], v[156:159], a[248:251], v[96:99]\n"
    "ds_read_b64_tr_b16 v[138:139], v178 offset:57856\n"
    "v_cvt_pk_bf16_f32 v197, v198, v199\n"
    "v_mfma_f32_16x16x32_bf16 v[112:115], v[144:147], a[204:207], v[112:115]\n"
    "v_cvt_pk_bf16_f32 v198, v200, v201\n"
    "v_mfma_f32_16x16x32_bf16 v[112:115], v[148:151], a[220:223], v[112:115]\n"
    "ds_read_b64_tr_b16 v[140:141], v178 offset:61440\n"
    "v_cvt_pk_bf16_f32 v199, v202, v203\n"
    "v_mfma_f32_16x16x32_bf16 v[112:115], v[152:155], a[236:239], v[112:115]\n"
    "s_add_u32 s68, s81, 193664\n"
    "buffer_load_dwordx4 v185, s[40:43], s68 offen offset:3072 lds\n"
    "v_mfma_f32_16x16x32_bf16 v[112:115], v[156:159], a[252:255], v[112:115]\n"
    "ds_read_b64_tr_b16 v[142:143], v178 offset:61952\n"
    "v_cvt_pk_bf16_f32 v200, v204, v205\n"
    "s_waitcnt lgkmcnt(8)\n"
    "v_mfma_f32_16x16x32_bf16 v[68:71], v[160:163], a[192:195], v[68:71]\n"
    "ds_read_b64_tr_b16 v[144:145], v179 offset:49152\n"
    "v_cvt_pk_bf16_f32 v201, v206, v207\n"
    "v_mfma_f32_16x16x32_bf16 v[68:71], v[164:167], a[208:211], v[68:71]\n"
    "ds_read_b64_tr_b16 v[146:147], v179 offset:49664\n"
    "v_cvt_pk_bf16_f32 v202, v208, v209\n"
    "v_mfma_f32_16x16x32_bf16 v[68:71], v[168:171], a[224:227], v[68:71]\n"
    "ds_read_b64_tr_b16 v[148:149], v179 offset:53248\n"
    "v_cvt_pk_bf16_f32 v203, v210, v211\n"
    "v_mfma_f32_16x16x32_bf16 v[68:71], v[172:175], a[240:243], v[68:71]\n"
    "ds_read_b64_tr_b16 v[150:151], v179 offset:53760\n"
    "v_and_b32 v214, 0x70, v190\n"
    "v_mfma_f32_16x16x32_bf16 v[84:87], v[160:163], a[196:199], v[84:87]\n"
    "ds_read_b64_tr_b16 v[152:153], v179 offset:57344\n"
    "v_lshrrev_b32 v215, 7, v190\n"
    "v_mfma_f32_16x16x32_bf16 v[84:87], v[164:167], a[212:215], v[84:87]\n"
    "ds_read_b64_tr_b16 v[154:155], v179 offset:57856\n"
    "v_lshl_or_b32 v214, v215, 8, v214\n"
    "v_mfma_f32_16x16x32_bf16 v[84:87], v[168:171], a[228:231], v[84:87]\n"
    "ds_read_b64_tr_b16 v[156:157], v179 offset:61440\n"
    "v_mfma_f32_16x16x32_bf16 v[84:87], v[172:175], a[244:247], v[84:87]\n"
    "ds_read_b64_tr_b16 v[158:159], v179 offset:61952\n"
    "v_mfma_f32_16x16x32_bf16 v[100:103], v[160:163], a[200:203], v[100:103]\n"
    "buffer_store_dwordx4 v[196:199], v214, s[76:79], s74 offen offset:0 sc1\n"
    "v_mfma_f32_16x16x32_bf16 v[100:103], v[164:167], a[216:219], v[100:103]\n"
    "buffer_store_dwordx4 v[200:203], v214, s[76:79], s74 offen offset:2048 sc1\n"
    "v_mfma_f32_16x16x32_bf16 v[100:103], v[168:171], a[232:235], v[100:103]\n"
    "v_mov_b32 v188, v248\n"
    "v_mfma_f32_16x16x32_bf16 v[100:103], v[172:175], a[248:251], v[100:103]\n"
    "v_mov_b32 v189, v249\n"
    "v_mfma_f32_16x16x32_bf16 v[116:119], v[160:163], a[204:207], v[116:119]\n"
    "v_mfma_f32_16x16x32_bf16 v[116:119], v[164:167], a[220:223], v[116:119]\n"
    "v_mfma_f32_16x16x32_bf16 v[116:119], v[168:171], a[236:239], v[116:119]\n"
    "v_mfma_f32_16x16x32_bf16 v[116:119], v[172:175], a[252:255], v[116:119]\n"
    "s_waitcnt lgkmcnt(8)\n"
    "s_waitcnt vmcnt(10) lgkmcnt(0)\n"
    "s_barrier\n"
    "v_mfma_f32_16x16x32_bf16 v[72:75], v[128:131], a[192:195], v[72:75]\n"
    "v_mfma_f32_16x16x32_bf16 v[72:75], v[132:135], a[208:211], v[72:75]\n"
    "s_add_u32 m0, s57, 32768\n"
    "ds_read_b64_tr_b16 v[160:161], v180 offset:0\n"
    "v_mfma_f32_16x16x32_bf16 v[72:75], v[136:139], a[224:227], v[72:75]\n"
    "s_add_u32 s68, s82, 128\n"
    "buffer_load_dwordx4 v184, s[40:43], s68 offen offset:0 lds\n"
    "v_mfma_f32_16x16x32_bf16 v[72:75], v[140:143], a[240:243], v[72:75]\n"
    "ds_read_b64_tr_b16 v[162:163], v180 offset:512\n"
    "v_mfma_f32_16x16x32_bf16 v[88:91], v[128:131], a[196:199], v[88:91]\n"
    "v_mfma_f32_16x16x32_bf16 v[88:91], v[132:135], a[212:215], v[88:91]\n"
    "ds_read_b64_tr_b16 v[164:165], v180 offset:4096\n"
    "v_mfma_f32_16x16x32_bf16 v[88:91], v[136:139], a[228:231], v[88:91]\n"
    "s_add_u32 s68, s82, 64640\n"
    "buffer_load_dwordx4 v185, s[40:43], s68 offen offset:1024 lds\n"
    "v_mfma_f32_16x16x32_bf16 v[88:91], v[140:143], a[244:247], v[88:91]\n"
    "ds_read_b64_tr_b16 v[166:167], v180 offset:4608\n"
    "v_mfma_f32_16x16x32_bf16 v[104:107], v[128:131], a[200:203], v[104:107]\n"
    "v_mfma_f32_16x16x32_bf16 v[104:107], v[132:135], a[216:219], v[104:107]\n"
    "ds_read_b64_tr_b16 v[168:169], v180 offset:8192\n"
    "v_mfma_f32_16x16x32_bf16 v[104:107], v[136:139], a[232:235], v[104:107]\n"
    "s_add_u32 s68, s82, 129152\n"
    "buffer_load_dwordx4 v184, s[40:43], s68 offen offset:2048 lds\n"
    "v_mfma_f32_16x16x32_bf16 v[104:107], v[140:143], a[248:251], v[104:107]\n"
    "ds_read_b64_tr_b16 v[170:171], v180 offset:8704\n"
    "v_mfma_f32_16x16x32_bf16 v[120:123], v[128:131], a[204:207], v[120:123]\n"
    "v_mfma_f32_16x16x32_bf16 v[120:123], v[132:135], a[220:223], v[120:123]\n"
    "ds_read_b64_tr_b16 v[172:173], v180 offset:12288\n"
    "v_mfma_f32_16x16x32_bf16 v[120:123], v[136:139], a[236:239], v[120:123]\n"
    "s_add_u32 s68, s82, 193664\n"
    "buffer_load_dwordx4 v185, s[40:43], s68 offen offset:3072 lds\n"
    "v_mfma_f32_16x16x32_bf16 v[120:123], v[140:143], a[252:255], v[120:123]\n"
    "ds_read_b64_tr_b16 v[174:175], v180 offset:12800\n"
    "v_mfma_f32_16x16x32_bf16 v[76:79], v[144:147], a[192:195], v[76:79]\n"
    "v_mfma_f32_16x16x32_bf16 v[76:79], v[148:151], a[208:211], v[76:79]\n"
    "s_add_u32 m0, s57, 49152\n"
    "ds_read_b64_tr_b16 v[128:129], v181 offset:0\n"
    "v_mfma_f32_16x16x32_bf16 v[76:79], v[152:155], a[224:227], v[76:79]\n"
    "s_add_u32 s68, s83, 128\n"
    "buffer_load_dwordx4 v184, s[40:43], s68 offen offset:0 lds\n"
    "v_mfma_f32_16x16x32_bf16 v[76:79], v[156:159], a[240:243], v[76:79]\n"
    "ds_read_b64_tr_b16 v[130:131], v181 offset:512\n"
    "v_mfma_f32_16x16x32_bf16 v[92:95], v[144:147], a[196:199], v[92:95]\n"
    "v_mfma_f32_16x16x32_bf16 v[92:95], v[148:151], a[212:215], v[92:95]\n"
    "ds_read_b64_tr_b16 v[132:133], v181 offset:4096\n"
    "v_mfma_f32_16x16x32_bf16 v[92:95], v[152:155], a[228:231], v[92:95]\n"
    "s_add_u32 s68, s83, 64640\n"
    "buffer_load_dwordx4 v185, s[40:43], s68 offen offset:1024 lds\n"
    "v_mfma_f32_16x16x32_bf16 v[92:95], v[156:159], a[244:247], v[92:95]\n"
    "ds_read_b64_tr_b16 v[134:135], v181 offset:4608\n"
    "v_mfma_f32_16x16x32_bf16 v[108:111], v[144:147], a[200:203], v[108:111]\n"
    "v_mfma_f32_16x16x32_bf16 v[108:111], v[148:151], a[216:219], v[108:111]\n"
    "ds_read_b64_tr_b16 v[136:137], v181 offset:8192\n"
    "v_mfma_f32_16x16x32_bf16 v[108:111], v[152:155], a[232:235], v[108:111]\n"
    "s_add_u32 s68, s83, 129152\n"
    "buffer_load_dwordx4 v184, s[40:43], s68 offen offset:2048 lds\n"
    "v_mfma_f32_16x16x32_bf16 v[108:111], v[156:159], a[248:251], v[108:111]\n"
    "ds_read_b64_tr_b16 v[138:139], v181 offset:8704\n"
    "v_mfma_f32_16x16x32_bf16 v[124:127], v[144:147], a[204:207], v[124:127]\n"
    "v_mfma_f32_16x16x32_bf16 v[124:127], v[148:151], a[220:223], v[124:127]\n"
    "ds_read_b64_tr_b16 v[140:141], v181 offset:12288\n"
    "v_mfma_f32_16x16x32_bf16 v[124:127], v[152:155], a[236:239], v[124:127]\n"
    "s_add_u32 s68, s83, 193664\n"
    "buffer_load_dwordx4 v185, s[40:43], s68 offen offset:3072 lds\n"
    "v_mfma_f32_16x16x32_bf16 v[124:127], v[156:159], a[252:255], v[124:127]\n"
    "ds_read_b64_tr_b16 v[142:143], v181 offset:12800\n"
    "s_waitcnt lgkmcnt(8)\n"
    "v_mfma_f32_16x16x32_bf16 v[192:195], a[0:3], v[160:163], 0\n"
    "v_mfma_f32_16x16x32_bf16 v[192:195], a[16:19], v[164:167], v[192:195]\n"
    "ds_read_b64_tr_b16 v[144:145], v182 offset:0\n"
    "v_cvt_pk_bf16_f32 v0, v0, v1\n"
    "v_mfma_f32_16x16x32_bf16 v[192:195], a[32:35], v[168:171], v[192:195]\n"
    "v_mfma_f32_16x16x32_bf16 v[192:195], a[48:51], v[172:175], v[192:195]\n"
    "ds_read_b64_tr_b16 v[146:147], v182 offset:512\n"
    "v_cvt_pk_bf16_f32 v1, v2, v3\n"
    "v_mfma_f32_16x16x32_bf16 v[208:211], a[4:7], v[160:163], 0\n"
    "v_mfma_f32_16x16x32_bf16 v[208:211], a[20:23], v[164:167], v[208:211]\n"
    "ds_read_b64_tr_b16 v[148:149], v182 offset:4096\n"
    "v_cvt_pk_bf16_f32 v2, v4, v5\n"
    "v_mfma_f32_16x16x32_bf16 v[208:211], a[36:39], v[168:171], v[208:211]\n"
    "v_mfma_f32_16x16x32_bf16 v[208:211], a[52:55], v[172:175], v[208:211]\n"
    "ds_read_b64_tr_b16 v[150:151], v182 offset:4608\n"
    "v_cvt_pk_bf16_f32 v3, v6, v7\n"
    "v_mfma_f32_16x16x32_bf16 v[224:227], a[8:11], v[160:163], 0\n"
    "s_add_u32 s69, s58, 0\n"
    "buffer_store_dwordx4 v[0:3], v190, s[44:47], s69 offen sc1\n"
    "v_mfma_f32_16x16x32_bf16 v[224:227], a[24:27], v[164:167], v[224:227]\n"
    "ds_read_b64_tr_b16 v[152:153], v182 offset:8192\n"
    "v_mfma_f32_16x16x32_bf16 v[224:227], a[40:43], v[168:171], v[224:227]\n"
    "v_mfma_f32_16x16x32_bf16 v[224:227], a[56:59], v[172:175], v[224:227]\n"
    "ds_read_b64_tr_b16 v[154:155], v182 offset:8704\n"
    "v_mfma_f32_16x16x32_bf16 v[240:243], a[12:15], v[160:163], 0\n"
    "v_mfma_f32_16x16x32_bf16 v[240:243], a[28:31], v[164:167], v[240:243]\n"
    "ds_read_b64_tr_b16 v[156:157], v182 offset:12288\n"
    "v_mfma_f32_16x16x32_bf16 v[240:243], a[44:47], v[168:171], v[240:243]\n"
    "v_mfma_f32_16x16x32_bf16 v[240:243], a[60:63], v[172:175], v[240:243]\n"
    "ds_read_b64_tr_b16 v[158:159], v182 offset:12800\n"
    "v_cvt_pk_bf16_f32 v16, v16, v17\n"
    "s_waitcnt lgkmcnt(8)\n"
    "v_mfma_f32_16x16x32_bf16 v[196:199], a[0:3], v[128:131], 0\n"
    "v_mfma_f32_16x16x32_bf16 v[196:199], a[16:19], v[132:135], v[196:199]\n"
    "ds_read_b64_tr_b16 v[160:161], v183 offset:0\n"
    "v_cvt_pk_bf16_f32 v17, v18, v19\n"
    "v_mfma_f32_16x16x32_bf16 v[196:199], a[32:35], v[136:139], v[196:199]\n"
    "v_mfma_f32_16x16x32_bf16 v[196:199], a[48:51], v[140:143], v[196:199]\n"
    "ds_read_b64_tr_b16 v[162:163], v183 offset:512\n"
    "v_cvt_pk_bf16_f32 v18, v20, v21\n"
    "v_mfma_f32_16x16x32_bf16 v[212:215], a[4:7], v[128:131], 0\n"
    "v_mfma_f32_16x16x32_bf16 v[212:215], a[20:23], v[132:135], v[212:215]\n"
    "ds_read_b64_tr_b16 v[164:165], v183 offset:4096\n"
    "v_cvt_pk_bf16_f32 v19, v22, v23\n"
    "v_mfma_f32_16x16x32_bf16 v[212:215], a[36:39], v[136:139], v[212:215]\n"
    "s_add_u32 s69, s58, 4096\n"
    "buffer_store_dwordx4 v[16:19], v190, s[44:47], s69 offen sc1\n"
    "v_mfma_f32_16x16x32_bf16 v[212:215], a[52:55], v[140:143], v[212:215]\n"
    "ds_read_b64_tr_b16 v[166:167], v183 offset:4608\n"
    "v_mfma_f32_16x16x32_bf16 v[228:231], a[8:11], v[128:131], 0\n"
    "v_mfma_f32_16x16x32_bf16 v[228:231], a[24:27], v[132:135], v[228:231]\n"
    "ds_read_b64_tr_b16 v[168:169], v183 offset:8192\n"
    "v_mfma_f32_16x16x32_bf16 v[228:231], a[40:43], v[136:139], v[228:231]\n"
    "v_mfma_f32_16x16x32_bf16 v[228:231], a[56:59], v[140:143], v[228:231]\n"
    "ds_read_b64_tr_b16 v[170:171], v183 offset:8704\n"
    "v_mfma_f32_16x16x32_bf16 v[244:247], a[12:15], v[128:131], 0\n"
    "v_mfma_f32_16x16x32_bf16 v[244:247], a[28:31], v[132:135], v[244:247]\n"
    "ds_read_b64_tr_b16 v[172:173], v183 offset:12288\n"
    "v_cvt_pk_bf16_f32 v32, v32, v33\n"
    "v_mfma_f32_16x16x32_bf16 v[244:247], a[44:47], v[136:139], v[244:247]\n"
    "v_mfma_f32_16x16x32_bf16 v[244:247], a[60:63], v[140:143], v[244:247]\n"
    "ds_read_b64_tr_b16 v[174:175], v183 offset:12800\n"
    "v_cvt_pk_bf16_f32 v33, v34, v35\n"
    "s_waitcnt lgkmcnt(8)\n"
    "v_mfma_f32_16x16x32_bf16 v[200:203], a[0:3], v[144:147], 0\n"
    "v_mfma_f32_16x16x32_bf16 v[200:203], a[16:19], v[148:151], v[200:203]\n"
    "ds_read_b64_tr_b16 v[128:129], v180 offset:16384\n"
    "v_cvt_pk_bf16_f32 v34, v36, v37\n"
    "v_mfma_f32_16x16x32_bf16 v[200:203], a[32:35], v[152:155], v[200:203]\n"
    "s_waitcnt vmcnt(10)\n"
    "buffer_store_dword v189, v188, s[76:79], s75 offen sc1\n"
    "v_mfma_f32_16x16x32_bf16 v[200:203], a[48:51], v[156:159], v[200:203]\n"
    "ds_read_b64_tr_b16 v[130:131], v180 offset:16896\n"
    "v_cvt_pk_bf16_f32 v35, v38, v39\n"
    "v_mfma_f32_16x16x32_bf16 v[216:219], a[4:7], v[144:147], 0\n"
    "s_add_u32 s69, s58, 8192\n"
    "buffer_store_dwordx4 v[32:35], v190, s[44:47], s69 offen sc1\n"
    "v_mfma_f32_16x16x32_bf16 v[216:219], a[20:23], v[148:151], v[216:219]\n"
    "ds_read_b64_tr_b16 v[132:133], v180 offset:20480\n"
    "v_mfma_f32_16x16x32_bf16 v[216:219], a[36:39], v[152:155], v[216:219]\n"
    "s_sub_u32 s74, s66, s64\n"
    "v_mfma_f32_16x16x32_bf16 v[216:219], a[52:55], v[156:159], v[216:219]\n"
    "s_lshr_b32 s74, s74, 7\n"
    "ds_read_b64_tr_b16 v[134:135], v180 offset:20992\n"
    "v_mfma_f32_16x16x32_bf16 v[232:235], a[8:11], v[144:147], 0\n"
    "v_bfe_u32 v186, v190, 8, 1\n"
    "v_mfma_f32_16x16x32_bf16 v[232:235], a[24:27], v[148:151], v[232:235]\n"
    "ds_read_b64_tr_b16 v[136:137], v180 offset:24576\n"
    "v_mul_u32_u24 v186, s74, v186\n"
    "v_mfma_f32_16x16x32_bf16 v[232:235], a[40:43], v[152:155], v[232:235]\n"
    "v_bfe_u32 v187, v190, 2, 5\n"
    "v_mfma_f32_16x16x32_bf16 v[232:235], a[56:59], v[156:159], v[232:235]\n"
    "ds_read_b64_tr_b16 v[138:139], v180 offset:25088\n"
    "v_cvt_pk_bf16_f32 v48, v48, v49\n"
    "v_mfma_f32_16x16x32_bf16 v[248:251], a[12:15], v[144:147], 0\n"
    "v_add_u32 v186, v186, v187\n"
    "v_mfma_f32_16x16x32_bf16 v[248:251], a[28:31], v[148:151], v[248:251]\n"
    "ds_read_b64_tr_b16 v[140:141], v180 offset:28672\n"
    "v_cvt_pk_bf16_f32 v49, v50, v51\n"
    "v_mfma_f32_16x16x32_bf16 v[248:251], a[44:47], v[152:155], v[248:251]\n"
    "v_and_b32 v187, 0x80, v190\n"
    "v_mfma_f32_16x16x32_bf16 v[248:251], a[60:63], v[156:159], v[248:251]\n"
    "ds_read_b64_tr_b16 v[142:143], v180 offset:29184\n"
    "v_cvt_pk_bf16_f32 v50, v52, v53\n"
    "s_waitcnt lgkmcnt(8)\n"
    "v_mfma_f32_16x16x32_bf16 v[204:207], a[0:3], v[160:163], 0\n"
    "v_add_u32 v186, v186, v187\n"
    "v_mfma_f32_16x16x32_bf16 v[204:207], a[16:19], v[164:167], v[204:207]\n"
    "s_lshr_b32 s74, s64, 7\n"
    "ds_read_b64_tr_b16 v[144:145], v181 offset:16384\n"
    "v_cvt_pk_bf16_f32 v51, v54, v55\n"
    "v_mfma_f32_16x16x32_bf16 v[204:207], a[32:35], v[168:171], v[204:207]\n"
    "s_add_u32 s69, s58, 12288\n"
    "s_add_u32 s74, s74, 0x1000\n"
    "buffer_store_dwordx4 v[48:51], v190, s[44:47], s69 offen sc1\n"
    "v_mfma_f32_16x16x32_bf16 v[204:207], a[48:51], v[172:175], v[204:207]\n"
    "ds_read_b64_tr_b16 v[146:147], v181 offset:16896\n"
    "v_lshlrev_b32 v186, 5, v186\n"
    "v_mfma_f32_16x16x32_bf16 v[220:223], a[4:7], v[160:163], 0\n"
    "s_lshl_b32 s74, s74, 5\n"
    "v_mfma_f32_16x16x32_bf16 v[220:223], a[20:23], v[164:167], v[220:223]\n"
    "s_add_u32 s74, s74, 0x5800000\n"
    "ds_read_b64_tr_b16 v[148:149], v181 offset:20480\n"
    "v_mfma_f32_16x16x32_bf16 v[220:223], a[36:39], v[168:171], v[220:223]\n"
    "v_mfma_f32_16x16x32_bf16 v[220:223], a[52:55], v[172:175], v[220:223]\n"
    "ds_read_b64_tr_b16 v[150:151], v181 offset:20992\n"
    "v_mfma_f32_16x16x32_bf16 v[236:239], a[8:11], v[160:163], 0\n"
    "v_mfma_f32_16x16x32_bf16 v[236:239], a[24:27], v[164:167], v[236:239]\n"
    "ds_read_b64_tr_b16 v[152:153], v181 offset:24576\n"
    "v_cvt_pk_bf16_f32 v8, v8, v9\n"
    "v_mfma_f32_16x16x32_bf16 v[236:239], a[40:43], v[168:171], v[236:239]\n"
    "v_mfma_f32_16x16x32_bf16 v[236:239], a[56:59], v[172:175], v[236:239]\n"
    "ds_read_b64_tr_b16 v[154:155], v181 offset:25088\n"
    "v_cvt_pk_bf16_f32 v9, v10, v11\n"
    "v_mfma_f32_16x16x32_bf16 v[252:255], a[12:15], v[160:163], 0\n"
    "v_mfma_f32_16x16x32_bf16 v[252:255], a[28:31], v[164:167], v[252:255]\n"
    "ds_read_b64_tr_b16 v[156:157], v181 offset:28672\n"
    "v_cvt_pk_bf16_f32 v10, v12, v13\n"
    "v_mfma_f32_16x16x32_bf16 v[252:255], a[44:47], v[168:171], v[252:255]\n"
    "v_mfma_f32_16x16x32_bf16 v[252:255], a[60:63], v[172:175], v[252:255]\n"
    "ds_read_b64_tr_b16 v[158:159], v181 offset:29184\n"
    "v_cvt_pk_bf16_f32 v11, v14, v15\n"
    "s_waitcnt lgkmcnt(8)\n"
    "v_mfma_f32_16x16x32_bf16 v[192:195], a[64:67], v[128:131], v[192:195]\n"
    "s_add_u32 s69, s58, 1024\n"
    "buffer_store_dwordx4 v[8:11], v190, s[44:47], s69 offen sc1\n"
    "v_mfma_f32_16x16x32_bf16 v[192:195], a[80:83], v[132:135], v[192:195]\n"
    "ds_read_b64_tr_b16 v[160:161], v182 offset:16384\n"
    "v_mfma_f32_16x16x32_bf16 v[192:195], a[96:99], v[136:139], v[192:195]\n"
    "v_mfma_f32_16x16x32_bf16 v[192:195], a[112:115], v[140:143], v[192:195]\n"
    "ds_read_b64_tr_b16 v[162:163], v182 offset:16896\n"
    "v_mfma_f32_16x16x32_bf16 v[208:211], a[68:71], v[128:131], v[208:211]\n"
    "v_mfma_f32_16x16x32_bf16 v[208:211], a[84:87], v[132:135], v[208:211]\n"
    "ds_read_b64_tr_b16 v[164:165], v182 offset:20480\n"
    "v_mfma_f32_16x16x32_bf16 v[208:211], a[100:103], v[136:139], v[208:211]\n"
    "v_mfma_f32_16x16x32_bf16 v[208:211], a[116:119], v[140:143], v[208:211]\n"
    "ds_read_b64_tr_b16 v[166:167], v182 offset:20992\n"
    "v_cvt_pk_bf16_f32 v24, v24, v25\n"
    "v_mfma_f32_16x16x32_bf16 v[224:227], a[72:75], v[128:131], v[224:227]\n"
    "v_mfma_f32_16x16x32_bf16 v[224:227], a[88:91], v[132:135], v[224:227]\n"
    "ds_read_b64_tr_b16 v[168:169], v182 offset:24576\n"
    "v_cvt_pk_bf16_f32 v25, v26, v27\n"
    "v_mfma_f32_16x16x32_bf16 v[224:227], a[104:107], v[136:139], v[224:227]\n"
    "v_mfma_f32_16x16x32_bf16 v[224:227], a[120:123], v[140:143], v[224:227]\n"
    "ds_read_b64_tr_b16 v[170:171], v182 offset:25088\n"
    "v_cvt_pk_bf16_f32 v26, v28, v29\n"
    "v_mfma_f32_16x16x32_bf16 v[240:243], a[76:79], v[128:131], v[240:243]\n"
    "v_mfma_f32_16x16x32_bf16 v[240:243], a[92:95], v[132:135], v[240:243]\n"
    "ds_read_b64_tr_b16 v[172:173], v182 offset:28672\n"
    "v_cvt_pk_bf16_f32 v27, v30, v31\n"
    "v_mfma_f32_16x16x32_bf16 v[240:243], a[108:111], v[136:139], v[240:243]\n"
    "s_add_u32 s69, s58, 5120\n"
    "buffer_store_dwordx4 v[24:27], v190, s[44:47], s69 offen sc1\n"
    "v_mfma_f32_16x16x32_bf16 v[240:243], a[124:127], v[140:143], v[240:243]\n"
    "ds_read_b64_tr_b16 v[174:175], v182 offset:29184\n"
    "s_waitcnt lgkmcnt(8)\n"
    "v_mfma_f32_16x16x32_bf16 v[196:199], a[64:67], v[144:147], v[196:199]\n"
    "v_mfma_f32_16x16x32_bf16 v[196:199], a[80:83], v[148:151], v[196:199]\n"
    "ds_read_b64_tr_b16 v[128:129], v183 offset:16384\n"
    "v_mfma_f32_16x16x32_bf16 v[196:199], a[96:99], v[152:155], v[196:199]\n"
    "v_mfma_f32_16x16x32_bf16 v[196:199], a[112:115], v[156:159], v[196:199]\n"
    "ds_read_b64_tr_b16 v[130:131], v183 offset:16896\n"
    "v_mfma_f32_16x16x32_bf16 v[212:215], a[68:71], v[144:147], v[212:215]\n"
    "v_mfma_f32_16x16x32_bf16 v[212:215], a[84:87], v[148:151], v[212:215]\n"
    "ds_read_b64_tr_b16 v[132:133], v183 offset:20480\n"
    "v_cvt_pk_bf16_f32 v40, v40, v41\n"
    "v_mfma_f32_16x16x32_bf16 v[212:215], a[100:103], v[152:155], v[212:215]\n"
    "v_mfma_f32_16x16x32_bf16 v[212:215], a[116:119], v[156:159], v[212:215]\n"
    "ds_read_b64_tr_b16 v[134:135], v183 offset:20992\n"
    "v_cvt_pk_bf16_f32 v41, v42, v43\n"
    "v_mfma_f32_16x16x32_bf16 v[228:231], a[72:75], v[144:147], v[228:231]\n"
    "v_mfma_f32_16x16x32_bf16 v[228:231], a[88:91], v[148:151], v[228:231]\n"
    "ds_read_b64_tr_b16 v[136:137], v183 offset:24576\n"
    "v_cvt_pk_bf16_f32 v42, v44, v45\n"
    "v_mfma_f32_16x16x32_bf16 v[228:231], a[104:107], v[152:155], v[228:231]\n"
    "v_mfma_f32_16x16x32_bf16 v[228:231], a[120:123], v[156:159], v[228:231]\n"
    "ds_read_b64_tr_b16 v[138:139], v183 offset:25088\n"
    "v_cvt_pk_bf16_f32 v43, v46, v47\n"
    "v_mfma_f32_16x16x32_bf16 v[244:247], a[76:79], v[144:147], v[244:247]\n"
    "s_add_u32 s69, s58, 9216\n"
    "buffer_store_dwordx4 v[40:43], v190, s[44:47], s69 offen sc1\n"
    "v_mfma_f32_16x16x32_bf16 v[244:247], a[92:95], v[148:151], v[244:247]\n"
    "ds_read_b64_tr_b16 v[140:141], v183 offset:28672\n"
    "v_mfma_f32_16x16x32_bf16 v[244:247], a[108:111], v[152:155], v[244:247]\n"
    "v_mfma_f32_16x16x32_bf16 v[244:247], a[124:127], v[156:159], v[244:247]\n"
    "ds_read_b64_tr_b16 v[142:143], v183 offset:29184\n"
    "s_waitcnt lgkmcnt(8)\n"
    "v_mfma_f32_16x16x32_bf16 v[200:203], a[64:67], v[160:163], v[200:203]\n"
    "v_mfma_f32_16x16x32_bf16 v[200:203], a[80:83], v[164:167], v[200:203]\n"
    "ds_read_b64_tr_b16 v[144:145], v180 offset:32768\n"
    "v_mfma_f32_16x16x32_bf16 v[200:203], a[96:99], v[168:171], v[200:203]\n"
    "v_mfma_f32_16x16x32_bf16 v[200:203], a[112:115], v[172:175], v[200:203]\n"
    "ds_read_b64_tr_b16 v[146:147], v180 offset:33280\n"
    "v_cvt_pk_bf16_f32 v56, v56, v57\n"
    "v_mfma_f32_16x16x32_bf16 v[216:219], a[68:71], v[160:163], v[216:219]\n"
    "v_mfma_f32_16x16x32_bf16 v[216:219], a[84:87], v[164:167], v[216:219]\n"
    "ds_read_b64_tr_b16 v[148:149], v180 offset:36864\n"
    "v_cvt_pk_bf16_f32 v57, v58, v59\n"
    "v_mfma_f32_16x16x32_bf16 v[216:219], a[100:103], v[168:171], v[216:219]\n"
    "v_mfma_f32_16x16x32_bf16 v[216:219], a[116:119], v[172:175], v[216:219]\n"
    "ds_read_b64_tr_b16 v[150:151], v180 offset:37376\n"
    "v_cvt_pk_bf16_f32 v58, v60, v61\n"
    "v_mfma_f32_16x16x32_bf16 v[232:235], a[72:75], v[160:163], v[232:235]\n"
    "v_mfma_f32_16x16x32_bf16 v[232:235], a[88:91], v[164:167], v[232:235]\n"
    "ds_read_b64_tr_b16 v[152:153], v180 offset:40960\n"
    "v_cvt_pk_bf16_f32 v59, v62, v63\n"
    "v_mfma_f32_16x16x32_bf16 v[232:235], a[104:107], v[168:171], v[232:235]\n"
    "s_add_u32 s69, s58, 13312\n"
    "buffer_store_dwordx4 v[56:59], v190, s[44:47], s69 offen sc1\n"
    "v_mfma_f32_16x16x32_bf16 v[232:235], a[120:123], v[172:175], v[232:235]\n"
    "ds_read_b64_tr_b16 v[154:155], v180 offset:41472\n"
    "v_mfma_f32_16x16x32_bf16 v[248:251], a[76:79], v[160:163], v[248:251]\n"
    "v_mfma_f32_16x16x32_bf16 v[248:251], a[92:95], v[164:167], v[248:251]\n"
    "ds_read_b64_tr_b16 v[156:157], v180 offset:45056\n"
    "v_mfma_f32_16x16x32_bf16 v[248:251], a[108:111], v[168:171], v[248:251]\n"
    "v_mfma_f32_16x16x32_bf16 v[248:251], a[124:127], v[172:175], v[248:251]\n"
    "ds_read_b64_tr_b16 v[158:159], v180 offset:45568\n"
    "s_waitcnt lgkmcnt(8)\n"
    "v_mfma_f32_16x16x32_bf16 v[204:207], a[64:67], v[128:131], v[204:207]\n"
    "v_mfma_f32_16x16x32_bf16 v[204:207], a[80:83], v[132:135], v[204:207]\n"
    "ds_read_b64_tr_b16 v[160:161], v181 offset:32768\n"
    "v_cvt_pk_bf16_f32 v64, v64, v65\n"
    "v_mfma_f32_16x16x32_bf16 v[204:207], a[96:99], v[136:139], v[204:207]\n"
    "v_mfma_f32_16x16x32_bf16 v[204:207], a[112:115], v[140:143], v[204:207]\n"
    "ds_read_b64_tr_b16 v[162:163], v181 offset:33280\n"
    "v_cvt_pk_bf16_f32 v65, v66, v67\n"
    "v_mfma_f32_16x16x32_bf16 v[220:223], a[68:71], v[128:131], v[220:223]\n"
    "v_mfma_f32_16x16x32_bf16 v[220:223], a[84:87], v[132:135], v[220:223]\n"
    "ds_read_b64_tr_b16 v[164:165], v181 offset:36864\n"
    "v_cvt_pk_bf16_f32 v66, v68, v69\n"
    "v_mfma_f32_16x16x32_bf16 v[220:223], a[100:103], v[136:139], v[220:223]\n"
    "v_mfma_f32_16x16x32_bf16 v[220:223], a[116:119], v[140:143], v[220:223]\n"
    "ds_read_b64_tr_b16 v[166:167], v181 offset:37376\n"
    "v_cvt_pk_bf16_f32 v67, v70, v71\n"
    "v_mfma_f32_16x16x32_bf16 v[236:239], a[72:75], v[128:131], v[236:239]\n"
    "s_add_u32 s69, s58, 2048\n"
    "buffer_store_dwordx4 v[64:67], v190, s[44:47], s69 offen sc1\n"
    "v_mfma_f32_16x16x32_bf16 v[236:239], a[88:91], v[132:135], v[236:239]\n"
    "ds_read_b64_tr_b16 v[168:169], v181 offset:40960\n"
    "v_mfma_f32_16x16x32_bf16 v[236:239], a[104:107], v[136:139], v[236:239]\n"
    "v_mfma_f32_16x16x32_bf16 v[236:239], a[120:123], v[140:143], v[236:239]\n"
    "ds_read_b64_tr_b16 v[170:171], v181 offset:41472\n"
    "v_mfma_f32_16x16x32_bf16 v[252:255], a[76:79], v[128:131], v[252:255]\n"
    "v_mfma_f32_16x16x32_bf16 v[252:255], a[92:95], v[132:135], v[252:255]\n"
    "ds_read_b64_tr_b16 v[172:173], v181 offset:45056\n"
    "v_mfma_f32_16x16x32_bf16 v[252:255], a[108:111], v[136:139], v[252:255]\n"
    "v_mfma_f32_16x16x32_bf16 v[252:255], a[124:127], v[140:143], v[252:255]\n"
    "ds_read_b64_tr_b16 v[174:175], v181 offset:45568\n"
    "v_cvt_pk_bf16_f32 v80, v80, v81\n"
    "s_waitcnt lgkmcnt(8)\n"
    "v_mfma_f32_16x16x32_bf16 v[192:195], a[128:131], v[144:147], v[192:195]\n"
    "v_mfma_f32_16x16x32_bf16 v[192:195], a[144:147], v[148:151], v[192:195]\n"
    "ds_read_b64_tr_b16 v[128:129], v182 offset:32768\n"
    "v_cvt_pk_bf16_f32 v81, v82, v83\n"
    "v_mfma_f32_16x16x32_bf16 v[192:195], a[160:163], v[152:155], v[192:195]\n"
    "v_mfma_f32_16x16x32_bf16 v[192:195], a[176:179], v[156:159], v[192:195]\n"
    "ds_read_b64_tr_b16 v[130:131], v182 offset:33280\n"
    "v_cvt_pk_bf16_f32 v82, v84, v85\n"
    "v_mfma_f32_16x16x32_bf16 v[208:211], a[132:135], v[144:147], v[208:211]\n"
    "v_mfma_f32_16x16x32_bf16 v[208:211], a[148:151], v[148:151], v[208:211]\n"
    "ds_read_b64_tr_b16 v[132:133], v182 offset:36864\n"
    "v_cvt_pk_bf16_f32 v83, v86, v87\n"
    "v_mfma_f32_16x16x32_bf16 v[208:211], a[164:167], v[152:155], v[208:211]\n"
    "s_add_u32 s69, s58, 6144\n"
    "buffer_store_dwordx4 v[80:83], v190, s[44:47], s69 offen sc1\n"
    "v_mfma_f32_16x16x32_bf16 v[208:211], a[180:183], v[156:159], v[208:211]\n"
    "ds_read_b64_tr_b16 v[134:135], v182 offset:37376\n"
    "v_mfma_f32_16x16x32_bf16 v[224:227], a[136:139], v[144:147], v[224:227]\n"
    "v_mfma_f32_16x16x32_bf16 v[224:227], a[152:155], v[148:151], v[224:227]\n"
    "ds_read_b64_tr_b16 v[136:137], v182 offset:40960\n"
    "v_mfma_f32_16x16x32_bf16 v[224:227], a[168:171], v[152:155], v[224:227]\n"
    "v_mfma_f32_16x16x32_bf16 v[224:227], a[184:187], v[156:159], v[224:227]\n"
    "ds_read_b64_tr_b16 v[138:139], v182 offset:41472\n"
    "v_mfma_f32_16x16x32_bf16 v[240:243], a[140:143], v[144:147], v[240:243]\n"
    "v_mfma_f32_16x16x32_bf16 v[240:243], a[156:159], v[148:151], v[240:243]\n"
    "ds_read_b64_tr_b16 v[140:141], v182 offset:45056\n"
    "v_cvt_pk_bf16_f32 v96, v96, v97\n"
    "v_mfma_f32_16x16x32_bf16 v[240:243], a[172:175], v[152:155], v[240:243]\n"
    "v_mfma_f32_16x16x32_bf16 v[240:243], a[188:191], v[156:159], v[240:243]\n"
    "ds_read_b64_tr_b16 v[142:143], v182 offset:45568\n"
    "v_cvt_pk_bf16_f32 v97, v98, v99\n"
    "s_waitcnt lgkmcnt(8)\n"
    "v_mfma_f32_16x16x32_bf16 v[196:199], a[128:131], v[160:163], v[196:199]\n"
    "v_mfma_f32_16x16x32_bf16 v[196:199], a[144:147], v[164:167], v[196:199]\n"
    "ds_read_b64_tr_b16 v[144:145], v183 offset:32768\n"
    "v_cvt_pk_bf16_f32 v98, v100, v101\n"
    "v_mfma_f32_16x16x32_bf16 v[196:199], a[160:163], v[168:171], v[196:199]\n"
    "v_mfma_f32_16x16x32_bf16 v[196:199], a[176:179], v[172:175], v[196:199]\n"
    "ds_read_b64_tr_b16 v[146:147], v183 offset:33280\n"
    "v_cvt_pk_bf16_f32 v99, v102, v103\n"
    "v_mfma_f32_16x16x32_bf16 v[212:215], a[132:135], v[160:163], v[212:215]\n"
    "s_add_u32 s69, s58, 10240\n"
    "buffer_store_dwordx4 v[96:99], v190, s[44:47], s69 offen sc1\n"
    "v_mfma_f32_16x16x32_bf16 v[212:215], a[148:151], v[164:167], v[212:215]\n"
    "ds_read_b64_tr_b16 v[148:149], v183 offset:36864\n"
    "v_mfma_f32_16x16x32_bf16 v[212:215], a[164:167], v[168:171], v[212:215]\n"
    "v_mfma_f32_16x16x32_bf16 v[212:215], a[180:183], v[172:175], v[212:215]\n"
    "ds_read_b64_tr_b16 v[150:151], v183 offset:37376\n"
    "v_mfma_f32_16x16x32_bf16 v[228:231], a[136:139], v[160:163], v[228:231]\n"
    "v_mfma_f32_16x16x32_bf16 v[228:231], a[152:155], v[164:167], v[228:231]\n"
    "ds_read_b64_tr_b16 v[152:153], v183 offset:40960\n"
    "v_mfma_f32_16x16x32_bf16 v[228:231], a[168:171], v[168:171], v[228:231]\n"
    "v_mfma_f32_16x16x32_bf16 v[228:231], a[184:187], v[172:175], v[228:231]\n"
    "ds_read_b64_tr_b16 v[154:155], v183 offset:41472\n"
    "v_cvt_pk_bf16_f32 v112, v112, v113\n"
    "v_mfma_f32_16x16x32_bf16 v[244:247], a[140:143], v[160:163], v[244:247]\n"
    "v_mfma_f32_16x16x32_bf16 v[244:247], a[156:159], v[164:167], v[244:247]\n"
    "ds_read_b64_tr_b16 v[156:157], v183 offset:45056\n"
    "v_cvt_pk_bf16_f32 v113, v114, v115\n"
    "v_mfma_f32_16x16x32_bf16 v[244:247], a[172:175], v[168:171], v[244:247]\n"
    "v_mfma_f32_16x16x32_bf16 v[244:247], a[188:191], v[172:175], v[244:247]\n"
    "ds_read_b64_tr_b16 v[158:159], v183 offset:45568\n"
    "v_cvt_pk_bf16_f32 v114, v116, v117\n"
    "s_waitcnt lgkmcnt(8)\n"
    "v_mfma_f32_16x16x32_bf16 v[200:203], a[128:131], v[128:131], v[200:203]\n"
    "v_mfma_f32_16x16x32_bf16 v[200:203], a[144:147], v[132:135], v[200:203]\n"
    "ds_read_b64_tr_b16 v[160:161], v180 offset:49152\n"
    "v_cvt_pk_bf16_f32 v115, v118, v119\n"
    "v_mfma_f32_16x16x32_bf16 v[200:203], a[160:163], v[136:139], v[200:203]\n"
    "s_add_u32 s69, s58, 14336\n"
    "buffer_store_dwordx4 v[112:115], v190, s[44:47], s69 offen sc1\n"
    "v_mfma_f32_16x16x32_bf16 v[200:203], a[176:179], v[140:143], v[200:203]\n"
    "ds_read_b64_tr_b16 v[162:163], v180 offset:49664\n"
    "v_mfma_f32_16x16x32_bf16 v[216:219], a[132:135], v[128:131], v[216:219]\n"
    "v_mfma_f32_16x16x32_bf16 v[216:219], a[148:151], v[132:135], v[216:219]\n"
    "ds_read_b64_tr_b16 v[164:165], v180 offset:53248\n"
    "v_mfma_f32_16x16x32_bf16 v[216:219], a[164:167], v[136:139], v[216:219]\n"
    "v_mfma_f32_16x16x32_bf16 v[216:219], a[180:183], v[140:143], v[216:219]\n"
    "ds_read_b64_tr_b16 v[166:167], v180 offset:53760\n"
    "L_mid:\n"
    "v_mfma_f32_16x16x32_bf16 v[232:235], a[136:139], v[128:131], v[232:235]\n"
    "v_mfma_f32_16x16x32_bf16 v[232:235], a[152:155], v[132:135], v[232:235]\n"
    "ds_read_b64_tr_b16 v[168:169], v180 offset:57344\n"
    "v_cvt_pk_bf16_f32 v72, v72, v73\n"
    "v_mfma_f32_16x16x32_bf16 v[232:235], a[168:171], v[136:139], v[232:235]\n"
    "v_mfma_f32_16x16x32_bf16 v[232:235], a[184:187], v[140:143], v[232:235]\n"
    "ds_read_b64_tr_b16 v[170:171], v180 offset:57856\n"
    "v_cvt_pk_bf16_f32 v73, v74, v75\n"
    "v_mfma_f32_16x16x32_bf16 v[248:251], a[140:143], v[128:131], v[248:251]\n"
    "v_mfma_f32_16x16x32_bf16 v[248:251], a[156:159], v[132:135], v[248:251]\n"
    "ds_read_b64_tr_b16 v[172:173], v180 offset:61440\n"
    "v_cvt_pk_bf16_f32 v74, v76, v77\n"
    "v_mfma_f32_16x16x32_bf16 v[248:251], a[172:175], v[136:139], v[248:251]\n"
    "v_mfma_f32_16x16x32_bf16 v[248:251], a[188:191], v[140:143], v[248:251]\n"
    "ds_read_b64_tr_b16 v[174:175], v180 offset:61952\n"
    "v_cvt_pk_bf16_f32 v75, v78, v79\n"
    "s_waitcnt lgkmcnt(8)\n"
    "v_mfma_f32_16x16x32_bf16 v[204:207], a[128:131], v[144:147], v[204:207]\n"
    "s_add_u32 s69, s58, 3072\n"
    "buffer_store_dwordx4 v[72:75], v190, s[44:47], s69 offen sc1\n"
    "v_mfma_f32_16x16x32_bf16 v[204:207], a[144:147], v[148:151], v[204:207]\n"
    "ds_read_b64_tr_b16 v[128:129], v181 offset:49152\n"
    "v_mfma_f32_16x16x32_bf16 v[204:207], a[160:163], v[152:155], v[204:207]\n"
    "v_mfma_f32_16x16x32_bf16 v[204:207], a[176:179], v[156:159], v[204:207]\n"
    "ds_read_b64_tr_b16 v[130:131], v181 offset:49664\n"
    "v_mfma_f32_16x16x32_bf16 v[220:223], a[132:135], v[144:147], v[220:223]\n"
    "v_mfma_f32_16x16x32_bf16 v[220:223], a[148:151], v[148:151], v[220:223]\n"
    "ds_read_b64_tr_b16 v[132:133], v181 offset:53248\n"
    "v_mfma_f32_16x16x32_bf16 v[220:223], a[164:167], v[152:155], v[220:223]\n"
    "v_mfma_f32_16x16x32_bf16 v[220:223], a[180:183], v[156:159], v[220:223]\n"
    "ds_read_b64_tr_b16 v[134:135], v181 offset:53760\n"
    "v_cvt_pk_bf16_f32 v88, v88, v89\n"
    "v_mfma_f32_16x16x32_bf16 v[236:239], a[136:139], v[144:147], v[236:239]\n"
    "v_mfma_f32_16x16x32_bf16 v[236:239], a[152:155], v[148:151], v[236:239]\n"
    "ds_read_b64_tr_b16 v[136:137], v181 offset:57344\n"
    "v_cvt_pk_bf16_f32 v89, v90, v91\n"
    "v_mfma_f32_16x16x32_bf16 v[236:239], a[168:171], v[152:155], v[236:239]\n"
    "v_mfma_f32_16x16x32_bf16 v[236:239], a[184:187], v[156:159], v[236:239]\n"
    ".p2align 12\n"
    "L_pad3:\n"
    "s_cbranch_execz L_pad7\n"
    "ds_read_b64_tr_b16 v[138:139], v181 offset:57856\n"
    "v_cvt_pk_bf16_f32 v90, v92, v93\n"
    "v_mfma_f32_16x16x32_bf16 v[252:255], a[140:143], v[144:147], v[252:255]\n"
    "v_mfma_f32_16x16x32_bf16 v[252:255], a[156:159], v[148:151], v[252:255]\n"
    "ds_read_b64_tr_b16 v[140:141], v181 offset:61440\n"
    "v_cvt_pk_bf16_f32 v91, v94, v95\n"
    "v_mfma_f32_16x16x32_bf16 v[252:255], a[172:175], v[152:155], v[252:255]\n"
    "s_add_u32 s69, s58, 7168\n"
    "buffer_store_dwordx4 v[88:91], v190, s[44:47], s69 offen sc1\n"
    "v_mfma_f32_16x16x32_bf16 v[252:255], a[188:191], v[156:159], v[252:255]\n"
    "ds_read_b64_tr_b16 v[142:143], v181 offset:61952\n"
    "s_waitcnt lgkmcnt(8)\n"
    "v_mfma_f32_16x16x32_bf16 v[192:195], a[192:195], v[160:163], v[192:195]\n"
    "v_mfma_f32_16x16x32_bf16 v[192:195], a[208:211], v[164:167], v[192:195]\n"
    "ds_read_b64_tr_b16 v[144:145], v182 offset:49152\n"
    "v_mfma_f32_16x16x32_bf16 v[192:195], a[224:227], v[168:171], v[192:195]\n"
    "v_mfma_f32_16x16x32_bf16 v[192:195], a[240:243], v[172:175], v[192:195]\n"
    "ds_read_b64_tr_b16 v[146:147], v182 offset:49664\n"
    "v_mfma_f32_16x16x32_bf16 v[208:211], a[196:199], v[160:163], v[208:211]\n"
    "v_mfma_f32_16x16x32_bf16 v[208:211], a[212:215], v[164:167], v[208:211]\n"
    "ds_read_b64_tr_b16 v[148:149], v182 offset:53248\n"
    "v_cvt_pk_bf16_f32 v104, v104, v105\n"
    "v_mfma_f32_16x16x32_bf16 v[208:211], a[228:231], v[168:171], v[208:211]\n"
    "v_mfma_f32_16x16x32_bf16 v[208:211], a[244:247], v[172:175], v[208:211]\n"
    "ds_read_b64_tr_b16 v[150:151], v182 offset:53760\n"
    "v_cvt_pk_bf16_f32 v105, v106, v107\n"
    "v_mfma_f32_16x16x32_bf16 v[224:227], a[200:203], v[160:163], v[224:227]\n"
    "v_mfma_f32_16x16x32_bf16 v[224:227], a[216:219], v[164:167], v[224:227]\n"
    "ds_read_b64_tr_b16 v[152:153], v182 offset:57344\n"
    "v_cvt_pk_bf16_f32 v106, v108, v109\n"
    "v_mfma_f32_16x16x32_bf16 v[224:227], a[232:235], v[168:171], v[224:227]\n"
    "v_mfma_f32_16x16x32_bf16 v[224:227], a[248:251], v[172:175], v[224:227]\n"
    "ds_read_b64_tr_b16 v[154:155], v182 offset:57856\n"
    "v_cvt_pk_bf16_f32 v107, v110, v111\n"
    "v_mfma_f32_16x16x32_bf16 v[240:243], a[204:207], v[160:163], v[240:243]\n"
    "s_add_u32 s69, s58, 11264\n"
    "buffer_store_dwordx4 v[104:107], v190, s[44:47], s69 offen sc1\n"
    "v_mfma_f32_16x16x32_bf16 v[240:243], a[220:223], v[164:167], v[240:243]\n"
    "ds_read_b64_tr_b16 v[156:157], v182 offset:61440\n"
    "v_mfma_f32_16x16x32_bf16 v[240:243], a[236:239], v[168:171], v[240:243]\n"
    "v_mfma_f32_16x16x32_bf16 v[240:243], a[252:255], v[172:175], v[240:243]\n"
    "ds_read_b64_tr_b16 v[158:159], v182 offset:61952\n"
    "s_waitcnt lgkmcnt(8)\n"
    "v_mfma_f32_16x16x32_bf16 v[196:199], a[192:195], v[128:131], v[196:199]\n"
    "ds_read_b64_tr_b16 v[160:161], v183 offset:49152\n"
    "v_mfma_f32_16x16x32_bf16 v[196:199], a[208:211], v[132:135], v[196:199]\n"
    "ds_read_b64_tr_b16 v[162:163], v183 offset:49664\n"
    "v_mfma_f32_16x16x32_bf16 v[196:199], a[224:227], v[136:139], v[196:199]\n"
    "ds_read_b64_tr_b16 v[164:165], v183 offset:53248\n"
    "v_mfma_f32_16x16x32_bf16 v[196:199], a[240:243], v[140:143], v[196:199]\n"
    "ds_read_b64_tr_b16 v[166:167], v183 offset:53760\n"
    "v_cvt_pk_bf16_f32 v120, v120, v121\n"
    "v_mfma_f32_16x16x32_bf16 v[212:215], a[196:199], v[128:131], v[212:215]\n"
    "ds_read_b64_tr_b16 v[168:169], v183 offset:57344\n"
    "v_cvt_pk_bf16_f32 v121, v122, v123\n"
    "v_mfma_f32_16x16x32_bf16 v[212:215], a[212:215], v[132:135], v[212:215]\n"
    "ds_read_b64_tr_b16 v[170:171], v183 offset:57856\n"
    "v_cvt_pk_bf16_f32 v122, v124, v125\n"
    "v_mfma_f32_16x16x32_bf16 v[212:215], a[228:231], v[136:139], v[212:215]\n"
    "ds_read_b64_tr_b16 v[172:173], v183 offset:61440\n"
    "v_cvt_pk_bf16_f32 v123, v126, v127\n"
    "v_mfma_f32_16x16x32_bf16 v[212:215], a[244:247], v[140:143], v[212:215]\n"
    "ds_read_b64_tr_b16 v[174:175], v183 offset:61952\n"
    "v_mfma_f32_16x16x32_bf16 v[228:231], a[200:203], v[128:131], v[228:231]\n"
    "s_add_u32 s69, s58, 15360\n"
    "buffer_store_dwordx4 v[120:123], v190, s[44:47], s69 offen sc1\n"
    "v_mfma_f32_16x16x32_bf16 v[228:231], a[216:219], v[132:135], v[228:231]\n"
    "v_mfma_f32_16x16x32_bf16 v[228:231], a[232:235], v[136:139], v[228:231]\n"
    "v_mfma_f32_16x16x32_bf16 v[228:231], a[248:251], v[140:143], v[228:231]\n"
    "v_mfma_f32_16x16x32_bf16 v[244:247], a[204:207], v[128:131], v[244:247]\n"
    "v_mfma_f32_16x16x32_bf16 v[244:247], a[220:223], v[132:135], v[244:247]\n"
    "v_mfma_f32_16x16x32_bf16 v[244:247], a[236:239], v[136:139], v[244:247]\n"
    "v_mfma_f32_16x16x32_bf16 v[244:247], a[252:255], v[140:143], v[244:247]\n"
    "s_mov_b32 s40, s52\n"
    "s_waitcnt lgkmcnt(8)\n"
    "s_waitcnt vmcnt(17) lgkmcnt(0)\n"
    "s_barrier\n"
    "v_mfma_f32_16x16x32_bf16 v[200:203], a[192:195], v[144:147], v[200:203]\n"
    "s_mov_b32 s41, s53\n"
    "v_mfma_f32_16x16x32_bf16 v[200:203], a[208:211], v[148:151], v[200:203]\n"
    "s_add_u32 m0, s57, 65536\n"
    "ds_read_b64_tr_b16 v[128:129], v176 offset:0\n"
    "v_mfma_f32_16x16x32_bf16 v[200:203], a[224:227], v[152:155], v[200:203]\n"
    "s_add_u32 s68, s80, 0\n"
    "buffer_load_dwordx4 v184, s[40:43], s68 offen offset:0 lds\n"
    "v_mfma_f32_16x16x32_bf16 v[200:203], a[240:243], v[156:159], v[200:203]\n"
    "ds_read_b64_tr_b16 v[130:131], v176 offset:512\n"
    "v_mfma_f32_16x16x32_bf16 v[216:219], a[196:199], v[144:147], v[216:219]\n"
    "v_mfma_f32_16x16x32_bf16 v[216:219], a[212:215], v[148:151], v[216:219]\n"
    "ds_read_b64_tr_b16 v[132:133], v176 offset:4096\n"
    "v_mfma_f32_16x16x32_bf16 v[216:219], a[228:231], v[152:155], v[216:219]\n"
    "s_add_u32 s68, s80, 64512\n"
    "buffer_load_dwordx4 v185, s[40:43], s68 offen offset:1024 lds\n"
    "v_mfma_f32_16x16x32_bf16 v[216:219], a[244:247], v[156:159], v[216:219]\n"
    "ds_read_b64_tr_b16 v[134:135], v176 offset:4608\n"
    "v_mfma_f32_16x16x32_bf16 v[232:235], a[200:203], v[144:147], v[232:235]\n"
    "v_mfma_f32_16x16x32_bf16 v[232:235], a[216:219], v[148:151], v[232:235]\n"
    "ds_read_b64_tr_b16 v[136:137], v176 offset:8192\n"
    "v_mfma_f32_16x16x32_bf16 v[232:235], a[232:235], v[152:155], v[232:235]\n"
    "s_add_u32 s68, s80, 129024\n"
    "buffer_load_dwordx4 v184, s[40:43], s68 offen offset:2048 lds\n"
    "v_mfma_f32_16x16x32_bf16 v[232:235], a[248:251], v[156:159], v[232:235]\n"
    "ds_read_b64_tr_b16 v[138:139], v176 offset:8704\n"
    "v_mfma_f32_16x16x32_bf16 v[248:251], a[204:207], v[144:147], v[248:251]\n"
    "v_mfma_f32_16x16x32_bf16 v[248:251], a[220:223], v[148:151], v[248:251]\n"
    "ds_read_b64_tr_b16 v[140:141], v176 offset:12288\n"
    "v_mfma_f32_16x16x32_bf16 v[248:251], a[236:239], v[152:155], v[248:251]\n"
    "s_add_u32 s68, s80, 193536\n"
    "buffer_load_dwordx4 v185, s[40:43], s68 offen offset:3072 lds\n"
    "v_mfma_f32_16x16x32_bf16 v[248:251], a[252:255], v[156:159], v[248:251]\n"
    "ds_read_b64_tr_b16 v[142:143], v176 offset:12800\n"
    "v_mfma_f32_16x16x32_bf16 v[204:207], a[192:195], v[160:163], v[204:207]\n"
    "v_mfma_f32_16x16x32_bf16 v[204:207], a[208:211], v[164:167], v[204:207]\n"
    "s_add_u32 m0, s57, 81920\n"
    "ds_read_b64_tr_b16 v[144:145], v177 offset:0\n"
    "v_mfma_f32_16x16x32_bf16 v[204:207], a[224:227], v[168:171], v[204:207]\n"
    "s_add_u32 s68, s80, 128\n"
    "buffer_load_dwordx4 v184, s[40:43], s68 offen offset:0 lds\n"
    "v_mfma_f32_16x16x32_bf16 v[204:207], a[240:243], v[172:175], v[204:207]\n"
    "ds_read_b64_tr_b16 v[146:147], v177 offset:512\n"
    "v_mfma_f32_16x16x32_bf16 v[220:223], a[196:199], v[160:163], v[220:223]\n"
    "v_mfma_f32_16x16x32_bf16 v[220:223], a[212:215], v[164:167], v[220:223]\n"
    "ds_read_b64_tr_b16 v[148:149], v177 offset:4096\n"
    "v_mfma_f32_16x16x32_bf16 v[220:223], a[228:231], v[168:171], v[220:223]\n"
    "s_add_u32 s68, s80, 64640\n"
    "buffer_load_dwordx4 v185, s[40:43], s68 offen offset:1024 lds\n"
    "v_mfma_f32_16x16x32_bf16 v[220:223], a[244:247], v[172:175], v[220:223]\n"
    "ds_read_b64_tr_b16 v[150:151], v177 offset:4608\n"
    "v_mfma_f32_16x16x32_bf16 v[236:239], a[200:203], v[160:163], v[236:239]\n"
    "v_mfma_f32_16x16x32_bf16 v[236:239], a[216:219], v[164:167], v[236:239]\n"
    "ds_read_b64_tr_b16 v[152:153], v177 offset:8192\n"
    "v_mfma_f32_16x16x32_bf16 v[236:239], a[232:235], v[168:171], v[236:239]\n"
    "s_add_u32 s68, s80, 129152\n"
    "buffer_load_dwordx4 v184, s[40:43], s68 offen offset:2048 lds\n"
    "v_mfma_f32_16x16x32_bf16 v[236:239], a[248:251], v[172:175], v[236:239]\n"
    "ds_read_b64_tr_b16 v[154:155], v177 offset:8704\n"
    "v_mfma_f32_16x16x32_bf16 v[252:255], a[204:207], v[160:163], v[252:255]\n"
    "v_mfma_f32_16x16x32_bf16 v[252:255], a[220:223], v[164:167], v[252:255]\n"
    "ds_read_b64_tr_b16 v[156:157], v177 offset:12288\n"
    "v_mfma_f32_16x16x32_bf16 v[252:255], a[236:239], v[168:171], v[252:255]\n"
    "s_add_u32 s68, s80, 193664\n"
    "buffer_load_dwordx4 v185, s[40:43], s68 offen offset:3072 lds\n"
    "v_mfma_f32_16x16x32_bf16 v[252:255], a[252:255], v[172:175], v[252:255]\n"
    "ds_read_b64_tr_b16 v[158:159], v177 offset:12800\n"
    "s_waitcnt lgkmcnt(8)\n"
    "v_mfma_f32_16x16x32_bf16 v[0:3], a[0:3], v[128:131], 0\n"
    "v_mfma_f32_16x16x32_bf16 v[0:3], a[16:19], v[132:135], v[0:3]\n"
    "s_add_u32 m0, s57, 98304\n"
    "ds_read_b64_tr_b16 v[160:161], v178 offset:0\n"
    "v_mfma_f32_16x16x32_bf16 v[0:3], a[32:35], v[136:139], v[0:3]\n"
    "s_add_u32 s68, s81, 0\n"
    "buffer_load_dwordx4 v184, s[40:43], s68 offen offset:0 lds\n"
    "v_mfma_f32_16x16x32_bf16 v[0:3], a[48:51], v[140:143], v[0:3]\n"
    "ds_read_b64_tr_b16 v[162:163], v178 offset:512\n"
    "v_mfma_f32_16x16x32_bf16 v[16:19], a[4:7], v[128:131], 0\n"
    "v_mfma_f32_16x16x32_bf16 v[16:19], a[20:23], v[132:135], v[16:19]\n"
    "ds_read_b64_tr_b16 v[164:165], v178 offset:4096\n"
    "v_mfma_f32_16x16x32_bf16 v[16:19], a[36:39], v[136:139], v[16:19]\n"
    "s_add_u32 s68, s81, 64512\n"
    "buffer_load_dwordx4 v185, s[40:43], s68 offen offset:1024 lds\n"
    "v_mfma_f32_16x16x32_bf16 v[16:19], a[52:55], v[140:143], v[16:19]\n"
    "ds_read_b64_tr_b16 v[166:167], v178 offset:4608\n"
    "v_mfma_f32_16x16x32_bf16 v[32:35], a[8:11], v[128:131], 0\n"
    "v_mfma_f32_16x16x32_bf16 v[32:35], a[24:27], v[132:135], v[32:35]\n"
    "ds_read_b64_tr_b16 v[168:169], v178 offset:8192\n"
    "v_mfma_f32_16x16x32_bf16 v[32:35], a[40:43], v[136:139], v[32:35]\n"
    "s_add_u32 s68, s81, 129024\n"
    "buffer_load_dwordx4 v184, s[40:43], s68 offen offset:2048 lds\n"
    "v_mfma_f32_16x16x32_bf16 v[32:35], a[56:59], v[140:143], v[32:35]\n"
    "ds_read_b64_tr_b16 v[170:171], v178 offset:8704\n"
    "v_mfma_f32_16x16x32_bf16 v[48:51], a[12:15], v[128:131], 0\n"
    "buffer_load_dword v187, v186, s[76:79], s74 offen sc1\n"
    "v_mfma_f32_16x16x32_bf16 v[48:51], a[28:31], v[132:135], v[48:51]\n"
    "ds_read_b64_tr_b16 v[172:173], v178 offset:12288\n"
    "v_mfma_f32_16x16x32_bf16 v[48:51], a[44:47], v[136:139], v[48:51]\n"
    "s_add_u32 s68, s81, 193536\n"
    "buffer_load_dwordx4 v185, s[40:43], s68 offen offset:3072 lds\n"
    "v_mfma_f32_16x16x32_bf16 v[48:51], a[60:63], v[140:143], v[48:51]\n"
    "ds_read_b64_tr_b16 v[174:175], v178 offset:12800\n"
    "s_waitcnt lgkmcnt(8)\n"
    "v_mfma_f32_16x16x32_bf16 v[4:7], a[0:3], v[144:147], 0\n"
    "v_mfma_f32_16x16x32_bf16 v[4:7], a[16:19], v[148:151], v[4:7]\n"
    "s_add_u32 m0, s57, 114688\n"
    "ds_read_b64_tr_b16 v[128:129], v179 offset:0\n"
    "v_mfma_f32_16x16x32_bf16 v[4:7], a[32:35], v[152:155], v[4:7]\n"
    "s_add_u32 s68, s81, 128\n"
    "buffer_load_dwordx4 v184, s[40:43], s68 offen offset:0 lds\n"
    "v_mfma_f32_16x16x32_bf16 v[4:7], a[48:51], v[156:159], v[4:7]\n"
    "ds_read_b64_tr_b16 v[130:131], v179 offset:512\n"
    "v_mfma_f32_16x16x32_bf16 v[20:23], a[4:7], v[144:147], 0\n"
    "v_mfma_f32_16x16x32_bf16 v[20:23], a[20:23], v[148:151], v[20:23]\n"
    "ds_read_b64_tr_b16 v[132:133], v179 offset:4096\n"
    "v_mfma_f32_16x16x32_bf16 v[20:23], a[36:39], v[152:155], v[20:23]\n"
    "s_add_u32 s68, s81, 64640\n"
    "buffer_load_dwordx4 v185, s[40:43], s68 offen offset:1024 lds\n"
    "v_mfma_f32_16x16x32_bf16 v[20:23], a[52:55], v[156:159], v[20:23]\n"
    "ds_read_b64_tr_b16 v[134:135], v179 offset:4608\n"
    "v_cvt_pk_bf16_f32 v192, v192, v193\n"
    "v_mfma_f32_16x16x32_bf16 v[36:39], a[8:11], v[144:147], 0\n"
    "v_mfma_f32_16x16x32_bf16 v[36:39], a[24:27], v[148:151], v[36:39]\n"
    "ds_read_b64_tr_b16 v[136:137], v179 offset:8192\n"
    "v_cvt_pk_bf16_f32 v193, v194, v195\n"
    "v_mfma_f32_16x16x32_bf16 v[36:39], a[40:43], v[152:155], v[36:39]\n"
    "s_add_u32 s68, s81, 129152\n"
    "buffer_load_dwordx4 v184, s[40:43], s68 offen offset:2048 lds\n"
    "v_mfma_f32_16x16x32_bf16 v[36:39], a[56:59], v[156:159], v[36:39]\n"
    "ds_read_b64_tr_b16 v[138:139], v179 offset:8704\n"
    "v_cvt_pk_bf16_f32 v194, v208, v209\n"
    "v_mfma_f32_16x16x32_bf16 v[52:55], a[12:15], v[144:147], 0\n"
    "v_mfma_f32_16x16x32_bf16 v[52:55], a[28:31], v[148:151], v[52:55]\n"
    "ds_read_b64_tr_b16 v[140:141], v179 offset:12288\n"
    "v_cvt_pk_bf16_f32 v195, v210, v211\n"
    "v_mfma_f32_16x16x32_bf16 v[52:55], a[44:47], v[152:155], v[52:55]\n"
    "s_add_u32 s68, s81, 193664\n"
    "buffer_load_dwordx4 v185, s[40:43], s68 offen offset:3072 lds\n"
    "v_mfma_f32_16x16x32_bf16 v[52:55], a[60:63], v[156:159], v[52:55]\n"
    "ds_read_b64_tr_b16 v[142:143], v179 offset:12800\n"
    "s_waitcnt lgkmcnt(8)\n"
    "v_mfma_f32_16x16x32_bf16 v[8:11], a[0:3], v[160:163], 0\n"
    "s_add_u32 s69, s58, 524288\n"
    "buffer_store_dwordx4 v[192:195], v190, s[44:47], s69 offen sc1\n"
    "v_mfma_f32_16x16x32_bf16 v[8:11], a[16:19], v[164:167], v[8:11]\n"
    "ds_read_b64_tr_b16 v[144:145], v176 offset:16384\n"
    "v_mfma_f32_16x16x32_bf16 v[8:11], a[32:35], v[168:171], v[8:11]\n"
    "v_mfma_f32_16x16x32_bf16 v[8:11], a[48:51], v[172:175], v[8:11]\n"
    "ds_read_b64_tr_b16 v[146:147], v176 offset:16896\n"
    "v_mfma_f32_16x16x32_bf16 v[24:27], a[4:7], v[160:163], 0\n"
    "v_mfma_f32_16x16x32_bf16 v[24:27], a[20:23], v[164:167], v[24:27]\n"
    "ds_read_b64_tr_b16 v[148:149], v176 offset:20480\n"
    "v_mfma_f32_16x16x32_bf16 v[24:27], a[36:39], v[168:171], v[24:27]\n"
    "v_mfma_f32_16x16x32_bf16 v[24:27], a[52:55], v[172:175], v[24:27]\n"
    "ds_read_b64_tr_b16 v[150:151], v176 offset:20992\n"
    "v_mfma_f32_16x16x32_bf16 v[40:43], a[8:11], v[160:163], 0\n"
    "v_mfma_f32_16x16x32_bf16 v[40:43], a[24:27], v[164:167], v[40:43]\n"
    "ds_read_b64_tr_b16 v[152:153], v176 offset:24576\n"
    "v_mfma_f32_16x16x32_bf16 v[40:43], a[40:43], v[168:171], v[40:43]\n"
    "v_mfma_f32_16x16x32_bf16 v[40:43], a[56:59], v[172:175], v[40:43]\n"
    "ds_read_b64_tr_b16 v[154:155], v176 offset:25088\n"
    "v_mfma_f32_16x16x32_bf16 v[56:59], a[12:15], v[160:163], 0\n"
    "v_mfma_f32_16x16x32_bf16 v[56:59], a[28:31], v[164:167], v[56:59]\n"
    "ds_read_b64_tr_b16 v[156:157], v176 offset:28672\n"
    "v_mfma_f32_16x16x32_bf16 v[56:59], a[44:47], v[168:171], v[56:59]\n"
    "v_mfma_f32_16x16x32_bf16 v[56:59], a[60:63], v[172:175], v[56:59]\n"
    "ds_read_b64_tr_b16 v[158:159], v176 offset:29184\n"
    "s_waitcnt lgkmcnt(8)\n"
    "v_mfma_f32_16x16x32_bf16 v[12:15], a[0:3], v[128:131], 0\n"
    "v_mfma_f32_16x16x32_bf16 v[12:15], a[16:19], v[132:135], v[12:15]\n"
    "ds_read_b64_tr_b16 v[160:161], v177 offset:16384\n"
    "v_mfma_f32_16x16x32_bf16 v[12:15], a[32:35], v[136:139], v[12:15]\n"
    "v_mfma_f32_16x16x32_bf16 v[12:15], a[48:51], v[140:143], v[12:15]\n"
    "ds_read_b64_tr_b16 v[162:163], v177 offset:16896\n"
    "v_cvt_pk_bf16_f32 v224, v224, v225\n"
    "v_mfma_f32_16x16x32_bf16 v[28:31], a[4:7], v[128:131], 0\n"
    "v_mfma_f32_16x16x32_bf16 v[28:31], a[20:23], v[132:135], v[28:31]\n"
    "ds_read_b64_tr_b16 v[164:165], v177 offset:20480\n"
    "v_cvt_pk_bf16_f32 v225, v226, v227\n"
    "v_mfma_f32_16x16x32_bf16 v[28:31], a[36:39], v[136:139], v[28:31]\n"
    "v_mfma_f32_16x16x32_bf16 v[28:31], a[52:55], v[140:143], v[28:31]\n"
    "ds_read_b64_tr_b16 v[166:167], v177 offset:20992\n"
    "v_cvt_pk_bf16_f32 v226, v240, v241\n"
    "v_mfma_f32_16x16x32_bf16 v[44:47], a[8:11], v[128:131], 0\n"
    "v_mfma_f32_16x16x32_bf16 v[44:47], a[24:27], v[132:135], v[44:47]\n"
    "ds_read_b64_tr_b16 v[168:169], v177 offset:24576\n"
    "v_cvt_pk_bf16_f32 v227, v242, v243\n"
    "v_mfma_f32_16x16x32_bf16 v[44:47], a[40:43], v[136:139], v[44:47]\n"
    "s_add_u32 s69, s58, 525312\n"
    "buffer_store_dwordx4 v[224:227], v190, s[44:47], s69 offen sc1\n"
    "v_mfma_f32_16x16x32_bf16 v[44:47], a[56:59], v[140:143], v[44:47]\n"
    "ds_read_b64_tr_b16 v[170:171], v177 offset:25088\n"
    "v_mfma_f32_16x16x32_bf16 v[60:63], a[12:15], v[128:131], 0\n"
    "s_waitcnt vmcnt(7)\n"
    "v_cmp_ne_u32 vcc, 1, v187\n"
    "s_cbranch_vccz L_fh_ok\n"
    "s_mov_b32 s14, 0x40000\n"
    "L_fh_spin:\n"
    "buffer_load_dword v187, v186, s[76:79], s74 offen sc1\n"
    "s_sub_u32 s14, s14, 1\n"
    "s_waitcnt vmcnt(0)\n"
    "s_cbranch_scc1 L_fh_ok\n"
    "v_cmp_ne_u32 vcc, 1, v187\n"
    "s_cbranch_vccz L_fh_ok\n"
    "s_sleep 3\n"
    "s_branch L_fh_spin\n"
    "L_fh_ok:\n"
    "v_mfma_f32_16x16x32_bf16 v[60:63], a[28:31], v[132:135], v[60:63]\n"
    "ds_read_b64_tr_b16 v[172:173], v177 offset:28672\n"
    "v_mfma_f32_16x16x32_bf16 v[60:63], a[44:47], v[136:139], v[60:63]\n"
    "v_mfma_f32_16x16x32_bf16 v[60:63], a[60:63], v[140:143], v[60:63]\n"
    "ds_read_b64_tr_b16 v[174:175], v177 offset:29184\n"
    "s_waitcnt lgkmcnt(8)\n"
    "v_mfma_f32_16x16x32_bf16 v[0:3], a[64:67], v[144:147], v[0:3]\n"
    "v_mfma_f32_16x16x32_bf16 v[0:3], a[80:83], v[148:151], v[0:3]\n"
    "ds_read_b64_tr_b16 v[128:129], v178 offset:16384\n"
    "v_mfma_f32_16x16x32_bf16 v[0:3], a[96:99], v[152:155], v[0:3]\n"
    "v_mfma_f32_16x16x32_bf16 v[0:3], a[112:115], v[156:159], v[0:3]\n"
    "ds_read_b64_tr_b16 v[130:131], v178 offset:16896\n"
    "v_mfma_f32_16x16x32_bf16 v[16:19], a[68:71], v[144:147], v[16:19]\n"
    "v_mfma_f32_16x16x32_bf16 v[16:19], a[84:87], v[148:151], v[16:19]\n"
    "ds_read_b64_tr_b16 v[132:133], v178 offset:20480\n"
    "v_mfma_f32_16x16x32_bf16 v[16:19], a[100:103], v[152:155], v[16:19]\n"
    "v_mfma_f32_16x16x32_bf16 v[16:19], a[116:119], v[156:159], v[16:19]\n"
    "ds_read_b64_tr_b16 v[134:135], v178 offset:20992\n"
    "v_mfma_f32_16x16x32_bf16 v[32:35], a[72:75], v[144:147], v[32:35]\n"
    "s_add_u32 s70, s64, s88\n"
    "buffer_load_dwordx4 a[0:3], v190, s[76:79], s70 offen offset:0 sc1\n"
    "v_mfma_f32_16x16x32_bf16 v[32:35], a[88:91], v[148:151], v[32:35]\n"
    "ds_read_b64_tr_b16 v[136:137], v178 offset:24576\n"
    "v_mfma_f32_16x16x32_bf16 v[32:35], a[104:107], v[152:155], v[32:35]\n"
    "v_mfma_f32_16x16x32_bf16 v[32:35], a[120:123], v[156:159], v[32:35]\n"
    "ds_read_b64_tr_b16 v[138:139], v178 offset:25088\n"
    "v_mfma_f32_16x16x32_bf16 v[48:51], a[76:79], v[144:147], v[48:51]\n"
    "v_mfma_f32_16x16x32_bf16 v[48:51], a[92:95], v[148:151], v[48:51]\n"
    "ds_read_b64_tr_b16 v[140:141], v178 offset:28672\n"
    "v_mfma_f32_16x16x32_bf16 v[48:51], a[108:111], v[152:155], v[48:51]\n"
    "v_mfma_f32_16x16x32_bf16 v[48:51], a[124:127], v[156:159], v[48:51]\n"
    "ds_read_b64_tr_b16 v[142:143], v178 offset:29184\n"
    "v_cvt_pk_bf16_f32 v196, v196, v197\n"
    "s_waitcnt lgkmcnt(8)\n"
    "v_mfma_f32_16x16x32_bf16 v[4:7], a[64:67], v[160:163], v[4:7]\n"
    "v_mfma_f32_16x16x32_bf16 v[4:7], a[80:83], v[164:167], v[4:7]\n"
    "ds_read_b64_tr_b16 v[144:145], v179 offset:16384\n"
    "v_cvt_pk_bf16_f32 v197, v198, v199\n"
    "v_mfma_f32_16x16x32_bf16 v[4:7], a[96:99], v[168:171], v[4:7]\n"
    "buffer_load_dwordx4 a[16:19], v190, s[76:79], s70 offen offset:1024 sc1\n"
    "v_mfma_f32_16x16x32_bf16 v[4:7], a[112:115], v[172:175], v[4:7]\n"
    "ds_read_b64_tr_b16 v[146:147], v179 offset:16896\n"
    "v_cvt_pk_bf16_f32 v198, v212, v213\n"
    "v_mfma_f32_16x16x32_bf16 v[20:23], a[68:71], v[160:163], v[20:23]\n"
    "v_mfma_f32_16x16x32_bf16 v[20:23], a[84:87], v[164:167], v[20:23]\n"
    "ds_read_b64_tr_b16 v[148:149], v179 offset:20480\n"
    "v_cvt_pk_bf16_f32 v199, v214, v215\n"
    "v_mfma_f32_16x16x32_bf16 v[20:23], a[100:103], v[168:171], v[20:23]\n"
    "s_add_u32 s69, s58, 526336\n"
    "buffer_store_dwordx4 v[196:199], v190, s[44:47], s69 offen sc1\n"
    "v_mfma_f32_16x16x32_bf16 v[20:23], a[116:119], v[172:175], v[20:23]\n"
    "ds_read_b64_tr_b16 v[150:151], v179 offset:20992\n"
    "v_mfma_f32_16x16x32_bf16 v[36:39], a[72:75], v[160:163], v[36:39]\n"
    "v_mfma_f32_16x16x32_bf16 v[36:39], a[88:91], v[164:167], v[36:39]\n"
    "ds_read_b64_tr_b16 v[152:153], v179 offset:24576\n"
    "v_mfma_f32_16x16x32_bf16 v[36:39], a[104:107], v[168:171], v[36:39]\n"
    "buffer_load_dwordx4 a[32:35], v190, s[76:79], s70 offen offset:2048 sc1\n"
    "v_mfma_f32_16x16x32_bf16 v[36:39], a[120:123], v[172:175], v[36:39]\n"
    "ds_read_b64_tr_b16 v[154:155], v179 offset:25088\n"
    "v_mfma_f32_16x16x32_bf16 v[52:55], a[76:79], v[160:163], v[52:55]\n"
    "v_mfma_f32_16x16x32_bf16 v[52:55], a[92:95], v[164:167], v[52:55]\n"
    "ds_read_b64_tr_b16 v[156:157], v179 offset:28672\n"
    "v_mfma_f32_16x16x32_bf16 v[52:55], a[108:111], v[168:171], v[52:55]\n"
    "v_mfma_f32_16x16x32_bf16 v[52:55], a[124:127], v[172:175], v[52:55]\n"
    "ds_read_b64_tr_b16 v[158:159], v179 offset:29184\n"
    "s_waitcnt lgkmcnt(8)\n"
    "v_mfma_f32_16x16x32_bf16 v[8:11], a[64:67], v[128:131], v[8:11]\n"
    "v_mfma_f32_16x16x32_bf16 v[8:11], a[80:83], v[132:135], v[8:11]\n"
    "ds_read_b64_tr_b16 v[160:161], v176 offset:32768\n"
    "v_mfma_f32_16x16x32_bf16 v[8:11], a[96:99], v[136:139], v[8:11]\n"
    "v_mfma_f32_16x16x32_bf16 v[8:11], a[112:115], v[140:143], v[8:11]\n"
    "ds_read_b64_tr_b16 v[162:163], v176 offset:33280\n"
    "v_mfma_f32_16x16x32_bf16 v[24:27], a[68:71], v[128:131], v[24:27]\n"
    "buffer_load_dwordx4 a[48:51], v190, s[76:79], s70 offen offset:3072 sc1\n"
    "v_mfma_f32_16x16x32_bf16 v[24:27], a[84:87], v[132:135], v[24:27]\n"
    "ds_read_b64_tr_b16 v[164:165], v176 offset:36864\n"
    "v_mfma_f32_16x16x32_bf16 v[24:27], a[100:103], v[136:139], v[24:27]\n"
    "v_mfma_f32_16x16x32_bf16 v[24:27], a[116:119], v[140:143], v[24:27]\n"
    "ds_read_b64_tr_b16 v[166:167], v176 offset:37376\n"
    "v_mfma_f32_16x16x32_bf16 v[40:43], a[72:75], v[128:131], v[40:43]\n"
    "v_mfma_f32_16x16x32_bf16 v[40:43], a[88:91], v[132:135], v[40:43]\n"
    "ds_read_b64_tr_b16 v[168:169], v176 offset:40960\n"
    "v_mfma_f32_16x16x32_bf16 v[40:43], a[104:107], v[136:139], v[40:43]\n"
    "v_mfma_f32_16x16x32_bf16 v[40:43], a[120:123], v[140:143], v[40:43]\n"
    "ds_read_b64_tr_b16 v[170:171], v176 offset:41472\n"
    "v_cvt_pk_bf16_f32 v228, v228, v229\n"
    "v_mfma_f32_16x16x32_bf16 v[56:59], a[76:79], v[128:131], v[56:59]\n"
    "s_add_u32 s70, s65, s88\n"
    "buffer_load_dwordx4 a[4:7], v190, s[76:79], s70 offen offset:0 sc1\n"
    "v_mfma_f32_16x16x32_bf16 v[56:59], a[92:95], v[132:135], v[56:59]\n"
    "ds_read_b64_tr_b16 v[172:173], v176 offset:45056\n"
    "v_cvt_pk_bf16_f32 v229, v230, v231\n"
    "v_mfma_f32_16x16x32_bf16 v[56:59], a[108:111], v[136:139], v[56:59]\n"
    "v_mfma_f32_16x16x32_bf16 v[56:59], a[124:127], v[140:143], v[56:59]\n"
    "ds_read_b64_tr_b16 v[174:175], v176 offset:45568\n"
    "v_cvt_pk_bf16_f32 v230, v244, v245\n"
    "s_waitcnt lgkmcnt(8)\n"
    "v_mfma_f32_16x16x32_bf16 v[12:15], a[64:67], v[144:147], v[12:15]\n"
    "v_mfma_f32_16x16x32_bf16 v[12:15], a[80:83], v[148:151], v[12:15]\n"
    "ds_read_b64_tr_b16 v[128:129], v177 offset:32768\n"
    "v_cvt_pk_bf16_f32 v231, v246, v247\n"
    "v_mfma_f32_16x16x32_bf16 v[12:15], a[96:99], v[152:155], v[12:15]\n"
    "s_add_u32 s69, s58, 527360\n"
    "buffer_store_dwordx4 v[228:231], v190, s[44:47], s69 offen sc1\n"
    "v_mfma_f32_16x16x32_bf16 v[12:15], a[112:115], v[156:159], v[12:15]\n"
    "ds_read_b64_tr_b16 v[130:131], v177 offset:33280\n"
    "v_mfma_f32_16x16x32_bf16 v[28:31], a[68:71], v[144:147], v[28:31]\n"
    "v_mfma_f32_16x16x32_bf16 v[28:31], a[84:87], v[148:151], v[28:31]\n"
    "ds_read_b64_tr_b16 v[132:133], v177 offset:36864\n"
    "v_mfma_f32_16x16x32_bf16 v[28:31], a[100:103], v[152:155], v[28:31]\n"
    "buffer_load_dwordx4 a[20:23], v190, s[76:79], s70 offen offset:1024 sc1\n"
    "v_mfma_f32_16x16x32_bf16 v[28:31], a[116:119], v[156:159], v[28:31]\n"
    "ds_read_b64_tr_b16 v[134:135], v177 offset:37376\n"
    "v_mfma_f32_16x16x32_bf16 v[44:47], a[72:75], v[144:147], v[44:47]\n"
    "v_mfma_f32_16x16x32_bf16 v[44:47], a[88:91], v[148:151], v[44:47]\n"
    "ds_read_b64_tr_b16 v[136:137], v177 offset:40960\n"
    "v_mfma_f32_16x16x32_bf16 v[44:47], a[104:107], v[152:155], v[44:47]\n"
    "v_mfma_f32_16x16x32_bf16 v[44:47], a[120:123], v[156:159], v[44:47]\n"
    "ds_read_b64_tr_b16 v[138:139], v177 offset:41472\n"
    "v_mfma_f32_16x16x32_bf16 v[60:63], a[76:79], v[144:147], v[60:63]\n"
    "v_mfma_f32_16x16x32_bf16 v[60:63], a[92:95], v[148:151], v[60:63]\n"
    "ds_read_b64_tr_b16 v[140:141], v177 offset:45056\n"
    "v_mfma_f32_16x16x32_bf16 v[60:63], a[108:111], v[152:155], v[60:63]\n"
    "buffer_load_dwordx4 a[36:39], v190, s[76:79], s70 offen offset:2048 sc1\n"
    "v_mfma_f32_16x16x32_bf16 v[60:63], a[124:127], v[156:159], v[60:63]\n"
    "ds_read_b64_tr_b16 v[142:143], v177 offset:45568\n"
    "s_waitcnt lgkmcnt(8)\n"
    "v_mfma_f32_16x16x32_bf16 v[0:3], a[128:131], v[160:163], v[0:3]\n"
    "v_mfma_f32_16x16x32_bf16 v[0:3], a[144:147], v[164:167], v[0:3]\n"
    "ds_read_b64_tr_b16 v[144:145], v178 offset:32768\n"
    "v_mfma_f32_16x16x32_bf16 v[0:3], a[160:163], v[168:171], v[0:3]\n"
    "v_mfma_f32_16x16x32_bf16 v[0:3], a[176:179], v[172:175], v[0:3]\n"
    "ds_read_b64_tr_b16 v[146:147], v178 offset:33280\n"
    "v_mfma_f32_16x16x32_bf16 v[16:19], a[132:135], v[160:163], v[16:19]\n"
    "v_mfma_f32_16x16x32_bf16 v[16:19], a[148:151], v[164:167], v[16:19]\n"
    "ds_read_b64_tr_b16 v[148:149], v178 offset:36864\n"
    "v_mfma_f32_16x16x32_bf16 v[16:19], a[164:167], v[168:171], v[16:19]\n"
    "v_mfma_f32_16x16x32_bf16 v[16:19], a[180:183], v[172:175], v[16:19]\n"
    "ds_read_b64_tr_b16 v[150:151], v178 offset:37376\n"
    "v_cvt_pk_bf16_f32 v200, v200, v201\n"
    "v_mfma_f32_16x16x32_bf16 v[32:35], a[136:139], v[160:163], v[32:35]\n"
    "buffer_load_dwordx4 a[52:55], v190, s[76:79], s70 offen offset:3072 sc1\n"
    "v_mfma_f32_16x16x32_bf16 v[32:35], a[152:155], v[164:167], v[32:35]\n"
    "ds_read_b64_tr_b16 v[152:153], v178 offset:40960\n"
    "v_cvt_pk_bf16_f32 v201, v202, v203\n"
    "v_mfma_f32_16x16x32_bf16 v[32:35], a[168:171], v[168:171], v[32:35]\n"
    "v_mfma_f32_16x16x32_bf16 v[32:35], a[184:187], v[172:175], v[32:35]\n"
    "ds_read_b64_tr_b16 v[154:155], v178 offset:41472\n"
    "v_cvt_pk_bf16_f32 v202, v216, v217\n"
    "v_mfma_f32_16x16x32_bf16 v[48:51], a[140:143], v[160:163], v[48:51]\n"
    "v_mfma_f32_16x16x32_bf16 v[48:51], a[156:159], v[164:167], v[48:51]\n"
    "ds_read_b64_tr_b16 v[156:157], v178 offset:45056\n"
    "v_cvt_pk_bf16_f32 v203, v218, v219\n"
    "v_mfma_f32_16x16x32_bf16 v[48:51], a[172:175], v[168:171], v[48:51]\n"
    "s_add_u32 s69, s58, 528384\n"
    "buffer_store_dwordx4 v[200:203], v190, s[44:47], s69 offen sc1\n"
    "v_mfma_f32_16x16x32_bf16 v[48:51], a[188:191], v[172:175], v[48:51]\n"
    "ds_read_b64_tr_b16 v[158:159], v178 offset:45568\n"
    "s_waitcnt lgkmcnt(8)\n"
    "v_mfma_f32_16x16x32_bf16 v[4:7], a[128:131], v[128:131], v[4:7]\n"
    "s_add_u32 s70, s66, s88\n"
    "buffer_load_dwordx4 a[8:11], v190, s[76:79], s70 offen offset:0 sc1\n"
    "v_mfma_f32_16x16x32_bf16 v[4:7], a[144:147], v[132:135], v[4:7]\n"
    "ds_read_b64_tr_b16 v[160:161], v179 offset:32768\n"
    "v_mfma_f32_16x16x32_bf16 v[4:7], a[160:163], v[136:139], v[4:7]\n"
    "v_mfma_f32_16x16x32_bf16 v[4:7], a[176:179], v[140:143], v[4:7]\n"
    "ds_read_b64_tr_b16 v[162:163], v179 offset:33280\n"
    "v_mfma_f32_16x16x32_bf16 v[20:23], a[132:135], v[128:131], v[20:23]\n"
    "v_mfma_f32_16x16x32_bf16 v[20:23], a[148:151], v[132:135], v[20:23]\n"
    "ds_read_b64_tr_b16 v[164:165], v179 offset:36864\n"
    "v_mfma_f32_16x16x32_bf16 v[20:23], a[164:167], v[136:139], v[20:23]\n"
    "v_mfma_f32_16x16x32_bf16 v[20:23], a[180:183], v[140:143], v[20:23]\n"
    "ds_read_b64_tr_b16 v[166:167], v179 offset:37376\n"
    "v_mfma_f32_16x16x32_bf16 v[36:39], a[136:139], v[128:131], v[36:39]\n"
    "v_mfma_f32_16x16x32_bf16 v[36:39], a[152:155], v[132:135], v[36:39]\n"
    "ds_read_b64_tr_b16 v[168:169], v179 offset:40960\n"
    "v_mfma_f32_16x16x32_bf16 v[36:39], a[168:171], v[136:139], v[36:39]\n"
    "buffer_load_dwordx4 a[24:27], v190, s[76:79], s70 offen offset:1024 sc1\n"
    "v_mfma_f32_16x16x32_bf16 v[36:39], a[184:187], v[140:143], v[36:39]\n"
    "ds_read_b64_tr_b16 v[170:171], v179 offset:41472\n"
    "v_mfma_f32_16x16x32_bf16 v[52:55], a[140:143], v[128:131], v[52:55]\n"
    "v_mfma_f32_16x16x32_bf16 v[52:55], a[156:159], v[132:135], v[52:55]\n"
    "ds_read_b64_tr_b16 v[172:173], v179 offset:45056\n"
    "v_mfma_f32_16x16x32_bf16 v[52:55], a[172:175], v[136:139], v[52:55]\n"
    "v_mfma_f32_16x16x32_bf16 v[52:55], a[188:191], v[140:143], v[52:55]\n"
    "ds_read_b64_tr_b16 v[174:175], v179 offset:45568\n"
    "s_waitcnt lgkmcnt(8)\n"
    "v_mfma_f32_16x16x32_bf16 v[8:11], a[128:131], v[144:147], v[8:11]\n"
    "v_mfma_f32_16x16x32_bf16 v[8:11], a[144:147], v[148:151], v[8:11]\n"
    "ds_read_b64_tr_b16 v[128:129], v176 offset:49152\n"
    "v_mfma_f32_16x16x32_bf16 v[8:11], a[160:163], v[152:155], v[8:11]\n"
    "buffer_load_dwordx4 a[40:43], v190, s[76:79], s70 offen offset:2048 sc1\n"
    "v_mfma_f32_16x16x32_bf16 v[8:11], a[176:179], v[156:159], v[8:11]\n"
    "ds_read_b64_tr_b16 v[130:131], v176 offset:49664\n"
    "v_cvt_pk_bf16_f32 v232, v232, v233\n"
    "v_mfma_f32_16x16x32_bf16 v[24:27], a[132:135], v[144:147], v[24:27]\n"
    "v_mfma_f32_16x16x32_bf16 v[24:27], a[148:151], v[148:151], v[24:27]\n"
    "ds_read_b64_tr_b16 v[132:133], v176 offset:53248\n"
    "v_cvt_pk_bf16_f32 v233, v234, v235\n"
    "v_mfma_f32_16x16x32_bf16 v[24:27], a[164:167], v[152:155], v[24:27]\n"
    "v_mfma_f32_16x16x32_bf16 v[24:27], a[180:183], v[156:159], v[24:27]\n"
    "ds_read_b64_tr_b16 v[134:135], v176 offset:53760\n"
    "v_cvt_pk_bf16_f32 v234, v248, v249\n"
    "v_mfma_f32_16x16x32_bf16 v[40:43], a[136:139], v[144:147], v[40:43]\n"
    "v_mfma_f32_16x16x32_bf16 v[40:43], a[152:155], v[148:151], v[40:43]\n"
    "ds_read_b64_tr_b16 v[136:137], v176 offset:57344\n"
    "v_cvt_pk_bf16_f32 v235, v250, v251\n"
    "v_mfma_f32_16x16x32_bf16 v[40:43], a[168:171], v[152:155], v[40:43]\n"
    "s_add_u32 s69, s58, 529408\n"
    "buffer_store_dwordx4 v[232:235], v190, s[44:47], s69 offen sc1\n"
    "v_mfma_f32_16x16x32_bf16 v[40:43], a[184:187], v[156:159], v[40:43]\n"
    "ds_read_b64_tr_b16 v[138:139], v176 offset:57856\n"
    "v_mfma_f32_16x16x32_bf16 v[56:59], a[140:143], v[144:147], v[56:59]\n"
    "buffer_load_dwordx4 a[56:59], v190, s[76:79], s70 offen offset:3072 sc1\n"
    "v_mfma_f32_16x16x32_bf16 v[56:59], a[156:159], v[148:151], v[56:59]\n"
    "ds_read_b64_tr_b16 v[140:141], v176 offset:61440\n"
    "v_mfma_f32_16x16x32_bf16 v[56:59], a[172:175], v[152:155], v[56:59]\n"
    "v_mfma_f32_16x16x32_bf16 v[56:59], a[188:191], v[156:159], v[56:59]\n"
    "ds_read_b64_tr_b16 v[142:143], v176 offset:61952\n"
    "s_waitcnt lgkmcnt(8)\n"
    "v_mfma_f32_16x16x32_bf16 v[12:15], a[128:131], v[160:163], v[12:15]\n"
    "v_mfma_f32_16x16x32_bf16 v[12:15], a[144:147], v[164:167], v[12:15]\n"
    "ds_read_b64_tr_b16 v[144:145], v177 offset:49152\n"
    "v_mfma_f32_16x16x32_bf16 v[12:15], a[160:163], v[168:171], v[12:15]\n"
    "v_mfma_f32_16x16x32_bf16 v[12:15], a[176:179], v[172:175], v[12:15]\n"
    "ds_read_b64_tr_b16 v[146:147], v177 offset:49664\n"
    "v_mfma_f32_16x16x32_bf16 v[28:31], a[132:135], v[160:163], v[28:31]\n"
    "s_add_u32 s70, s67, s88\n"
    "buffer_load_dwordx4 a[12:15], v190, s[76:79], s70 offen offset:0 sc1\n"
    "v_mfma_f32_16x16x32_bf16 v[28:31], a[148:151], v[164:167], v[28:31]\n"
    "ds_read_b64_tr_b16 v[148:149], v177 offset:53248\n"
    "v_mfma_f32_16x16x32_bf16 v[28:31], a[164:167], v[168:171], v[28:31]\n"
    "v_mfma_f32_16x16x32_bf16 v[28:31], a[180:183], v[172:175], v[28:31]\n"
    "ds_read_b64_tr_b16 v[150:151], v177 offset:53760\n"
    "v_mfma_f32_16x16x32_bf16 v[44:47], a[136:139], v[160:163], v[44:47]\n"
    "v_mfma_f32_16x16x32_bf16 v[44:47], a[152:155], v[164:167], v[44:47]\n"
    "ds_read_b64_tr_b16 v[152:153], v177 offset:57344\n"
    "v_mfma_f32_16x16x32_bf16 v[44:47], a[168:171], v[168:171], v[44:47]\n"
    "v_mfma_f32_16x16x32_bf16 v[44:47], a[184:187], v[172:175], v[44:47]\n"
    "ds_read_b64_tr_b16 v[154:155], v177 offset:57856\n"
    "v_mfma_f32_16x16x32_bf16 v[60:63], a[140:143], v[160:163], v[60:63]\n"
    "v_mfma_f32_16x16x32_bf16 v[60:63], a[156:159], v[164:167], v[60:63]\n"
    "ds_read_b64_tr_b16 v[156:157], v177 offset:61440\n"
    "v_mfma_f32_16x16x32_bf16 v[60:63], a[172:175], v[168:171], v[60:63]\n"
    "buffer_load_dwordx4 a[28:31], v190, s[76:79], s70 offen offset:1024 sc1\n"
    "v_mfma_f32_16x16x32_bf16 v[60:63], a[188:191], v[172:175], v[60:63]\n"
    "ds_read_b64_tr_b16 v[158:159], v177 offset:61952\n"
    ".p2align 12\n"
    "L_pad4:\n"
    "s_cbranch_execz L_pad8\n"
    "v_cvt_pk_bf16_f32 v204, v204, v205\n"
    "s_waitcnt lgkmcnt(8)\n"
    "v_mfma_f32_16x16x32_bf16 v[0:3], a[192:195], v[128:131], v[0:3]\n"
    "v_mfma_f32_16x16x32_bf16 v[0:3], a[208:211], v[132:135], v[0:3]\n"
    "ds_read_b64_tr_b16 v[160:161], v178 offset:49152\n"
    "v_cvt_pk_bf16_f32 v205, v206, v207\n"
    "v_mfma_f32_16x16x32_bf16 v[0:3], a[224:227], v[136:139], v[0:3]\n"
    "v_mfma_f32_16x16x32_bf16 v[0:3], a[240:243], v[140:143], v[0:3]\n"
    "ds_read_b64_tr_b16 v[162:163], v178 offset:49664\n"
    "v_cvt_pk_bf16_f32 v206, v220, v221\n"
    "v_mfma_f32_16x16x32_bf16 v[16:19], a[196:199], v[128:131], v[16:19]\n"
    "v_mfma_f32_16x16x32_bf16 v[16:19], a[212:215], v[132:135], v[16:19]\n"
    "ds_read_b64_tr_b16 v[164:165], v178 offset:53248\n"
    "v_cvt_pk_bf16_f32 v207, v222, v223\n"
    "v_mfma_f32_16x16x32_bf16 v[16:19], a[228:231], v[136:139], v[16:19]\n"
    "buffer_load_dwordx4 a[44:47], v190, s[76:79], s70 offen offset:2048 sc1\n"
    "v_mfma_f32_16x16x32_bf16 v[16:19], a[244:247], v[140:143], v[16:19]\n"
    "ds_read_b64_tr_b16 v[166:167], v178 offset:53760\n"
    "v_mfma_f32_16x16x32_bf16 v[32:35], a[200:203], v[128:131], v[32:35]\n"
    "s_add_u32 s69, s58, 530432\n"
    "buffer_store_dwordx4 v[204:207], v190, s[44:47], s69 offen sc1\n"
    "v_mfma_f32_16x16x32_bf16 v[32:35], a[216:219], v[132:135], v[32:35]\n"
    "ds_read_b64_tr_b16 v[168:169], v178 offset:57344\n"
    "v_mfma_f32_16x16x32_bf16 v[32:35], a[232:235], v[136:139], v[32:35]\n"
    "v_mfma_f32_16x16x32_bf16 v[32:35], a[248:251], v[140:143], v[32:35]\n"
    "ds_read_b64_tr_b16 v[170:171], v178 offset:57856\n"
    "v_mfma_f32_16x16x32_bf16 v[48:51], a[204:207], v[128:131], v[48:51]\n"
    "v_mfma_f32_16x16x32_bf16 v[48:51], a[220:223], v[132:135], v[48:51]\n"
    "ds_read_b64_tr_b16 v[172:173], v178 offset:61440\n"
    "v_mfma_f32_16x16x32_bf16 v[48:51], a[236:239], v[136:139], v[48:51]\n"
    "v_mfma_f32_16x16x32_bf16 v[48:51], a[252:255], v[140:143], v[48:51]\n"
    "ds_read_b64_tr_b16 v[174:175], v178 offset:61952\n"
    "s_waitcnt lgkmcnt(8)\n"
    "v_mfma_f32_16x16x32_bf16 v[4:7], a[192:195], v[144:147], v[4:7]\n"
    "ds_read_b64_tr_b16 v[128:129], v179 offset:49152\n"
    "v_mfma_f32_16x16x32_bf16 v[4:7], a[208:211], v[148:151], v[4:7]\n"
    "ds_read_b64_tr_b16 v[130:131], v179 offset:49664\n"
    "v_mfma_f32_16x16x32_bf16 v[4:7], a[224:227], v[152:155], v[4:7]\n"
    "ds_read_b64_tr_b16 v[132:133], v179 offset:53248\n"
    "v_mfma_f32_16x16x32_bf16 v[4:7], a[240:243], v[156:159], v[4:7]\n"
    "ds_read_b64_tr_b16 v[134:135], v179 offset:53760\n"
    "v_mfma_f32_16x16x32_bf16 v[20:23], a[196:199], v[144:147], v[20:23]\n"
    "ds_read_b64_tr_b16 v[136:137], v179 offset:57344\n"
    "v_mfma_f32_16x16x32_bf16 v[20:23], a[212:215], v[148:151], v[20:23]\n"
    "ds_read_b64_tr_b16 v[138:139], v179 offset:57856\n"
    "v_mfma_f32_16x16x32_bf16 v[20:23], a[228:231], v[152:155], v[20:23]\n"
    "ds_read_b64_tr_b16 v[140:141], v179 offset:61440\n"
    "v_mfma_f32_16x16x32_bf16 v[20:23], a[244:247], v[156:159], v[20:23]\n"
    "ds_read_b64_tr_b16 v[142:143], v179 offset:61952\n"
    "v_mfma_f32_16x16x32_bf16 v[36:39], a[200:203], v[144:147], v[36:39]\n"
    "buffer_load_dwordx4 a[60:63], v190, s[76:79], s70 offen offset:3072 sc1\n"
    "v_mfma_f32_16x16x32_bf16 v[36:39], a[216:219], v[148:151], v[36:39]\n"
    "v_mfma_f32_16x16x32_bf16 v[36:39], a[232:235], v[152:155], v[36:39]\n"
    "v_mfma_f32_16x16x32_bf16 v[36:39], a[248:251], v[156:159], v[36:39]\n"
    "v_cvt_pk_bf16_f32 v236, v236, v237\n"
    "v_cvt_pk_bf16_f32 v237, v238, v239\n"
    "v_mfma_f32_16x16x32_bf16 v[52:55], a[204:207], v[144:147], v[52:55]\n"
    "v_mfma_f32_16x16x32_bf16 v[52:55], a[220:223], v[148:151], v[52:55]\n"
    "v_cvt_pk_bf16_f32 v238, v252, v253\n"
    "v_cvt_pk_bf16_f32 v239, v254, v255\n"
    "v_mfma_f32_16x16x32_bf16 v[52:55], a[236:239], v[152:155], v[52:55]\n"
    "s_add_u32 s70, s64, s89\n"
    "buffer_load_dwordx4 a[64:67], v190, s[76:79], s70 offen offset:0 sc1\n"
    "v_mfma_f32_16x16x32_bf16 v[52:55], a[252:255], v[156:159], v[52:55]\n"
    "s_add_u32 s69, s58, 531456\n"
    "buffer_store_dwordx4 v[236:239], v190, s[44:47], s69 offen sc1\n"
    "s_waitcnt lgkmcnt(8)\n"
    "s_waitcnt vmcnt(25) lgkmcnt(0)\n"
    "s_barrier\n"
    "v_mfma_f32_16x16x32_bf16 v[8:11], a[192:195], v[160:163], v[8:11]\n"
    "v_mfma_f32_16x16x32_bf16 v[8:11], a[208:211], v[164:167], v[8:11]\n"
    "ds_read_b64_tr_b16 v[144:145], v180 offset:0\n"
    "v_mfma_f32_16x16x32_bf16 v[8:11], a[224:227], v[168:171], v[8:11]\n"
    "v_mfma_f32_16x16x32_bf16 v[8:11], a[240:243], v[172:175], v[8:11]\n"
    "ds_read_b64_tr_b16 v[146:147], v180 offset:512\n"
    "v_mfma_f32_16x16x32_bf16 v[24:27], a[196:199], v[160:163], v[24:27]\n"
    "v_mfma_f32_16x16x32_bf16 v[24:27], a[212:215], v[164:167], v[24:27]\n"
    "ds_read_b64_tr_b16 v[148:149], v180 offset:4096\n"
    "v_mfma_f32_16x16x32_bf16 v[24:27], a[228:231], v[168:171], v[24:27]\n"
    "buffer_load_dwordx4 a[80:83], v190, s[76:79], s70 offen offset:1024 sc1\n"
    "v_mfma_f32_16x16x32_bf16 v[24:27], a[244:247], v[172:175], v[24:27]\n"
    "ds_read_b64_tr_b16 v[150:151], v180 offset:4608\n"
    "v_mfma_f32_16x16x32_bf16 v[40:43], a[200:203], v[160:163], v[40:43]\n"
    "v_mfma_f32_16x16x32_bf16 v[40:43], a[216:219], v[164:167], v[40:43]\n"
    "ds_read_b64_tr_b16 v[152:153], v180 offset:8192\n"
    "v_mfma_f32_16x16x32_bf16 v[40:43], a[232:235], v[168:171], v[40:43]\n"
    "v_mfma_f32_16x16x32_bf16 v[40:43], a[248:251], v[172:175], v[40:43]\n"
    "ds_read_b64_tr_b16 v[154:155], v180 offset:8704\n"
    "v_mfma_f32_16x16x32_bf16 v[56:59], a[204:207], v[160:163], v[56:59]\n"
    "v_mfma_f32_16x16x32_bf16 v[56:59], a[220:223], v[164:167], v[56:59]\n"
    "ds_read_b64_tr_b16 v[156:157], v180 offset:12288\n"
    "v_mfma_f32_16x16x32_bf16 v[56:59], a[236:239], v[168:171], v[56:59]\n"
    "buffer_load_dwordx4 a[96:99], v190, s[76:79], s70 offen offset:2048 sc1\n"
    "v_mfma_f32_16x16x32_bf16 v[56:59], a[252:255], v[172:175], v[56:59]\n"
    "ds_read_b64_tr_b16 v[158:159], v180 offset:12800\n"
    "v_mfma_f32_16x16x32_bf16 v[12:15], a[192:195], v[128:131], v[12:15]\n"
    "v_mfma_f32_16x16x32_bf16 v[12:15], a[208:211], v[132:135], v[12:15]\n"
    "ds_read_b64_tr_b16 v[160:161], v181 offset:0\n"
    "v_mfma_f32_16x16x32_bf16 v[12:15], a[224:227], v[136:139], v[12:15]\n"
    "v_mfma_f32_16x16x32_bf16 v[12:15], a[240:243], v[140:143], v[12:15]\n"
    "ds_read_b64_tr_b16 v[162:163], v181 offset:512\n"
    "v_mfma_f32_16x16x32_bf16 v[28:31], a[196:199], v[128:131], v[28:31]\n"
    "buffer_load_dwordx4 a[112:115], v190, s[76:79], s70 offen offset:3072 sc1\n"
    "v_mfma_f32_16x16x32_bf16 v[28:31], a[212:215], v[132:135], v[28:31]\n"
    "ds_read_b64_tr_b16 v[164:165], v181 offset:4096\n"
    "v_mfma_f32_16x16x32_bf16 v[28:31], a[228:231], v[136:139], v[28:31]\n"
    "v_mfma_f32_16x16x32_bf16 v[28:31], a[244:247], v[140:143], v[28:31]\n"
    "ds_read_b64_tr_b16 v[166:167], v181 offset:4608\n"
    "v_mfma_f32_16x16x32_bf16 v[44:47], a[200:203], v[128:131], v[44:47]\n"
    "v_mfma_f32_16x16x32_bf16 v[44:47], a[216:219], v[132:135], v[44:47]\n"
    "ds_read_b64_tr_b16 v[168:169], v181 offset:8192\n"
    "v_mfma_f32_16x16x32_bf16 v[44:47], a[232:235], v[136:139], v[44:47]\n"
    "v_mfma_f32_16x16x32_bf16 v[44:47], a[248:251], v[140:143], v[44:47]\n"
    "ds_read_b64_tr_b16 v[170:171], v181 offset:8704\n"
    "v_mfma_f32_16x16x32_bf16 v[60:63], a[204:207], v[128:131], v[60:63]\n"
    "s_add_u32 s70, s65, s89\n"
    "buffer_load_dwordx4 a[68:71], v190, s[76:79], s70 offen offset:0 sc1\n"
    "v_mfma_f32_16x16x32_bf16 v[60:63], a[220:223], v[132:135], v[60:63]\n"
    "ds_read_b64_tr_b16 v[172:173], v181 offset:12288\n"
    "v_mfma_f32_16x16x32_bf16 v[60:63], a[236:239], v[136:139], v[60:63]\n"
    "v_mfma_f32_16x16x32_bf16 v[60:63], a[252:255], v[140:143], v[60:63]\n"
    "ds_read_b64_tr_b16 v[174:175], v181 offset:12800\n"
    "s_waitcnt vmcnt(22) lgkmcnt(8)\n"
    "v_mfma_f32_16x16x32_bf16 v[64:67], v[144:147], a[0:3], 0\n"
    "v_mfma_f32_16x16x32_bf16 v[64:67], v[148:151], a[16:19], v[64:67]\n"
    "ds_read_b64_tr_b16 v[128:129], v182 offset:0\n"
    "v_cvt_pk_bf16_f32 v0, v0, v1\n"
    "v_mfma_f32_16x16x32_bf16 v[64:67], v[152:155], a[32:35], v[64:67]\n"
    "v_mfma_f32_16x16x32_bf16 v[64:67], v[156:159], a[48:51], v[64:67]\n"
    "ds_read_b64_tr_b16 v[130:131], v182 offset:512\n"
    "v_cvt_pk_bf16_f32 v1, v2, v3\n"
    "s_waitcnt vmcnt(17)\n"
    "v_mfma_f32_16x16x32_bf16 v[80:83], v[144:147], a[4:7], 0\n"
    "buffer_load_dwordx4 a[84:87], v190, s[76:79], s70 offen offset:1024 sc1\n"
    "v_mfma_f32_16x16x32_bf16 v[80:83], v[148:151], a[20:23], v[80:83]\n"
    "ds_read_b64_tr_b16 v[132:133], v182 offset:4096\n"
    "v_cvt_pk_bf16_f32 v2, v16, v17\n"
    "v_mfma_f32_16x16x32_bf16 v[80:83], v[152:155], a[36:39], v[80:83]\n"
    "v_mfma_f32_16x16x32_bf16 v[80:83], v[156:159], a[52:55], v[80:83]\n"
    "ds_read_b64_tr_b16 v[134:135], v182 offset:4608\n"
    "v_cvt_pk_bf16_f32 v3, v18, v19\n"
    "s_waitcnt vmcnt(12)\n"
    "v_mfma_f32_16x16x32_bf16 v[96:99], v[144:147], a[8:11], 0\n"
    "s_add_u32 s69, s58, 532480\n"
    "buffer_store_dwordx4 v[0:3], v190, s[44:47], s69 offen sc1\n"
    "v_mfma_f32_16x16x32_bf16 v[96:99], v[148:151], a[24:27], v[96:99]\n"
    "ds_read_b64_tr_b16 v[136:137], v182 offset:8192\n"
    "v_mfma_f32_16x16x32_bf16 v[96:99], v[152:155], a[40:43], v[96:99]\n"
    "v_mfma_f32_16x16x32_bf16 v[96:99], v[156:159], a[56:59], v[96:99]\n"
    "ds_read_b64_tr_b16 v[138:139], v182 offset:8704\n"
    "s_waitcnt vmcnt(8)\n"
    "v_mfma_f32_16x16x32_bf16 v[112:115], v[144:147], a[12:15], 0\n"
    "buffer_load_dwordx4 a[100:103], v190, s[76:79], s70 offen offset:2048 sc1\n"
    "v_mfma_f32_16x16x32_bf16 v[112:115], v[148:151], a[28:31], v[112:115]\n"
    "ds_read_b64_tr_b16 v[140:141], v182 offset:12288\n"
    "v_mfma_f32_16x16x32_bf16 v[112:115], v[152:155], a[44:47], v[112:115]\n"
    "v_mfma_f32_16x16x32_bf16 v[112:115], v[156:159], a[60:63], v[112:115]\n"
    "ds_read_b64_tr_b16 v[142:143], v182 offset:12800\n"
    "s_waitcnt lgkmcnt(8)\n"
    "v_mfma_f32_16x16x32_bf16 v[68:71], v[160:163], a[0:3], 0\n"
    "v_mfma_f32_16x16x32_bf16 v[68:71], v[164:167], a[16:19], v[68:71]\n"
    "ds_read_b64_tr_b16 v[144:145], v183 offset:0\n"
    "v_mfma_f32_16x16x32_bf16 v[68:71], v[168:171], a[32:35], v[68:71]\n"
    "buffer_load_dwordx4 a[116:119], v190, s[76:79], s70 offen offset:3072 sc1\n"
    "v_mfma_f32_16x16x32_bf16 v[68:71], v[172:175], a[48:51], v[68:71]\n"
    "ds_read_b64_tr_b16 v[146:147], v183 offset:512\n"
    "v_mfma_f32_16x16x32_bf16 v[84:87], v[160:163], a[4:7], 0\n"
    "v_mfma_f32_16x16x32_bf16 v[84:87], v[164:167], a[20:23], v[84:87]\n"
    "ds_read_b64_tr_b16 v[148:149], v183 offset:4096\n"
    "v_mfma_f32_16x16x32_bf16 v[84:87], v[168:171], a[36:39], v[84:87]\n"
    "v_mfma_f32_16x16x32_bf16 v[84:87], v[172:175], a[52:55], v[84:87]\n"
    "ds_read_b64_tr_b16 v[150:151], v183 offset:4608\n"
    "v_cvt_pk_bf16_f32 v32, v32, v33\n"
    "v_mfma_f32_16x16x32_bf16 v[100:103], v[160:163], a[8:11], 0\n"
    "v_mfma_f32_16x16x32_bf16 v[100:103], v[164:167], a[24:27], v[100:103]\n"
    "ds_read_b64_tr_b16 v[152:153], v183 offset:8192\n"
    "v_cvt_pk_bf16_f32 v33, v34, v35\n"
    "v_mfma_f32_16x16x32_bf16 v[100:103], v[168:171], a[40:43], v[100:103]\n"
    "s_add_u32 s70, s66, s89\n"
    "buffer_load_dwordx4 a[72:75], v190, s[76:79], s70 offen offset:0 sc1\n"
    "v_mfma_f32_16x16x32_bf16 v[100:103], v[172:175], a[56:59], v[100:103]\n"
    "ds_read_b64_tr_b16 v[154:155], v183 offset:8704\n"
    "v_cvt_pk_bf16_f32 v34, v48, v49\n"
    "v_mfma_f32_16x16x32_bf16 v[116:119], v[160:163], a[12:15], 0\n"
    "v_mfma_f32_16x16x32_bf16 v[116:119], v[164:167], a[28:31], v[116:119]\n"
    "ds_read_b64_tr_b16 v[156:157], v183 offset:12288\n"
    "v_cvt_pk_bf16_f32 v35, v50, v51\n"
    "v_mfma_f32_16x16x32_bf16 v[116:119], v[168:171], a[44:47], v[116:119]\n"
    "s_add_u32 s69, s58, 533504\n"
    "buffer_store_dwordx4 v[32:35], v190, s[44:47], s69 offen sc1\n"
    "v_mfma_f32_16x16x32_bf16 v[116:119], v[172:175], a[60:63], v[116:119]\n"
    "ds_read_b64_tr_b16 v[158:159], v183 offset:12800\n"
    "s_waitcnt lgkmcnt(8)\n"
    "v_mfma_f32_16x16x32_bf16 v[72:75], v[128:131], a[0:3], 0\n"
    "v_mfma_f32_16x16x32_bf16 v[72:75], v[132:135], a[16:19], v[72:75]\n"
    "ds_read_b64_tr_b16 v[160:161], v180 offset:16384\n"
    "v_mfma_f32_16x16x32_bf16 v[72:75], v[136:139], a[32:35], v[72:75]\n"
    "buffer_load_dwordx4 a[88:91], v190, s[76:79], s70 offen offset:1024 sc1\n"
    "v_mfma_f32_16x16x32_bf16 v[72:75], v[140:143], a[48:51], v[72:75]\n"
    "ds_read_b64_tr_b16 v[162:163], v180 offset:16896\n"
    "v_mfma_f32_16x16x32_bf16 v[88:91], v[128:131], a[4:7], 0\n"
    "v_mfma_f32_16x16x32_bf16 v[88:91], v[132:135], a[20:23], v[88:91]\n"
    "ds_read_b64_tr_b16 v[164:165], v180 offset:20480\n"
    "v_mfma_f32_16x16x32_bf16 v[88:91], v[136:139], a[36:39], v[88:91]\n"
    "v_mfma_f32_16x16x32_bf16 v[88:91], v[140:143], a[52:55], v[88:91]\n"
    "ds_read_b64_tr_b16 v[166:167], v180 offset:20992\n"
    "v_mfma_f32_16x16x32_bf16 v[104:107], v[128:131], a[8:11], 0\n"
    "v_mfma_f32_16x16x32_bf16 v[104:107], v[132:135], a[24:27], v[104:107]\n"
    "ds_read_b64_tr_b16 v[168:169], v180 offset:24576\n"
    "v_mfma_f32_16x16x32_bf16 v[104:107], v[136:139], a[40:43], v[104:107]\n"
    "buffer_load_dwordx4 a[104:107], v190, s[76:79], s70 offen offset:2048 sc1\n"
    "v_mfma_f32_16x16x32_bf16 v[104:107], v[140:143], a[56:59], v[104:107]\n"
    "ds_read_b64_tr_b16 v[170:171], v180 offset:25088\n"
    "v_mfma_f32_16x16x32_bf16 v[120:123], v[128:131], a[12:15], 0\n"
    "v_mfma_f32_16x16x32_bf16 v[120:123], v[132:135], a[28:31], v[120:123]\n"
    "ds_read_b64_tr_b16 v[172:173], v180 offset:28672\n"
    "v_cvt_pk_bf16_f32 v4, v4, v5\n"
    "v_mfma_f32_16x16x32_bf16 v[120:123], v[136:139], a[44:47], v[120:123]\n"
    "v_mfma_f32_16x16x32_bf16 v[120:123], v[140:143], a[60:63], v[120:123]\n"
    "ds_read_b64_tr_b16 v[174:175], v180 offset:29184\n"
    "v_cvt_pk_bf16_f32 v5, v6, v7\n"
    "s_waitcnt lgkmcnt(8)\n"
    "v_mfma_f32_16x16x32_bf16 v[76:79], v[144:147], a[0:3], 0\n"
    "buffer_load_dwordx4 a[120:123], v190, s[76:79], s70 offen offset:3072 sc1\n"
    "v_mfma_f32_16x16x32_bf16 v[76:79], v[148:151], a[16:19], v[76:79]\n"
    "ds_read_b64_tr_b16 v[128:129], v181 offset:16384\n"
    "v_cvt_pk_bf16_f32 v6, v20, v21\n"
    "v_mfma_f32_16x16x32_bf16 v[76:79], v[152:155], a[32:35], v[76:79]\n"
    "v_mfma_f32_16x16x32_bf16 v[76:79], v[156:159], a[48:51], v[76:79]\n"
    "ds_read_b64_tr_b16 v[130:131], v181 offset:16896\n"
    "v_cvt_pk_bf16_f32 v7, v22, v23\n"
    "v_mfma_f32_16x16x32_bf16 v[92:95], v[144:147], a[4:7], 0\n"
    "s_add_u32 s69, s58, 534528\n"
    "buffer_store_dwordx4 v[4:7], v190, s[44:47], s69 offen sc1\n"
    "v_mfma_f32_16x16x32_bf16 v[92:95], v[148:151], a[20:23], v[92:95]\n"
    "ds_read_b64_tr_b16 v[132:133], v181 offset:20480\n"
    "v_mfma_f32_16x16x32_bf16 v[92:95], v[152:155], a[36:39], v[92:95]\n"
    "v_mfma_f32_16x16x32_bf16 v[92:95], v[156:159], a[52:55], v[92:95]\n"
    "ds_read_b64_tr_b16 v[134:135], v181 offset:20992\n"
    "v_mfma_f32_16x16x32_bf16 v[108:111], v[144:147], a[8:11], 0\n"
    "s_add_u32 s70, s67, s89\n"
    "buffer_load_dwordx4 a[76:79], v190, s[76:79], s70 offen offset:0 sc1\n"
    "v_mfma_f32_16x16x32_bf16 v[108:111], v[148:151], a[24:27], v[108:111]\n"
    "ds_read_b64_tr_b16 v[136:137], v181 offset:24576\n"
    "v_mfma_f32_16x16x32_bf16 v[108:111], v[152:155], a[40:43], v[108:111]\n"
    "v_mfma_f32_16x16x32_bf16 v[108:111], v[156:159], a[56:59], v[108:111]\n"
    "ds_read_b64_tr_b16 v[138:139], v181 offset:25088\n"
    "v_mfma_f32_16x16x32_bf16 v[124:127], v[144:147], a[12:15], 0\n"
    "v_mfma_f32_16x16x32_bf16 v[124:127], v[148:151], a[28:31], v[124:127]\n"
    "ds_read_b64_tr_b16 v[140:141], v181 offset:28672\n"
    "v_mfma_f32_16x16x32_bf16 v[124:127], v[152:155], a[44:47], v[124:127]\n"
    "v_mfma_f32_16x16x32_bf16 v[124:127], v[156:159], a[60:63], v[124:127]\n"
    "ds_read_b64_tr_b16 v[142:143], v181 offset:29184\n"
    "s_waitcnt lgkmcnt(8)\n"
    "v_mfma_f32_16x16x32_bf16 v[192:195], v[160:163], a[0:3], 0\n"
    "buffer_load_dwordx4 a[92:95], v190, s[76:79], s70 offen offset:1024 sc1\n"
    "v_mfma_f32_16x16x32_bf16 v[192:195], v[164:167], a[16:19], v[192:195]\n"
    "ds_read_b64_tr_b16 v[144:145], v182 offset:16384\n"
    "v_mfma_f32_16x16x32_bf16 v[192:195], v[168:171], a[32:35], v[192:195]\n"
    "v_mfma_f32_16x16x32_bf16 v[192:195], v[172:175], a[48:51], v[192:195]\n"
    "ds_read_b64_tr_b16 v[146:147], v182 offset:16896\n"
    "v_cvt_pk_bf16_f32 v36, v36, v37\n"
    "v_mfma_f32_16x16x32_bf16 v[208:211], v[160:163], a[4:7], 0\n"
    "v_mfma_f32_16x16x32_bf16 v[208:211], v[164:167], a[20:23], v[208:211]\n"
    "ds_read_b64_tr_b16 v[148:149], v182 offset:20480\n"
    "v_cvt_pk_bf16_f32 v37, v38, v39\n"
    "v_mfma_f32_16x16x32_bf16 v[208:211], v[168:171], a[36:39], v[208:211]\n"
    "v_mfma_f32_16x16x32_bf16 v[208:211], v[172:175], a[52:55], v[208:211]\n"
    "ds_read_b64_tr_b16 v[150:151], v182 offset:20992\n"
    "v_cvt_pk_bf16_f32 v38, v52, v53\n"
    "v_mfma_f32_16x16x32_bf16 v[224:227], v[160:163], a[8:11], 0\n"
    "buffer_load_dwordx4 a[108:111], v190, s[76:79], s70 offen offset:2048 sc1\n"
    "v_mfma_f32_16x16x32_bf16 v[224:227], v[164:167], a[24:27], v[224:227]\n"
    "ds_read_b64_tr_b16 v[152:153], v182 offset:24576\n"
    "v_cvt_pk_bf16_f32 v39, v54, v55\n"
    "v_mfma_f32_16x16x32_bf16 v[224:227], v[168:171], a[40:43], v[224:227]\n"
    "s_add_u32 s69, s58, 535552\n"
    "buffer_store_dwordx4 v[36:39], v190, s[44:47], s69 offen sc1\n"
    "v_mfma_f32_16x16x32_bf16 v[224:227], v[172:175], a[56:59], v[224:227]\n"
    "ds_read_b64_tr_b16 v[154:155], v182 offset:25088\n"
    "v_mfma_f32_16x16x32_bf16 v[240:243], v[160:163], a[12:15], 0\n"
    "v_mfma_f32_16x16x32_bf16 v[240:243], v[164:167], a[28:31], v[240:243]\n"
    "ds_read_b64_tr_b16 v[156:157], v182 offset:28672\n"
    "v_mfma_f32_16x16x32_bf16 v[240:243], v[168:171], a[44:47], v[240:243]\n"
    "buffer_load_dwordx4 a[124:127], v190, s[76:79], s70 offen offset:3072 sc1\n"
    "v_mfma_f32_16x16x32_bf16 v[240:243], v[172:175], a[60:63], v[240:243]\n"
    "ds_read_b64_tr_b16 v[158:159], v182 offset:29184\n"
    "s_waitcnt lgkmcnt(8)\n"
    "v_mfma_f32_16x16x32_bf16 v[196:199], v[128:131], a[0:3], 0\n"
    "v_mfma_f32_16x16x32_bf16 v[196:199], v[132:135], a[16:19], v[196:199]\n"
    "ds_read_b64_tr_b16 v[160:161], v183 offset:16384\n"
    "v_mfma_f32_16x16x32_bf16 v[196:199], v[136:139], a[32:35], v[196:199]\n"
    "v_mfma_f32_16x16x32_bf16 v[196:199], v[140:143], a[48:51], v[196:199]\n"
    "ds_read_b64_tr_b16 v[162:163], v183 offset:16896\n"
    "v_mfma_f32_16x16x32_bf16 v[212:215], v[128:131], a[4:7], 0\n"
    "v_mfma_f32_16x16x32_bf16 v[212:215], v[132:135], a[20:23], v[212:215]\n"
    "ds_read_b64_tr_b16 v[164:165], v183 offset:20480\n"
    "v_mfma_f32_16x16x32_bf16 v[212:215], v[136:139], a[36:39], v[212:215]\n"
    "s_add_u32 s70, s64, s90\n"
    "buffer_load_dwordx4 a[128:131], v190, s[76:79], s70 offen offset:0 sc1\n"
    "v_mfma_f32_16x16x32_bf16 v[212:215], v[140:143], a[52:55], v[212:215]\n"
    "ds_read_b64_tr_b16 v[166:167], v183 offset:20992\n"
    "v_mfma_f32_16x16x32_bf16 v[228:231], v[128:131], a[8:11], 0\n"
    "v_mfma_f32_16x16x32_bf16 v[228:231], v[132:135], a[24:27], v[228:231]\n"
    "ds_read_b64_tr_b16 v[168:169], v183 offset:24576\n"
    "v_cvt_pk_bf16_f32 v8, v8, v9\n"
    "v_mfma_f32_16x16x32_bf16 v[228:231], v[136:139], a[40:43], v[228:231]\n"
    "v_mfma_f32_16x16x32_bf16 v[228:231], v[140:143], a[56:59], v[228:231]\n"
    "ds_read_b64_tr_b16 v[170:171], v183 offset:25088\n"
    "v_cvt_pk_bf16_f32 v9, v10, v11\n"
    "v_mfma_f32_16x16x32_bf16 v[244:247], v[128:131], a[12:15], 0\n"
    "v_mfma_f32_16x16x32_bf16 v[244:247], v[132:135], a[28:31], v[244:247]\n"
    "ds_read_b64_tr_b16 v[172:173], v183 offset:28672\n"
    "v_cvt_pk_bf16_f32 v10, v24, v25\n"
    "v_mfma_f32_16x16x32_bf16 v[244:247], v[136:139], a[44:47], v[244:247]\n"
    "buffer_load_dwordx4 a[144:147], v190, s[76:79], s70 offen offset:1024 sc1\n"
    "v_mfma_f32_16x16x32_bf16 v[244:247], v[140:143], a[60:63], v[244:247]\n"
    "s_add_u32 m0, s57, 0\n"
    "ds_read_b64_tr_b16 v[174:175], v183 offset:29184\n"
    "v_cvt_pk_bf16_f32 v11, v26, v27\n"
    "s_waitcnt lgkmcnt(8)\n"
    "v_mfma_f32_16x16x32_bf16 v[200:203], v[144:147], a[0:3], 0\n"
    "s_add_u32 s68, s82, 0\n"
    "buffer_load_dwordx4 v184, s[40:43], s68 offen offset:0 lds\n"
    "v_mfma_f32_16x16x32_bf16 v[200:203], v[148:151], a[16:19], v[200:203]\n"
    "ds_read_b64_tr_b16 v[128:129], v180 offset:32768\n"
    "v_mfma_f32_16x16x32_bf16 v[200:203], v[152:155], a[32:35], v[200:203]\n"
    "s_add_u32 s69, s58, 536576\n"
    "buffer_store_dwordx4 v[8:11], v190, s[44:47], s69 offen sc1\n"
    "v_mfma_f32_16x16x32_bf16 v[200:203], v[156:159], a[48:51], v[200:203]\n"
    "ds_read_b64_tr_b16 v[130:131], v180 offset:33280\n"
    "v_mfma_f32_16x16x32_bf16 v[216:219], v[144:147], a[4:7], 0\n"
    "v_mfma_f32_16x16x32_bf16 v[216:219], v[148:151], a[20:23], v[216:219]\n"
    "ds_read_b64_tr_b16 v[132:133], v180 offset:36864\n"
    "v_mfma_f32_16x16x32_bf16 v[216:219], v[152:155], a[36:39], v[216:219]\n"
    "buffer_load_dwordx4 a[160:163], v190, s[76:79], s70 offen offset:2048 sc1\n"
    "v_mfma_f32_16x16x32_bf16 v[216:219], v[156:159], a[52:55], v[216:219]\n"
    "ds_read_b64_tr_b16 v[134:135], v180 offset:37376\n"
    "v_mfma_f32_16x16x32_bf16 v[232:235], v[144:147], a[8:11], 0\n"
    "v_mfma_f32_16x16x32_bf16 v[232:235], v[148:151], a[24:27], v[232:235]\n"
    "ds_read_b64_tr_b16 v[136:137], v180 offset:40960\n"
    "v_mfma_f32_16x16x32_bf16 v[232:235], v[152:155], a[40:43], v[232:235]\n"
    "s_add_u32 s68, s82, 64512\n"
    "buffer_load_dwordx4 v185, s[40:43], s68 offen offset:1024 lds\n"
    "v_mfma_f32_16x16x32_bf16 v[232:235], v[156:159], a[56:59], v[232:235]\n"
    "ds_read_b64_tr_b16 v[138:139], v180 offset:41472\n"
    "v_mfma_f32_16x16x32_bf16 v[248:251], v[144:147], a[12:15], 0\n"
    "buffer_load_dwordx4 a[176:179], v190, s[76:79], s70 offen offset:3072 sc1\n"
    "v_mfma_f32_16x16x32_bf16 v[248:251], v[148:151], a[28:31], v[248:251]\n"
    "ds_read_b64_tr_b16 v[140:141], v180 offset:45056\n"
    "v_mfma_f32_16x16x32_bf16 v[248:251], v[152:155], a[44:47], v[248:251]\n"
    "v_mfma_f32_16x16x32_bf16 v[248:251], v[156:159], a[60:63], v[248:251]\n"
    "ds_read_b64_tr_b16 v[142:143], v180 offset:45568\n"
    "v_cvt_pk_bf16_f32 v40, v40, v41\n"
    "s_waitcnt lgkmcnt(8)\n"
    "v_mfma_f32_16x16x32_bf16 v[204:207], v[160:163], a[0:3], 0\n"
    "v_mfma_f32_16x16x32_bf16 v[204:207], v[164:167], a[16:19], v[204:207]\n"
    "ds_read_b64_tr_b16 v[144:145], v181 offset:32768\n"
    "v_cvt_pk_bf16_f32 v41, v42, v43\n"
    "v_mfma_f32_16x16x32_bf16 v[204:207], v[168:171], a[32:35], v[204:207]\n"
    "v_mfma_f32_16x16x32_bf16 v[204:207], v[172:175], a[48:51], v[204:207]\n"
    "ds_read_b64_tr_b16 v[146:147], v181 offset:33280\n"
    "v_cvt_pk_bf16_f32 v42, v56, v57\n"
    "v_mfma_f32_16x16x32_bf16 v[220:223], v[160:163], a[4:7], 0\n"
    "s_add_u32 s68, s82, 129024\n"
    "buffer_load_dwordx4 v184, s[40:43], s68 offen offset:2048 lds\n"
    "v_mfma_f32_16x16x32_bf16 v[220:223], v[164:167], a[20:23], v[220:223]\n"
    "ds_read_b64_tr_b16 v[148:149], v181 offset:36864\n"
    "v_cvt_pk_bf16_f32 v43, v58, v59\n"
    "v_mfma_f32_16x16x32_bf16 v[220:223], v[168:171], a[36:39], v[220:223]\n"
    "s_add_u32 s70, s65, s90\n"
    "buffer_load_dwordx4 a[132:135], v190, s[76:79], s70 offen offset:0 sc1\n"
    "v_mfma_f32_16x16x32_bf16 v[220:223], v[172:175], a[52:55], v[220:223]\n"
    "ds_read_b64_tr_b16 v[150:151], v181 offset:37376\n"
    "v_mfma_f32_16x16x32_bf16 v[236:239], v[160:163], a[8:11], 0\n"
    "s_add_u32 s69, s58, 537600\n"
    "buffer_store_dwordx4 v[40:43], v190, s[44:47], s69 offen sc1\n"
    "v_mfma_f32_16x16x32_bf16 v[236:239], v[164:167], a[24:27], v[236:239]\n"
    "ds_read_b64_tr_b16 v[152:153], v181 offset:40960\n"
    "v_mfma_f32_16x16x32_bf16 v[236:239], v[168:171], a[40:43], v[236:239]\n"
    "v_mfma_f32_16x16x32_bf16 v[236:239], v[172:175], a[56:59], v[236:239]\n"
    "ds_read_b64_tr_b16 v[154:155], v181 offset:41472\n"
    "v_mfma_f32_16x16x32_bf16 v[252:255], v[160:163], a[12:15], 0\n"
    "buffer_load_dwordx4 a[148:151], v190, s[76:79], s70 offen offset:1024 sc1\n"
    "v_mfma_f32_16x16x32_bf16 v[252:255], v[164:167], a[28:31], v[252:255]\n"
    "ds_read_b64_tr_b16 v[156:157], v181 offset:45056\n"
    "v_mfma_f32_16x16x32_bf16 v[252:255], v[168:171], a[44:47], v[252:255]\n"
    "s_add_u32 s68, s82, 193536\n"
    "buffer_load_dwordx4 v185, s[40:43], s68 offen offset:3072 lds\n"
    "v_mfma_f32_16x16x32_bf16 v[252:255], v[172:175], a[60:63], v[252:255]\n"
    "ds_read_b64_tr_b16 v[158:159], v181 offset:45568\n"
    "s_waitcnt vmcnt(28) lgkmcnt(8)\n"
    "v_mfma_f32_16x16x32_bf16 v[64:67], v[128:131], a[64:67], v[64:67]\n"
    "v_mfma_f32_16x16x32_bf16 v[64:67], v[132:135], a[80:83], v[64:67]\n"
    "ds_read_b64_tr_b16 v[160:161], v182 offset:32768\n"
    "v_accvgpr_write_b32 a0, 0\n"
    "v_mfma_f32_16x16x32_bf16 v[64:67], v[136:139], a[96:99], v[64:67]\n"
    "v_mfma_f32_16x16x32_bf16 v[64:67], v[140:143], a[112:115], v[64:67]\n"
    "ds_read_b64_tr_b16 v[162:163], v182 offset:33280\n"
    "v_accvgpr_write_b32 a1, 0\n"
    "s_waitcnt vmcnt(23)\n"
    "v_mfma_f32_16x16x32_bf16 v[80:83], v[128:131], a[68:71], v[80:83]\n"
    "buffer_load_dwordx4 a[164:167], v190, s[76:79], s70 offen offset:2048 sc1\n"
    "v_mfma_f32_16x16x32_bf16 v[80:83], v[132:135], a[84:87], v[80:83]\n"
    "ds_read_b64_tr_b16 v[164:165], v182 offset:36864\n"
    "v_cvt_pk_bf16_f32 v12, v12, v13\n"
    "v_mfma_f32_16x16x32_bf16 v[80:83], v[136:139], a[100:103], v[80:83]\n"
    "v_mfma_f32_16x16x32_bf16 v[80:83], v[140:143], a[116:119], v[80:83]\n"
    "s_add_u32 m0, s57, 16384\n"
    "ds_read_b64_tr_b16 v[166:167], v182 offset:37376\n"
    "v_cvt_pk_bf16_f32 v13, v14, v15\n"
    "s_waitcnt vmcnt(19)\n"
    "v_mfma_f32_16x16x32_bf16 v[96:99], v[128:131], a[72:75], v[96:99]\n"
    "s_add_u32 s68, s82, 128\n"
    "buffer_load_dwordx4 v184, s[40:43], s68 offen offset:0 lds\n"
    "v_mfma_f32_16x16x32_bf16 v[96:99], v[132:135], a[88:91], v[96:99]\n"
    "ds_read_b64_tr_b16 v[168:169], v182 offset:40960\n"
    "v_cvt_pk_bf16_f32 v14, v28, v29\n"
    "v_mfma_f32_16x16x32_bf16 v[96:99], v[136:139], a[104:107], v[96:99]\n"
    "buffer_load_dwordx4 a[180:183], v190, s[76:79], s70 offen offset:3072 sc1\n"
    "v_mfma_f32_16x16x32_bf16 v[96:99], v[140:143], a[120:123], v[96:99]\n"
    "ds_read_b64_tr_b16 v[170:171], v182 offset:41472\n"
    "v_cvt_pk_bf16_f32 v15, v30, v31\n"
    "s_waitcnt vmcnt(15)\n"
    "v_mfma_f32_16x16x32_bf16 v[112:115], v[128:131], a[76:79], v[112:115]\n"
    "s_add_u32 s69, s58, 538624\n"
    "buffer_store_dwordx4 v[12:15], v190, s[44:47], s69 offen sc1\n"
    "v_mfma_f32_16x16x32_bf16 v[112:115], v[132:135], a[92:95], v[112:115]\n"
    "ds_read_b64_tr_b16 v[172:173], v182 offset:45056\n"
    "v_accvgpr_write_b32 a2, 0\n"
    "v_mfma_f32_16x16x32_bf16 v[112:115], v[136:139], a[108:111], v[112:115]\n"
    "v_mfma_f32_16x16x32_bf16 v[112:115], v[140:143], a[124:127], v[112:115]\n"
    "ds_read_b64_tr_b16 v[174:175], v182 offset:45568\n"
    "v_accvgpr_write_b32 a3, 0\n"
    "s_waitcnt lgkmcnt(8)\n"
    "v_mfma_f32_16x16x32_bf16 v[68:71], v[144:147], a[64:67], v[68:71]\n"
    "v_mfma_f32_16x16x32_bf16 v[68:71], v[148:151], a[80:83], v[68:71]\n"
    "ds_read_b64_tr_b16 v[128:129], v183 offset:32768\n"
    "v_accvgpr_write_b32 a4, 0\n"
    "v_mfma_f32_16x16x32_bf16 v[68:71], v[152:155], a[96:99], v[68:71]\n"
    "s_add_u32 s68, s82, 64640\n"
    "buffer_load_dwordx4 v185, s[40:43], s68 offen offset:1024 lds\n"
    "v_mfma_f32_16x16x32_bf16 v[68:71], v[156:159], a[112:115], v[68:71]\n"
    "ds_read_b64_tr_b16 v[130:131], v183 offset:33280\n"
    "v_accvgpr_write_b32 a5, 0\n"
    "v_mfma_f32_16x16x32_bf16 v[84:87], v[144:147], a[68:71], v[84:87]\n"
    "s_add_u32 s70, s66, s90\n"
    "buffer_load_dwordx4 a[136:139], v190, s[76:79], s70 offen offset:0 sc1\n"
    "v_mfma_f32_16x16x32_bf16 v[84:87], v[148:151], a[84:87], v[84:87]\n"
    "ds_read_b64_tr_b16 v[132:133], v183 offset:36864\n"
    "v_accvgpr_write_b32 a6, 0\n"
    "v_mfma_f32_16x16x32_bf16 v[84:87], v[152:155], a[100:103], v[84:87]\n"
    "v_mfma_f32_16x16x32_bf16 v[84:87], v[156:159], a[116:119], v[84:87]\n"
    "ds_read_b64_tr_b16 v[134:135], v183 offset:37376\n"
    "v_accvgpr_write_b32 a7, 0\n"
    "v_mfma_f32_16x16x32_bf16 v[100:103], v[144:147], a[72:75], v[100:103]\n"
    "v_mfma_f32_16x16x32_bf16 v[100:103], v[148:151], a[88:91], v[100:103]\n"
    "ds_read_b64_tr_b16 v[136:137], v183 offset:40960\n"
    "v_accvgpr_write_b32 a8, 0\n"
    "v_mfma_f32_16x16x32_bf16 v[100:103], v[152:155], a[104:107], v[100:103]\n"
    "buffer_load_dwordx4 a[152:155], v190, s[76:79], s70 offen offset:1024 sc1\n"
    "v_mfma_f32_16x16x32_bf16 v[100:103], v[156:159], a[120:123], v[100:103]\n"
    "ds_read_b64_tr_b16 v[138:139], v183 offset:41472\n"
    "v_cvt_pk_bf16_f32 v44, v44, v45\n"
    "v_mfma_f32_16x16x32_bf16 v[116:119], v[144:147], a[76:79], v[116:119]\n"
    "s_add_u32 s68, s82, 129152\n"
    "buffer_load_dwordx4 v184, s[40:43], s68 offen offset:2048 lds\n"
    "v_mfma_f32_16x16x32_bf16 v[116:119], v[148:151], a[92:95], v[116:119]\n"
    "ds_read_b64_tr_b16 v[140:141], v183 offset:45056\n"
    "v_cvt_pk_bf16_f32 v45, v46, v47\n"
    "v_mfma_f32_16x16x32_bf16 v[116:119], v[152:155], a[108:111], v[116:119]\n"
    "v_mfma_f32_16x16x32_bf16 v[116:119], v[156:159], a[124:127], v[116:119]\n"
    "ds_read_b64_tr_b16 v[142:143], v183 offset:45568\n"
    "v_cvt_pk_bf16_f32 v46, v60, v61\n"
    "s_waitcnt lgkmcnt(8)\n"
    "v_mfma_f32_16x16x32_bf16 v[72:75], v[160:163], a[64:67], v[72:75]\n"
    "v_mfma_f32_16x16x32_bf16 v[72:75], v[164:167], a[80:83], v[72:75]\n"
    "ds_read_b64_tr_b16 v[144:145], v180 offset:49152\n"
    "v_cvt_pk_bf16_f32 v47, v62, v63\n"
    "v_mfma_f32_16x16x32_bf16 v[72:75], v[168:171], a[96:99], v[72:75]\n"
    "buffer_load_dwordx4 a[168:171], v190, s[76:79], s70 offen offset:2048 sc1\n"
    "v_mfma_f32_16x16x32_bf16 v[72:75], v[172:175], a[112:115], v[72:75]\n"
    "ds_read_b64_tr_b16 v[146:147], v180 offset:49664\n"
    "v_accvgpr_write_b32 a9, 0\n"
    "v_mfma_f32_16x16x32_bf16 v[88:91], v[160:163], a[68:71], v[88:91]\n"
    "s_add_u32 s69, s58, 539648\n"
    "buffer_store_dwordx4 v[44:47], v190, s[44:47], s69 offen sc1\n"
    "v_mfma_f32_16x16x32_bf16 v[88:91], v[164:167], a[84:87], v[88:91]\n"
    "ds_read_b64_tr_b16 v[148:149], v180 offset:53248\n"
    "v_accvgpr_write_b32 a10, 0\n"
    "v_mfma_f32_16x16x32_bf16 v[88:91], v[168:171], a[100:103], v[88:91]\n"
    "s_add_u32 s68, s82, 193664\n"
    "buffer_load_dwordx4 v185, s[40:43], s68 offen offset:3072 lds\n"
    "v_mfma_f32_16x16x32_bf16 v[88:91], v[172:175], a[116:119], v[88:91]\n"
    "ds_read_b64_tr_b16 v[150:151], v180 offset:53760\n"
    "v_accvgpr_write_b32 a11, 0\n"
    "v_mfma_f32_16x16x32_bf16 v[104:107], v[160:163], a[72:75], v[104:107]\n"
    "buffer_load_dwordx4 a[184:187], v190, s[76:79], s70 offen offset:3072 sc1\n"
    "v_mfma_f32_16x16x32_bf16 v[104:107], v[164:167], a[88:91], v[104:107]\n"
    "ds_read_b64_tr_b16 v[152:153], v180 offset:57344\n"
    "v_accvgpr_write_b32 a12, 0\n"
    "v_mfma_f32_16x16x32_bf16 v[104:107], v[168:171], a[104:107], v[104:107]\n"
    "v_mfma_f32_16x16x32_bf16 v[104:107], v[172:175], a[120:123], v[104:107]\n"
    "ds_read_b64_tr_b16 v[154:155], v180 offset:57856\n"
    "v_accvgpr_write_b32 a13, 0\n"
    "v_mfma_f32_16x16x32_bf16 v[120:123], v[160:163], a[76:79], v[120:123]\n"
    "v_mfma_f32_16x16x32_bf16 v[120:123], v[164:167], a[92:95], v[120:123]\n"
    "ds_read_b64_tr_b16 v[156:157], v180 offset:61440\n"
    "v_accvgpr_write_b32 a14, 0\n"
    "v_mfma_f32_16x16x32_bf16 v[120:123], v[168:171], a[108:111], v[120:123]\n"
    "v_mfma_f32_16x16x32_bf16 v[120:123], v[172:175], a[124:127], v[120:123]\n"
    "ds_read_b64_tr_b16 v[158:159], v180 offset:61952\n"
    "v_accvgpr_write_b32 a15, 0\n"
    "s_waitcnt lgkmcnt(8)\n"
    "v_mfma_f32_16x16x32_bf16 v[76:79], v[128:131], a[64:67], v[76:79]\n"
    "s_add_u32 s70, s67, s90\n"
    "buffer_load_dwordx4 a[140:143], v190, s[76:79], s70 offen offset:0 sc1\n"
    "v_mfma_f32_16x16x32_bf16 v[76:79], v[132:135], a[80:83], v[76:79]\n"
    "ds_read_b64_tr_b16 v[160:161], v181 offset:49152\n"
    "v_accvgpr_write_b32 a16, 0\n"
    "v_mfma_f32_16x16x32_bf16 v[76:79], v[136:139], a[96:99], v[76:79]\n"
    "v_mfma_f32_16x16x32_bf16 v[76:79], v[140:143], a[112:115], v[76:79]\n"
    "ds_read_b64_tr_b16 v[162:163], v181 offset:49664\n"
    "v_accvgpr_write_b32 a17, 0\n"
    "v_mfma_f32_16x16x32_bf16 v[92:95], v[128:131], a[68:71], v[92:95]\n"
    "v_mfma_f32_16x16x32_bf16 v[92:95], v[132:135], a[84:87], v[92:95]\n"
    "ds_read_b64_tr_b16 v[164:165], v181 offset:53248\n"
    "v_accvgpr_write_b32 a18, 0\n"
    ".p2align 12\n"
    "L_pad5:\n"
    "s_cbranch_execz L_pad9\n"
    "v_mfma_f32_16x16x32_bf16 v[92:95], v[136:139], a[100:103], v[92:95]\n"
    "v_mfma_f32_16x16x32_bf16 v[92:95], v[140:143], a[116:119], v[92:95]\n"
    "ds_read_b64_tr_b16 v[166:167], v181 offset:53760\n"
    "v_accvgpr_write_b32 a19, 0\n"
    "v_mfma_f32_16x16x32_bf16 v[108:111], v[128:131], a[72:75], v[108:111]\n"
    "buffer_load_dwordx4 a[156:159], v190, s[76:79], s70 offen offset:1024 sc1\n"
    "v_mfma_f32_16x16x32_bf16 v[108:111], v[132:135], a[88:91], v[108:111]\n"
    "ds_read_b64_tr_b16 v[168:169], v181 offset:57344\n"
    "v_accvgpr_write_b32 a20, 0\n"
    "v_mfma_f32_16x16x32_bf16 v[108:111], v[136:139], a[104:107], v[108:111]\n"
    "v_mfma_f32_16x16x32_bf16 v[108:111], v[140:143], a[120:123], v[108:111]\n"
    "ds_read_b64_tr_b16 v[170:171], v181 offset:57856\n"
    "v_accvgpr_write_b32 a21, 0\n"
    "v_mfma_f32_16x16x32_bf16 v[124:127], v[128:131], a[76:79], v[124:127]\n"
    "v_mfma_f32_16x16x32_bf16 v[124:127], v[132:135], a[92:95], v[124:127]\n"
    "ds_read_b64_tr_b16 v[172:173], v181 offset:61440\n"
    "v_accvgpr_write_b32 a22, 0\n"
    "v_mfma_f32_16x16x32_bf16 v[124:127], v[136:139], a[108:111], v[124:127]\n"
    "v_mfma_f32_16x16x32_bf16 v[124:127], v[140:143], a[124:127], v[124:127]\n"
    "ds_read_b64_tr_b16 v[174:175], v181 offset:61952\n"
    "v_accvgpr_write_b32 a23, 0\n"
    "s_waitcnt lgkmcnt(8)\n"
    "v_mfma_f32_16x16x32_bf16 v[192:195], v[144:147], a[64:67], v[192:195]\n"
    "s_and_b32 s20, s30, 7\n"
    "buffer_load_dwordx4 a[172:175], v190, s[76:79], s70 offen offset:2048 sc1\n"
    "v_mfma_f32_16x16x32_bf16 v[192:195], v[148:151], a[80:83], v[192:195]\n"
    "s_lshl_b32 s74, s20, 8\n"
    "ds_read_b64_tr_b16 v[128:129], v182 offset:49152\n"
    "v_accvgpr_write_b32 a24, 0\n"
    "v_mfma_f32_16x16x32_bf16 v[192:195], v[152:155], a[96:99], v[192:195]\n"
    "s_add_u32 s74, s74, 0x18400\n"
    "v_mfma_f32_16x16x32_bf16 v[192:195], v[156:159], a[112:115], v[192:195]\n"
    "ds_read_b64_tr_b16 v[130:131], v182 offset:49664\n"
    "v_lshrrev_b32 v48, 6, v190\n"
    "v_mfma_f32_16x16x32_bf16 v[208:211], v[144:147], a[68:71], v[208:211]\n"
    "v_bfe_u32 v49, v190, 2, 4\n"
    "v_accvgpr_write_b32 a25, 0\n"
    "v_mfma_f32_16x16x32_bf16 v[208:211], v[148:151], a[84:87], v[208:211]\n"
    "ds_read_b64_tr_b16 v[132:133], v182 offset:53248\n"
    "v_lshl_or_b32 v48, v48, 4, v49\n"
    "v_mfma_f32_16x16x32_bf16 v[208:211], v[152:155], a[100:103], v[208:211]\n"
    "buffer_load_dwordx4 a[188:191], v190, s[76:79], s70 offen offset:3072 sc1\n"
    "v_mfma_f32_16x16x32_bf16 v[208:211], v[156:159], a[116:119], v[208:211]\n"
    "ds_read_b64_tr_b16 v[134:135], v182 offset:53760\n"
    "v_accvgpr_write_b32 a26, 0\n"
    "v_mfma_f32_16x16x32_bf16 v[224:227], v[144:147], a[72:75], v[224:227]\n"
    "buffer_load_dword v49, v48, s[76:79], s74 offen sc1\n"
    "v_mfma_f32_16x16x32_bf16 v[224:227], v[148:151], a[88:91], v[224:227]\n"
    "s_lshl_b32 s75, s20, 10\n"
    "ds_read_b64_tr_b16 v[136:137], v182 offset:57344\n"
    "v_accvgpr_write_b32 a27, 0\n"
    "v_mfma_f32_16x16x32_bf16 v[224:227], v[152:155], a[104:107], v[224:227]\n"
    "v_mfma_f32_16x16x32_bf16 v[224:227], v[156:159], a[120:123], v[224:227]\n"
    "ds_read_b64_tr_b16 v[138:139], v182 offset:57856\n"
    "v_accvgpr_write_b32 a28, 0\n"
    "v_mfma_f32_16x16x32_bf16 v[240:243], v[144:147], a[76:79], v[240:243]\n"
    "v_mfma_f32_16x16x32_bf16 v[240:243], v[148:151], a[92:95], v[240:243]\n"
    "ds_read_b64_tr_b16 v[140:141], v182 offset:61440\n"
    "v_accvgpr_write_b32 a29, 0\n"
    "v_mfma_f32_16x16x32_bf16 v[240:243], v[152:155], a[108:111], v[240:243]\n"
    "s_add_u32 s70, s64, s91\n"
    "buffer_load_dwordx4 a[192:195], v190, s[76:79], s70 offen offset:0 sc1\n"
    "v_mfma_f32_16x16x32_bf16 v[240:243], v[156:159], a[124:127], v[240:243]\n"
    "ds_read_b64_tr_b16 v[142:143], v182 offset:61952\n"
    "v_accvgpr_write_b32 a30, 0\n"
    "s_waitcnt lgkmcnt(8)\n"
    "v_mfma_f32_16x16x32_bf16 v[196:199], v[160:163], a[64:67], v[196:199]\n"
    "ds_read_b64_tr_b16 v[144:145], v183 offset:49152\n"
    "v_accvgpr_write_b32 a31, 0\n"
    "v_mfma_f32_16x16x32_bf16 v[196:199], v[164:167], a[80:83], v[196:199]\n"
    "ds_read_b64_tr_b16 v[146:147], v183 offset:49664\n"
    "v_accvgpr_write_b32 a32, 0\n"
    "v_mfma_f32_16x16x32_bf16 v[196:199], v[168:171], a[96:99], v[196:199]\n"
    "ds_read_b64_tr_b16 v[148:149], v183 offset:53248\n"
    "v_accvgpr_write_b32 a33, 0\n"
    "v_mfma_f32_16x16x32_bf16 v[196:199], v[172:175], a[112:115], v[196:199]\n"
    "ds_read_b64_tr_b16 v[150:151], v183 offset:53760\n"
    "v_accvgpr_write_b32 a34, 0\n"
    "v_mfma_f32_16x16x32_bf16 v[212:215], v[160:163], a[68:71], v[212:215]\n"
    "ds_read_b64_tr_b16 v[152:153], v183 offset:57344\n"
    "v_accvgpr_write_b32 a35, 0\n"
    "v_mfma_f32_16x16x32_bf16 v[212:215], v[164:167], a[84:87], v[212:215]\n"
    "ds_read_b64_tr_b16 v[154:155], v183 offset:57856\n"
    "v_accvgpr_write_b32 a36, 0\n"
    "v_mfma_f32_16x16x32_bf16 v[212:215], v[168:171], a[100:103], v[212:215]\n"
    "ds_read_b64_tr_b16 v[156:157], v183 offset:61440\n"
    "v_accvgpr_write_b32 a37, 0\n"
    "v_mfma_f32_16x16x32_bf16 v[212:215], v[172:175], a[116:119], v[212:215]\n"
    "ds_read_b64_tr_b16 v[158:159], v183 offset:61952\n"
    "v_accvgpr_write_b32 a38, 0\n"
    "v_mfma_f32_16x16x32_bf16 v[228:231], v[160:163], a[72:75], v[228:231]\n"
    "buffer_load_dwordx4 a[208:211], v190, s[76:79], s70 offen offset:1024 sc1\n"
    "v_mfma_f32_16x16x32_bf16 v[228:231], v[164:167], a[88:91], v[228:231]\n"
    "v_mfma_f32_16x16x32_bf16 v[228:231], v[168:171], a[104:107], v[228:231]\n"
    "v_mfma_f32_16x16x32_bf16 v[228:231], v[172:175], a[120:123], v[228:231]\n"
    "v_mfma_f32_16x16x32_bf16 v[244:247], v[160:163], a[76:79], v[244:247]\n"
    "v_mfma_f32_16x16x32_bf16 v[244:247], v[164:167], a[92:95], v[244:247]\n"
    "buffer_load_dwordx4 a[224:227], v190, s[76:79], s70 offen offset:2048 sc1\n"
    "v_mfma_f32_16x16x32_bf16 v[244:247], v[168:171], a[108:111], v[244:247]\n"
    "v_mfma_f32_16x16x32_bf16 v[244:247], v[172:175], a[124:127], v[244:247]\n"
    "s_waitcnt lgkmcnt(8)\n"
    "s_waitcnt vmcnt(9) lgkmcnt(0)\n"
    "s_barrier\n"
    "v_mfma_f32_16x16x32_bf16 v[200:203], v[128:131], a[64:67], v[200:203]\n"
    "v_mfma_f32_16x16x32_bf16 v[200:203], v[132:135], a[80:83], v[200:203]\n"
    "s_add_u32 m0, s57, 32768\n"
    "ds_read_b64_tr_b16 v[160:161], v176 offset:0\n"
    "v_accvgpr_write_b32 a39, 0\n"
    "v_mfma_f32_16x16x32_bf16 v[200:203], v[136:139], a[96:99], v[200:203]\n"
    "s_add_u32 s68, s83, 0\n"
    "buffer_load_dwordx4 v184, s[40:43], s68 offen offset:0 lds\n"
    "v_mfma_f32_16x16x32_bf16 v[200:203], v[140:143], a[112:115], v[200:203]\n"
    "ds_read_b64_tr_b16 v[162:163], v176 offset:512\n"
    "v_accvgpr_write_b32 a40, 0\n"
    "v_mfma_f32_16x16x32_bf16 v[216:219], v[128:131], a[68:71], v[216:219]\n"
    "s_lshr_b32 s16, s60, 9\n"
    "buffer_load_dwordx4 a[240:243], v190, s[76:79], s70 offen offset:3072 sc1\n"
    "v_mfma_f32_16x16x32_bf16 v[216:219], v[132:135], a[84:87], v[216:219]\n"
    "ds_read_b64_tr_b16 v[164:165], v176 offset:4096\n"
    "v_mov_b32 v187, 1\n"
    "v_mfma_f32_16x16x32_bf16 v[216:219], v[136:139], a[100:103], v[216:219]\n"
    "v_mov_b32 v186, s16\n"
    "v_accvgpr_write_b32 a41, 0\n"
    "v_mfma_f32_16x16x32_bf16 v[216:219], v[140:143], a[116:119], v[216:219]\n"
    "ds_read_b64_tr_b16 v[166:167], v176 offset:4608\n"
    "v_accvgpr_write_b32 a42, 0\n"
    "v_mfma_f32_16x16x32_bf16 v[232:235], v[128:131], a[72:75], v[232:235]\n"
    "v_mfma_f32_16x16x32_bf16 v[232:235], v[132:135], a[88:91], v[232:235]\n"
    "ds_read_b64_tr_b16 v[168:169], v176 offset:8192\n"
    "v_accvgpr_write_b32 a43, 0\n"
    "v_mfma_f32_16x16x32_bf16 v[232:235], v[136:139], a[104:107], v[232:235]\n"
    "s_add_u32 s68, s83, 64512\n"
    "buffer_load_dwordx4 v185, s[40:43], s68 offen offset:1024 lds\n"
    "v_mfma_f32_16x16x32_bf16 v[232:235], v[140:143], a[120:123], v[232:235]\n"
    "ds_read_b64_tr_b16 v[170:171], v176 offset:8704\n"
    "v_accvgpr_write_b32 a44, 0\n"
    "v_mfma_f32_16x16x32_bf16 v[248:251], v[128:131], a[76:79], v[248:251]\n"
    "s_add_u32 s70, s65, s91\n"
    "buffer_load_dwordx4 a[196:199], v190, s[76:79], s70 offen offset:0 sc1\n"
    "v_mfma_f32_16x16x32_bf16 v[248:251], v[132:135], a[92:95], v[248:251]\n"
    "ds_read_b64_tr_b16 v[172:173], v176 offset:12288\n"
    "v_accvgpr_write_b32 a45, 0\n"
    "v_mfma_f32_16x16x32_bf16 v[248:251], v[136:139], a[108:111], v[248:251]\n"
    "s_mov_b64 exec, 1\n"
    "buffer_store_dword v187, v186, s[76:79], s12 offen sc1\n"
    "s_mov_b64 exec, -1\n"
    "v_mfma_f32_16x16x32_bf16 v[248:251], v[140:143], a[124:127], v[248:251]\n"
    "ds_read_b64_tr_b16 v[174:175], v176 offset:12800\n"
    "v_accvgpr_write_b32 a46, 0\n"
    "v_mfma_f32_16x16x32_bf16 v[204:207], v[144:147], a[64:67], v[204:207]\n"
    "v_mfma_f32_16x16x32_bf16 v[204:207], v[148:151], a[80:83], v[204:207]\n"
    "ds_read_b64_tr_b16 v[128:129], v177 offset:0\n"
    "v_accvgpr_write_b32 a47, 0\n"
    "v_mfma_f32_16x16x32_bf16 v[204:207], v[152:155], a[96:99], v[204:207]\n"
    "s_add_u32 s68, s83, 129024\n"
    "buffer_load_dwordx4 v184, s[40:43], s68 offen offset:2048 lds\n"
    "v_mfma_f32_16x16x32_bf16 v[204:207], v[156:159], a[112:115], v[204:207]\n"
    "ds_read_b64_tr_b16 v[130:131], v177 offset:512\n"
    "v_bfe_u32 v186, v190, 2, 7\n"
    "v_mfma_f32_16x16x32_bf16 v[220:223], v[144:147], a[68:71], v[220:223]\n"
    "buffer_load_dwordx4 a[212:215], v190, s[76:79], s70 offen offset:1024 sc1\n"
    "v_mfma_f32_16x16x32_bf16 v[220:223], v[148:151], a[84:87], v[220:223]\n"
    "ds_read_b64_tr_b16 v[132:133], v177 offset:4096\n"
    "v_lshlrev_b32 v186, 5, v186\n"
    "v_mfma_f32_16x16x32_bf16 v[220:223], v[152:155], a[100:103], v[220:223]\n"
    "v_mfma_f32_16x16x32_bf16 v[220:223], v[156:159], a[116:119], v[220:223]\n"
    "ds_read_b64_tr_b16 v[134:135], v177 offset:4608\n"
    "v_accvgpr_write_b32 a48, 0\n"
    "v_mfma_f32_16x16x32_bf16 v[236:239], v[144:147], a[72:75], v[236:239]\n"
    "v_mfma_f32_16x16x32_bf16 v[236:239], v[148:151], a[88:91], v[236:239]\n"
    "ds_read_b64_tr_b16 v[136:137], v177 offset:8192\n"
    "v_accvgpr_write_b32 a49, 0\n"
    "v_mfma_f32_16x16x32_bf16 v[236:239], v[152:155], a[104:107], v[236:239]\n"
    "s_add_u32 s68, s83, 193536\n"
    "buffer_load_dwordx4 v185, s[40:43], s68 offen offset:3072 lds\n"
    "v_mfma_f32_16x16x32_bf16 v[236:239], v[156:159], a[120:123], v[236:239]\n"
    "ds_read_b64_tr_b16 v[138:139], v177 offset:8704\n"
    "v_accvgpr_write_b32 a50, 0\n"
    "v_mfma_f32_16x16x32_bf16 v[252:255], v[144:147], a[76:79], v[252:255]\n"
    "buffer_load_dwordx4 a[228:231], v190, s[76:79], s70 offen offset:2048 sc1\n"
    "v_mfma_f32_16x16x32_bf16 v[252:255], v[148:151], a[92:95], v[252:255]\n"
    "ds_read_b64_tr_b16 v[140:141], v177 offset:12288\n"
    "v_accvgpr_write_b32 a51, 0\n"
    "v_mfma_f32_16x16x32_bf16 v[252:255], v[152:155], a[108:111], v[252:255]\n"
    "v_mfma_f32_16x16x32_bf16 v[252:255], v[156:159], a[124:127], v[252:255]\n"
    "ds_read_b64_tr_b16 v[142:143], v177 offset:12800\n"
    "v_accvgpr_write_b32 a52, 0\n"
    "s_waitcnt lgkmcnt(8)\n"
    "v_mfma_f32_16x16x32_bf16 v[64:67], v[160:163], a[128:131], v[64:67]\n"
    "v_mfma_f32_16x16x32_bf16 v[64:67], v[164:167], a[144:147], v[64:67]\n"
    "s_add_u32 m0, s57, 49152\n"
    "ds_read_b64_tr_b16 v[144:145], v178 offset:0\n"
    "v_accvgpr_write_b32 a53, 0\n"
    "v_mfma_f32_16x16x32_bf16 v[64:67], v[168:171], a[160:163], v[64:67]\n"
    "s_add_u32 s68, s83, 128\n"
    "buffer_load_dwordx4 v184, s[40:43], s68 offen offset:0 lds\n"
    "v_mfma_f32_16x16x32_bf16 v[64:67], v[172:175], a[176:179], v[64:67]\n"
    "ds_read_b64_tr_b16 v[146:147], v178 offset:512\n"
    "v_accvgpr_write_b32 a54, 0\n"
    "v_mfma_f32_16x16x32_bf16 v[80:83], v[160:163], a[132:135], v[80:83]\n"
    "s_lshr_b32 s4, s73, 2\n"
    "buffer_load_dwordx4 a[244:247], v190, s[76:79], s70 offen offset:3072 sc1\n"
    "v_mfma_f32_16x16x32_bf16 v[80:83], v[164:167], a[148:151], v[80:83]\n"
    "s_and_b32 s15, s73, 3\n"
    "ds_read_b64_tr_b16 v[148:149], v178 offset:4096\n"
    "v_accvgpr_write_b32 a55, 0\n"
    "v_mfma_f32_16x16x32_bf16 v[80:83], v[168:171], a[164:167], v[80:83]\n"
    "s_lshl_b32 s5, s4, 3\n"
    "s_waitcnt vmcnt(14)\n"
    "v_cmp_ne_u32 vcc, 1, v49\n"
    "s_cbranch_vccz L_ff_ok\n"
    "s_mov_b32 s14, 0x40000\n"
    "L_ff_spin:\n"
    "buffer_load_dword v49, v48, s[76:79], s74 offen sc1\n"
    "s_sub_u32 s14, s14, 1\n"
    "s_waitcnt vmcnt(0)\n"
    "s_cbranch_scc1 L_ff_ok\n"
    "v_cmp_ne_u32 vcc, 1, v49\n"
    "s_cbranch_vccz L_ff_ok\n"
    "s_sleep 3\n"
    "s_branch L_ff_spin\n"
    "L_ff_ok:\n"
    "v_mfma_f32_16x16x32_bf16 v[80:83], v[172:175], a[180:183], v[80:83]\n"
    "s_add_u32 s5, s5, s72\n"
    "ds_read_b64_tr_b16 v[150:151], v178 offset:4608\n"
    "v_accvgpr_write_b32 a56, 0\n"
    "v_mfma_f32_16x16x32_bf16 v[96:99], v[160:163], a[136:139], v[96:99]\n"
    "s_add_u32 s74, s75, 0x61000\n"
    "buffer_load_dwordx4 v[16:19], v190, s[76:79], s74 offen sc1\n"
    "v_mfma_f32_16x16x32_bf16 v[96:99], v[164:167], a[152:155], v[96:99]\n"
    "s_lshl_b32 s5, s5, 2\n"
    "ds_read_b64_tr_b16 v[152:153], v178 offset:8192\n"
    "v_accvgpr_write_b32 a57, 0\n"
    "v_mfma_f32_16x16x32_bf16 v[96:99], v[168:171], a[168:171], v[96:99]\n"
    "s_add_u32 s68, s83, 64640\n"
    "buffer_load_dwordx4 v185, s[40:43], s68 offen offset:1024 lds\n"
    "v_mfma_f32_16x16x32_bf16 v[96:99], v[172:175], a[184:187], v[96:99]\n"
    "s_add_u32 s5, s5, s71\n"
    "ds_read_b64_tr_b16 v[154:155], v178 offset:8704\n"
    "v_accvgpr_write_b32 a58, 0\n"
    "v_mfma_f32_16x16x32_bf16 v[112:115], v[160:163], a[140:143], v[112:115]\n"
    "s_add_u32 s70, s66, s91\n"
    "buffer_load_dwordx4 a[200:203], v190, s[76:79], s70 offen offset:0 sc1\n"
    "v_mfma_f32_16x16x32_bf16 v[112:115], v[164:167], a[156:159], v[112:115]\n"
    "s_lshl_b32 s5, s5, 4\n"
    "ds_read_b64_tr_b16 v[156:157], v178 offset:12288\n"
    "v_accvgpr_write_b32 a59, 0\n"
    "v_mfma_f32_16x16x32_bf16 v[112:115], v[168:171], a[172:175], v[112:115]\n"
    "s_add_u32 s74, s75, 0x64000\n"
    "buffer_load_dwordx4 v[20:23], v190, s[76:79], s74 offen sc1\n"
    "v_mfma_f32_16x16x32_bf16 v[112:115], v[172:175], a[188:191], v[112:115]\n"
    "s_add_u32 s52, s5, 0x2000\n"
    "ds_read_b64_tr_b16 v[158:159], v178 offset:12800\n"
    "v_accvgpr_write_b32 a60, 0\n"
    "s_waitcnt lgkmcnt(8)\n"
    "v_mfma_f32_16x16x32_bf16 v[68:71], v[128:131], a[128:131], v[68:71]\n"
    "s_lshl_b32 s5, s15, 2\n"
    "v_mfma_f32_16x16x32_bf16 v[68:71], v[132:135], a[144:147], v[68:71]\n"
    "s_add_u32 s56, s5, s52\n"
    "ds_read_b64_tr_b16 v[160:161], v179 offset:0\n"
    "v_accvgpr_write_b32 a61, 0\n"
    "v_mfma_f32_16x16x32_bf16 v[68:71], v[136:139], a[160:163], v[68:71]\n"
    "s_add_u32 s68, s83, 129152\n"
    "buffer_load_dwordx4 v184, s[40:43], s68 offen offset:2048 lds\n"
    "v_mfma_f32_16x16x32_bf16 v[68:71], v[140:143], a[176:179], v[68:71]\n"
    "s_lshl_b32 s4, s4, 16\n"
    "ds_read_b64_tr_b16 v[162:163], v179 offset:512\n"
    "v_accvgpr_write_b32 a62, 0\n"
    "v_mfma_f32_16x16x32_bf16 v[84:87], v[128:131], a[132:135], v[84:87]\n"
    "s_lshl_b32 s5, s15, 20\n"
    "buffer_load_dwordx4 a[216:219], v190, s[76:79], s70 offen offset:1024 sc1\n"
    "v_mfma_f32_16x16x32_bf16 v[84:87], v[132:135], a[148:151], v[84:87]\n"
    "s_add_u32 s4, s4, s5\n"
    "ds_read_b64_tr_b16 v[164:165], v179 offset:4096\n"
    "v_accvgpr_write_b32 a63, 0\n"
    "v_mfma_f32_16x16x32_bf16 v[84:87], v[136:139], a[164:167], v[84:87]\n"
    "s_add_u32 s74, s75, 0x67000\n"
    "buffer_load_dwordx4 v[24:27], v190, s[76:79], s74 offen sc1\n"
    "v_mfma_f32_16x16x32_bf16 v[84:87], v[140:143], a[180:183], v[84:87]\n"
    "s_add_u32 s4, s4, s57\n"
    "ds_read_b64_tr_b16 v[166:167], v179 offset:4608\n"
    "v_accvgpr_write_b32 a64, 0\n"
    "v_mfma_f32_16x16x32_bf16 v[100:103], v[128:131], a[136:139], v[100:103]\n"
    "s_add_u32 s4, s4, 0x4000000\n"
    "v_mfma_f32_16x16x32_bf16 v[100:103], v[132:135], a[152:155], v[100:103]\n"
    "s_add_u32 s5, s15, 0\n"
    "ds_read_b64_tr_b16 v[168:169], v179 offset:8192\n"
    "v_accvgpr_write_b32 a65, 0\n"
    "v_mfma_f32_16x16x32_bf16 v[100:103], v[136:139], a[168:171], v[100:103]\n"
    "s_add_u32 s68, s83, 193664\n"
    "buffer_load_dwordx4 v185, s[40:43], s68 offen offset:3072 lds\n"
    "v_mfma_f32_16x16x32_bf16 v[100:103], v[140:143], a[184:187], v[100:103]\n"
    "s_and_b32 s5, s5, 3\n"
    "ds_read_b64_tr_b16 v[170:171], v179 offset:8704\n"
    "v_accvgpr_write_b32 a66, 0\n"
    "v_mfma_f32_16x16x32_bf16 v[116:119], v[128:131], a[140:143], v[116:119]\n"
    "s_lshl_b32 s5, s5, 14\n"
    "buffer_load_dwordx4 a[232:235], v190, s[76:79], s70 offen offset:2048 sc1\n"
    "v_mfma_f32_16x16x32_bf16 v[116:119], v[132:135], a[156:159], v[116:119]\n"
    "s_add_u32 s48, s5, s4\n"
    "ds_read_b64_tr_b16 v[172:173], v179 offset:12288\n"
    "v_accvgpr_write_b32 a67, 0\n"
    "v_mfma_f32_16x16x32_bf16 v[116:119], v[136:139], a[172:175], v[116:119]\n"
    "s_add_u32 s74, s75, 0x6a000\n"
    "buffer_load_dwordx4 v[28:31], v190, s[76:79], s74 offen sc1\n"
    "v_mfma_f32_16x16x32_bf16 v[116:119], v[140:143], a[188:191], v[116:119]\n"
    "s_add_u32 s5, s15, 1\n"
    "ds_read_b64_tr_b16 v[174:175], v179 offset:12800\n"
    "v_accvgpr_write_b32 a68, 0\n"
    "s_waitcnt lgkmcnt(8)\n"
    "v_mfma_f32_16x16x32_bf16 v[72:75], v[144:147], a[128:131], v[72:75]\n"
    "s_and_b32 s5, s5, 3\n"
    "buffer_load_dwordx4 a[248:251], v190, s[76:79], s70 offen offset:3072 sc1\n"
    "v_mfma_f32_16x16x32_bf16 v[72:75], v[148:151], a[144:147], v[72:75]\n"
    "s_lshl_b32 s5, s5, 14\n"
    "ds_read_b64_tr_b16 v[128:129], v176 offset:16384\n"
    "v_accvgpr_write_b32 a69, 0\n"
    "v_mfma_f32_16x16x32_bf16 v[72:75], v[152:155], a[160:163], v[72:75]\n"
    "s_add_u32 s49, s5, s4\n"
    "v_mfma_f32_16x16x32_bf16 v[72:75], v[156:159], a[176:179], v[72:75]\n"
    "s_add_u32 s5, s15, 2\n"
    "ds_read_b64_tr_b16 v[130:131], v176 offset:16896\n"
    "v_accvgpr_write_b32 a70, 0\n"
    "v_mfma_f32_16x16x32_bf16 v[88:91], v[144:147], a[132:135], v[88:91]\n"
    "s_and_b32 s5, s5, 3\n"
    "v_mfma_f32_16x16x32_bf16 v[88:91], v[148:151], a[148:151], v[88:91]\n"
    "s_lshl_b32 s5, s5, 14\n"
    "ds_read_b64_tr_b16 v[132:133], v176 offset:20480\n"
    "v_accvgpr_write_b32 a71, 0\n"
    "v_mfma_f32_16x16x32_bf16 v[88:91], v[152:155], a[164:167], v[88:91]\n"
    "s_add_u32 s50, s5, s4\n"
    "v_mfma_f32_16x16x32_bf16 v[88:91], v[156:159], a[180:183], v[88:91]\n"
    "s_add_u32 s5, s15, 3\n"
    "ds_read_b64_tr_b16 v[134:135], v176 offset:20992\n"
    "v_accvgpr_write_b32 a72, 0\n"
    "v_mfma_f32_16x16x32_bf16 v[104:107], v[144:147], a[136:139], v[104:107]\n"
    "s_add_u32 s70, s67, s91\n"
    "buffer_load_dwordx4 a[204:207], v190, s[76:79], s70 offen offset:0 sc1\n"
    "v_mfma_f32_16x16x32_bf16 v[104:107], v[148:151], a[152:155], v[104:107]\n"
    "s_and_b32 s5, s5, 3\n"
    "ds_read_b64_tr_b16 v[136:137], v176 offset:24576\n"
    "v_accvgpr_write_b32 a73, 0\n"
    "v_mfma_f32_16x16x32_bf16 v[104:107], v[152:155], a[168:171], v[104:107]\n"
    "s_lshl_b32 s5, s5, 14\n"
    "v_mfma_f32_16x16x32_bf16 v[104:107], v[156:159], a[184:187], v[104:107]\n"
    "s_add_u32 s51, s5, s4\n"
    "ds_read_b64_tr_b16 v[138:139], v176 offset:25088\n"
    "v_accvgpr_write_b32 a74, 0\n"
    "v_mfma_f32_16x16x32_bf16 v[120:123], v[144:147], a[140:143], v[120:123]\n"
    "s_lshl_b32 s5, s71, 10\n"
    "v_mfma_f32_16x16x32_bf16 v[120:123], v[148:151], a[156:159], v[120:123]\n"
    "s_add_u32 s58, s5, 0x8000\n"
    "ds_read_b64_tr_b16 v[140:141], v176 offset:28672\n"
    "v_accvgpr_write_b32 a75, 0\n"
    "v_mfma_f32_16x16x32_bf16 v[120:123], v[152:155], a[172:175], v[120:123]\n"
    "s_lshl_b32 s15, s56, 5\n"
    "v_mfma_f32_16x16x32_bf16 v[120:123], v[156:159], a[188:191], v[120:123]\n"
    "s_add_u32 s15, s15, 0x5800000\n"
    "ds_read_b64_tr_b16 v[142:143], v176 offset:29184\n"
    "v_accvgpr_write_b32 a76, 0\n"
    "s_waitcnt lgkmcnt(8)\n"
    "v_mfma_f32_16x16x32_bf16 v[76:79], v[160:163], a[128:131], v[76:79]\n"
    "buffer_load_dwordx4 a[220:223], v190, s[76:79], s70 offen offset:1024 sc1\n"
    "v_mfma_f32_16x16x32_bf16 v[76:79], v[164:167], a[144:147], v[76:79]\n"
    "ds_read_b64_tr_b16 v[144:145], v177 offset:16384\n"
    "v_accvgpr_write_b32 a77, 0\n"
    "v_mfma_f32_16x16x32_bf16 v[76:79], v[168:171], a[160:163], v[76:79]\n"
    "v_mfma_f32_16x16x32_bf16 v[76:79], v[172:175], a[176:179], v[76:79]\n"
    "ds_read_b64_tr_b16 v[146:147], v177 offset:16896\n"
    "v_accvgpr_write_b32 a78, 0\n"
    "v_mfma_f32_16x16x32_bf16 v[92:95], v[160:163], a[132:135], v[92:95]\n"
    "v_mfma_f32_16x16x32_bf16 v[92:95], v[164:167], a[148:151], v[92:95]\n"
    "ds_read_b64_tr_b16 v[148:149], v177 offset:20480\n"
    "v_accvgpr_write_b32 a79, 0\n"
    "v_mfma_f32_16x16x32_bf16 v[92:95], v[168:171], a[164:167], v[92:95]\n"
    "v_mfma_f32_16x16x32_bf16 v[92:95], v[172:175], a[180:183], v[92:95]\n"
    "ds_read_b64_tr_b16 v[150:151], v177 offset:20992\n"
    "v_accvgpr_write_b32 a80, 0\n"
    "v_mfma_f32_16x16x32_bf16 v[108:111], v[160:163], a[136:139], v[108:111]\n"
    "buffer_load_dwordx4 a[236:239], v190, s[76:79], s70 offen offset:2048 sc1\n"
    "v_mfma_f32_16x16x32_bf16 v[108:111], v[164:167], a[152:155], v[108:111]\n"
    "ds_read_b64_tr_b16 v[152:153], v177 offset:24576\n"
    "v_accvgpr_write_b32 a81, 0\n"
    "v_mfma_f32_16x16x32_bf16 v[108:111], v[168:171], a[168:171], v[108:111]\n"
    "v_mfma_f32_16x16x32_bf16 v[108:111], v[172:175], a[184:187], v[108:111]\n"
    "ds_read_b64_tr_b16 v[154:155], v177 offset:25088\n"
    "v_accvgpr_write_b32 a82, 0\n"
    "v_mfma_f32_16x16x32_bf16 v[124:127], v[160:163], a[140:143], v[124:127]\n"
    "v_mfma_f32_16x16x32_bf16 v[124:127], v[164:167], a[156:159], v[124:127]\n"
    "ds_read_b64_tr_b16 v[156:157], v177 offset:28672\n"
    "v_accvgpr_write_b32 a83, 0\n"
    "v_mfma_f32_16x16x32_bf16 v[124:127], v[168:171], a[172:175], v[124:127]\n"
    "buffer_load_dwordx4 a[252:255], v190, s[76:79], s70 offen offset:3072 sc1\n"
    "v_mfma_f32_16x16x32_bf16 v[124:127], v[172:175], a[188:191], v[124:127]\n"
    "ds_read_b64_tr_b16 v[158:159], v177 offset:29184\n"
    "v_accvgpr_write_b32 a84, 0\n"
    "s_waitcnt lgkmcnt(8)\n"
    "v_mfma_f32_16x16x32_bf16 v[192:195], v[128:131], a[128:131], v[192:195]\n"
    "v_mfma_f32_16x16x32_bf16 v[192:195], v[132:135], a[144:147], v[192:195]\n"
    "ds_read_b64_tr_b16 v[160:161], v178 offset:16384\n"
    "v_accvgpr_write_b32 a85, 0\n"
    "v_mfma_f32_16x16x32_bf16 v[192:195], v[136:139], a[160:163], v[192:195]\n"
    "v_mfma_f32_16x16x32_bf16 v[192:195], v[140:143], a[176:179], v[192:195]\n"
    "ds_read_b64_tr_b16 v[162:163], v178 offset:16896\n"
    "v_accvgpr_write_b32 a86, 0\n"
    "v_mfma_f32_16x16x32_bf16 v[208:211], v[128:131], a[132:135], v[208:211]\n"
    "v_mfma_f32_16x16x32_bf16 v[208:211], v[132:135], a[148:151], v[208:211]\n"
    "ds_read_b64_tr_b16 v[164:165], v178 offset:20480\n"
    "v_accvgpr_write_b32 a87, 0\n"
    "v_mfma_f32_16x16x32_bf16 v[208:211], v[136:139], a[164:167], v[208:211]\n"
    "v_mfma_f32_16x16x32_bf16 v[208:211], v[140:143], a[180:183], v[208:211]\n"
    "ds_read_b64_tr_b16 v[166:167], v178 offset:20992\n"
    "v_accvgpr_write_b32 a88, 0\n"
    "v_mfma_f32_16x16x32_bf16 v[224:227], v[128:131], a[136:139], v[224:227]\n"
    "v_mfma_f32_16x16x32_bf16 v[224:227], v[132:135], a[152:155], v[224:227]\n"
    "ds_read_b64_tr_b16 v[168:169], v178 offset:24576\n"
    "v_accvgpr_write_b32 a89, 0\n"
    "v_mfma_f32_16x16x32_bf16 v[224:227], v[136:139], a[168:171], v[224:227]\n"
    "s_lshr_b32 s74, s20, 1\n"
    "s_cmp_eq_u32 s74, 2\n"
    "s_cselect_b32 s20, 1.0, 0\n"
    "v_mfma_f32_16x16x32_bf16 v[224:227], v[140:143], a[184:187], v[224:227]\n"
    "s_cmp_lt_u32 s30, 8\n"
    "s_cselect_b32 s75, s75, 0x40000000\n"
    "ds_read_b64_tr_b16 v[170:171], v178 offset:25088\n"
    "v_accvgpr_write_b32 a90, 0\n"
    "v_mfma_f32_16x16x32_bf16 v[240:243], v[128:131], a[140:143], v[240:243]\n"
    "s_waitcnt vmcnt(5)\n"
    "v_mfma_f32_16x16x32_bf16 v[240:243], v[132:135], a[156:159], v[240:243]\n"
    "ds_read_b64_tr_b16 v[172:173], v178 offset:28672\n"
    "v_add_f32 v16, v16, v20\n"
    "v_mfma_f32_16x16x32_bf16 v[240:243], v[136:139], a[172:175], v[240:243]\n"
    "v_add_f32 v24, v24, v28\n"
    "v_accvgpr_write_b32 a91, 0\n"
    "v_mfma_f32_16x16x32_bf16 v[240:243], v[140:143], a[188:191], v[240:243]\n"
    "ds_read_b64_tr_b16 v[174:175], v178 offset:29184\n"
    "v_add_f32 v17, v17, v21\n"
    "s_waitcnt lgkmcnt(8)\n"
    "v_mfma_f32_16x16x32_bf16 v[196:199], v[144:147], a[128:131], v[196:199]\n"
    "ds_read_b64_tr_b16 v[128:129], v179 offset:16384\n"
    "v_add_f32 v25, v25, v29\n"
    "v_mfma_f32_16x16x32_bf16 v[196:199], v[148:151], a[144:147], v[196:199]\n"
    "ds_read_b64_tr_b16 v[130:131], v179 offset:16896\n"
    "v_add_f32 v18, v18, v22\n"
    "v_mfma_f32_16x16x32_bf16 v[196:199], v[152:155], a[160:163], v[196:199]\n"
    "ds_read_b64_tr_b16 v[132:133], v179 offset:20480\n"
    "v_add_f32 v26, v26, v30\n"
    "v_mfma_f32_16x16x32_bf16 v[196:199], v[156:159], a[176:179], v[196:199]\n"
    "ds_read_b64_tr_b16 v[134:135], v179 offset:20992\n"
    "v_add_f32 v19, v19, v23\n"
    "v_mfma_f32_16x16x32_bf16 v[212:215], v[144:147], a[132:135], v[212:215]\n"
    "ds_read_b64_tr_b16 v[136:137], v179 offset:24576\n"
    "v_add_f32 v27, v27, v31\n"
    "v_mfma_f32_16x16x32_bf16 v[212:215], v[148:151], a[148:151], v[212:215]\n"
    "ds_read_b64_tr_b16 v[138:139], v179 offset:25088\n"
    "v_add_f32 v16, v16, v24\n"
    "v_mfma_f32_16x16x32_bf16 v[212:215], v[152:155], a[164:167], v[212:215]\n"
    "ds_read_b64_tr_b16 v[140:141], v179 offset:28672\n"
    "v_add_f32 v17, v17, v25\n"
    "v_mfma_f32_16x16x32_bf16 v[212:215], v[156:159], a[180:183], v[212:215]\n"
    "ds_read_b64_tr_b16 v[142:143], v179 offset:29184\n"
    "v_add_f32 v18, v18, v26\n"
    "v_mfma_f32_16x16x32_bf16 v[228:231], v[144:147], a[136:139], v[228:231]\n"
    "v_add_f32 v19, v19, v27\n"
    "v_accvgpr_write_b32 a92, 0\n"
    "v_mfma_f32_16x16x32_bf16 v[228:231], v[148:151], a[152:155], v[228:231]\n"
    "v_cvt_pk_bf16_f32 v20, v16, v17\n"
    "v_accvgpr_write_b32 a93, 0\n"
    "v_mfma_f32_16x16x32_bf16 v[228:231], v[152:155], a[168:171], v[228:231]\n"
    "v_cvt_pk_bf16_f32 v21, v18, v19\n"
    "v_accvgpr_write_b32 a94, 0\n"
    "v_mfma_f32_16x16x32_bf16 v[228:231], v[156:159], a[184:187], v[228:231]\n"
    "v_lshlrev_b32 v16, 16, v20\n"
    "v_accvgpr_write_b32 a95, 0\n"
    "v_mfma_f32_16x16x32_bf16 v[244:247], v[144:147], a[140:143], v[244:247]\n"
    "v_and_b32 v17, 0xffff0000, v20\n"
    "v_accvgpr_write_b32 a96, 0\n"
    "v_mfma_f32_16x16x32_bf16 v[244:247], v[148:151], a[156:159], v[244:247]\n"
    "v_lshlrev_b32 v18, 16, v21\n"
    "v_accvgpr_write_b32 a97, 0\n"
    "v_mfma_f32_16x16x32_bf16 v[244:247], v[152:155], a[172:175], v[244:247]\n"
    "v_and_b32 v19, 0xffff0000, v21\n"
    "v_accvgpr_write_b32 a98, 0\n"
    "v_mfma_f32_16x16x32_bf16 v[244:247], v[156:159], a[188:191], v[244:247]\n"
    "v_add_f32 v16, s20, v16\n"
    "v_accvgpr_write_b32 a99, 0\n"
    "s_waitcnt lgkmcnt(8)\n"
    "s_waitcnt lgkmcnt(0)\n"
    "s_barrier\n"
    "v_mfma_f32_16x16x32_bf16 v[200:203], v[160:163], a[128:131], v[200:203]\n"
    "v_add_f32 v17, s20, v17\n"
    "v_accvgpr_write_b32 a100, 0\n"
    "v_mfma_f32_16x16x32_bf16 v[200:203], v[164:167], a[144:147], v[200:203]\n"
    "ds_read_b64_tr_b16 v[144:145], v176 offset:32768\n"
    "v_add_f32 v18, s20, v18\n"
    "v_mfma_f32_16x16x32_bf16 v[200:203], v[168:171], a[160:163], v[200:203]\n"
    "v_add_f32 v19, s20, v19\n"
    "v_accvgpr_write_b32 a101, 0\n"
    "v_mfma_f32_16x16x32_bf16 v[200:203], v[172:175], a[176:179], v[200:203]\n"
    "ds_read_b64_tr_b16 v[146:147], v176 offset:33280\n"
    "v_cvt_pk_bf16_f32 v20, v16, v17\n"
    "v_mfma_f32_16x16x32_bf16 v[216:219], v[160:163], a[132:135], v[216:219]\n"
    "s_mov_b32 s59, 0x30000\n"
    "v_cvt_pk_bf16_f32 v21, v18, v19\n"
    "v_accvgpr_write_b32 a102, 0\n"
    "v_mfma_f32_16x16x32_bf16 v[216:219], v[164:167], a[148:151], v[216:219]\n"
    "s_lshl_b32 s81, s72, 1\n"
    "ds_read_b64_tr_b16 v[148:149], v176 offset:36864\n"
    "v_lshlrev_b32 v16, 16, v20\n"
    "v_mfma_f32_16x16x32_bf16 v[216:219], v[168:171], a[164:167], v[216:219]\n"
    "s_add_u32 s81, s81, 2\n"
    "v_and_b32 v17, 0xffff0000, v20\n"
    "v_accvgpr_write_b32 a103, 0\n"
    "v_mfma_f32_16x16x32_bf16 v[216:219], v[172:175], a[180:183], v[216:219]\n"
    "s_lshl_b32 s82, 7, s81\n"
    "ds_read_b64_tr_b16 v[150:151], v176 offset:37376\n"
    "v_lshlrev_b32 v18, 16, v21\n"
    "v_mfma_f32_16x16x32_bf16 v[232:235], v[160:163], a[136:139], v[232:235]\n"
    "s_lshr_b32 s82, s82, 3\n"
    "v_and_b32 v19, 0xffff0000, v21\n"
    "v_accvgpr_write_b32 a104, 0\n"
    "v_mfma_f32_16x16x32_bf16 v[232:235], v[164:167], a[152:155], v[232:235]\n"
    "s_or_b32 s82, s82, 0x8000\n"
    "ds_read_b64_tr_b16 v[152:153], v176 offset:40960\n"
    "v_lshrrev_b32 v48, 1, v190\n"
    "v_mfma_f32_16x16x32_bf16 v[232:235], v[168:171], a[168:171], v[232:235]\n"
    "s_add_u32 s74, s75, 0x11000\n"
    "buffer_load_dword v187, v186, s[76:79], s12 offen sc1\n"
    "v_mfma_f32_16x16x32_bf16 v[232:235], v[172:175], a[184:187], v[232:235]\n"
    "s_lshr_b32 s75, s75, 1\n"
    "ds_read_b64_tr_b16 v[154:155], v176 offset:41472\n"
    "v_accvgpr_write_b32 a105, 0\n"
    "v_mfma_f32_16x16x32_bf16 v[248:251], v[160:163], a[140:143], v[248:251]\n"
    "s_add_u32 s75, s75, 0x14800\n"
    "v_mfma_f32_16x16x32_bf16 v[248:251], v[164:167], a[156:159], v[248:251]\n"
    "s_and_b32 s91, s71, 1\n"
    "ds_read_b64_tr_b16 v[156:157], v176 offset:45056\n"
    "v_accvgpr_write_b32 a106, 0\n"
    "v_mfma_f32_16x16x32_bf16 v[248:251], v[168:171], a[172:175], v[248:251]\n"
    "s_lshl_b32 s91, s91, 14\n"
    "buffer_store_dwordx4 v[16:19], v190, s[76:79], s74 offen sc1\n"
    "v_mfma_f32_16x16x32_bf16 v[248:251], v[172:175], a[188:191], v[248:251]\n"
    "s_or_b32 s60, s91, 0x30000\n"
    "ds_read_b64_tr_b16 v[158:159], v176 offset:45568\n"
    "v_accvgpr_write_b32 a107, 0\n"
    "v_mfma_f32_16x16x32_bf16 v[204:207], v[128:131], a[128:131], v[204:207]\n"
    "s_mov_b32 s80, -1\n"
    "buffer_store_dwordx2 v[20:21], v48, s[76:79], s75 offen sc1\n"
    "v_mfma_f32_16x16x32_bf16 v[204:207], v[132:135], a[144:147], v[204:207]\n"
    "s_mov_b32 s92, 0\n"
    "ds_read_b64_tr_b16 v[160:161], v177 offset:32768\n"
    "v_mov_b64 v[24:25], 0\n"
    "v_mfma_f32_16x16x32_bf16 v[204:207], v[136:139], a[160:163], v[204:207]\n"
    "s_mov_b64 s[94:95], 0\n"
    "v_mov_b64 v[26:27], 0\n"
    "v_accvgpr_write_b32 a108, 0\n"
    "v_mfma_f32_16x16x32_bf16 v[204:207], v[140:143], a[176:179], v[204:207]\n"
    "s_lshl_b32 s74, s30, 10\n"
    "ds_read_b64_tr_b16 v[162:163], v177 offset:33280\n"
    "v_accvgpr_write_b32 a109, 0\n"
    "v_mfma_f32_16x16x32_bf16 v[220:223], v[128:131], a[132:135], v[220:223]\n"
    "s_add_u32 s74, s74, 0x5880000\n"
    "v_mfma_f32_16x16x32_bf16 v[220:223], v[132:135], a[148:151], v[220:223]\n"
    "s_lshl_b32 s75, s13, 30\n"
    "ds_read_b64_tr_b16 v[164:165], v177 offset:36864\n"
    "v_accvgpr_write_b32 a110, 0\n"
    "v_mfma_f32_16x16x32_bf16 v[220:223], v[136:139], a[164:167], v[220:223]\n"
    "s_and_b32 s75, s75, 0x40000000\n"
    "v_mfma_f32_16x16x32_bf16 v[220:223], v[140:143], a[180:183], v[220:223]\n"
    "s_or_b32 s74, s74, s75\n"
    ".p2align 12\n"
    "L_pad6:\n"
    "s_cbranch_execz L_pad10\n"
    "ds_read_b64_tr_b16 v[166:167], v177 offset:37376\n"
    "v_accvgpr_write_b32 a111, 0\n"
    "v_mfma_f32_16x16x32_bf16 v[236:239], v[128:131], a[136:139], v[236:239]\n"
    "s_add_u32 s83, s57, 16384\n"
    "buffer_store_dwordx4 v[24:27], v190, s[76:79], s74 offen sc1\n"
    "v_mfma_f32_16x16x32_bf16 v[236:239], v[132:135], a[152:155], v[236:239]\n"
    "s_mov_b32 s84, 0x40000000\n"
    "ds_read_b64_tr_b16 v[168:169], v177 offset:40960\n"
    "v_accvgpr_write_b32 a112, 0\n"
    "v_mfma_f32_16x16x32_bf16 v[236:239], v[136:139], a[168:171], v[236:239]\n"
    "s_add_u32 s87, s57, 0x20000\n"
    "v_mfma_f32_16x16x32_bf16 v[236:239], v[140:143], a[184:187], v[236:239]\n"
    "s_add_u32 s88, s57, 0x18000\n"
    "ds_read_b64_tr_b16 v[170:171], v177 offset:41472\n"
    "v_accvgpr_write_b32 a113, 0\n"
    "v_mfma_f32_16x16x32_bf16 v[252:255], v[128:131], a[140:143], v[252:255]\n"
    "s_add_u32 s85, s57, 0x80000\n"
    "s_waitcnt vmcnt(3)\n"
    "v_cmp_ne_u32 vcc, 1, v187\n"
    "s_cbranch_vccz L_kv_ok\n"
    "s_mov_b32 s14, 0x40000\n"
    "L_kv_spin:\n"
    "buffer_load_dword v187, v186, s[76:79], s12 offen sc1\n"
    "s_sub_u32 s14, s14, 1\n"
    "s_waitcnt vmcnt(0)\n"
    "s_cbranch_scc1 L_kv_ok\n"
    "v_cmp_ne_u32 vcc, 1, v187\n"
    "s_cbranch_vccz L_kv_ok\n"
    "s_sleep 3\n"
    "s_branch L_kv_spin\n"
    "L_kv_ok:\n"
    "v_mfma_f32_16x16x32_bf16 v[252:255], v[132:135], a[156:159], v[252:255]\n"
    "s_mov_b32 s31, 0x7fffffff\n"
    "s_add_u32 m0, s57, 0x10000\n"
    "ds_read_b64_tr_b16 v[172:173], v177 offset:45056\n"
    "v_accvgpr_write_b32 a114, 0\n"
    "v_mfma_f32_16x16x32_bf16 v[252:255], v[136:139], a[172:175], v[252:255]\n"
    "s_lshl_b32 s89, s71, 4\n"
    "buffer_load_dwordx4 v190, s[44:47], s57 offen offset:0 sc1 lds\n"
    "v_mfma_f32_16x16x32_bf16 v[252:255], v[140:143], a[188:191], v[252:255]\n"
    "ds_read_b64_tr_b16 v[174:175], v177 offset:45568\n"
    "v_accvgpr_write_b32 a115, 0\n"
    "s_waitcnt lgkmcnt(8)\n"
    "v_mfma_f32_16x16x32_bf16 v[64:67], v[144:147], a[192:195], v[64:67]\n"
    "buffer_load_dwordx4 v190, s[44:47], s57 offen offset:1024 sc1 lds\n"
    "v_mfma_f32_16x16x32_bf16 v[64:67], v[148:151], a[208:211], v[64:67]\n"
    "ds_read_b64_tr_b16 v[128:129], v178 offset:32768\n"
    "v_accvgpr_write_b32 a116, 0\n"
    "v_mfma_f32_16x16x32_bf16 v[64:67], v[152:155], a[224:227], v[64:67]\n"
    "buffer_load_dwordx4 v190, s[44:47], s57 offen offset:2048 sc1 lds\n"
    "v_mfma_f32_16x16x32_bf16 v[64:67], v[156:159], a[240:243], v[64:67]\n"
    "ds_read_b64_tr_b16 v[130:131], v178 offset:33280\n"
    "v_lshrrev_b32 v184, 8, v190\n"
    "v_mfma_f32_16x16x32_bf16 v[80:83], v[144:147], a[196:199], v[80:83]\n"
    "buffer_load_dwordx4 v190, s[44:47], s57 offen offset:3072 sc1 lds\n"
    "v_mfma_f32_16x16x32_bf16 v[80:83], v[148:151], a[212:215], v[80:83]\n"
    "s_add_u32 m0, s57, 0x18000\n"
    "ds_read_b64_tr_b16 v[132:133], v178 offset:36864\n"
    "v_lshlrev_b32 v184, 2, v184\n"
    "v_mfma_f32_16x16x32_bf16 v[80:83], v[152:155], a[228:231], v[80:83]\n"
    "buffer_load_dwordx4 v190, s[44:47], s85 offen offset:0 sc1 lds\n"
    "v_mfma_f32_16x16x32_bf16 v[80:83], v[156:159], a[244:247], v[80:83]\n"
    "ds_read_b64_tr_b16 v[134:135], v178 offset:37376\n"
    "v_bfe_u32 v183, v190, 4, 4\n"
    "v_mfma_f32_16x16x32_bf16 v[96:99], v[144:147], a[200:203], v[96:99]\n"
    "buffer_load_dwordx4 v190, s[44:47], s85 offen offset:1024 sc1 lds\n"
    "v_mfma_f32_16x16x32_bf16 v[96:99], v[148:151], a[216:219], v[96:99]\n"
    "ds_read_b64_tr_b16 v[136:137], v178 offset:40960\n"
    "v_sub_u32 v183, v183, v184\n"
    "v_mfma_f32_16x16x32_bf16 v[96:99], v[152:155], a[232:235], v[96:99]\n"
    "buffer_load_dwordx4 v190, s[44:47], s85 offen offset:2048 sc1 lds\n"
    "v_mfma_f32_16x16x32_bf16 v[96:99], v[156:159], a[248:251], v[96:99]\n"
    "ds_read_b64_tr_b16 v[138:139], v178 offset:41472\n"
    "v_add_u32 v183, s89, v183\n"
    "v_mfma_f32_16x16x32_bf16 v[112:115], v[144:147], a[204:207], v[112:115]\n"
    "buffer_load_dwordx4 v190, s[44:47], s85 offen offset:3072 sc1 lds\n"
    "v_mfma_f32_16x16x32_bf16 v[112:115], v[148:151], a[220:223], v[112:115]\n"
    "s_add_u32 m0, s57, 0x20000\n"
    "ds_read_b64_tr_b16 v[140:141], v178 offset:45056\n"
    "v_mov_b32 v188, 0xff800000\n"
    "v_mfma_f32_16x16x32_bf16 v[112:115], v[152:155], a[236:239], v[112:115]\n"
    "buffer_load_dwordx4 v190, s[44:47], s83 offen offset:0 sc1 lds\n"
    "v_mfma_f32_16x16x32_bf16 v[112:115], v[156:159], a[252:255], v[112:115]\n"
    "ds_read_b64_tr_b16 v[142:143], v178 offset:45568\n"
    "v_mov_b32 v189, 0xff800000\n"
    "s_waitcnt lgkmcnt(8)\n"
    "v_mfma_f32_16x16x32_bf16 v[68:71], v[160:163], a[192:195], v[68:71]\n"
    "buffer_load_dwordx4 v190, s[44:47], s83 offen offset:1024 sc1 lds\n"
    "v_mfma_f32_16x16x32_bf16 v[68:71], v[164:167], a[208:211], v[68:71]\n"
    "ds_read_b64_tr_b16 v[144:145], v179 offset:32768\n"
    "v_add_u32 v184, 0x10000, v190\n"
    "v_mfma_f32_16x16x32_bf16 v[68:71], v[168:171], a[224:227], v[68:71]\n"
    "buffer_load_dwordx4 v190, s[44:47], s83 offen offset:2048 sc1 lds\n"
    "v_mfma_f32_16x16x32_bf16 v[68:71], v[172:175], a[240:243], v[68:71]\n"
    "ds_read_b64_tr_b16 v[146:147], v179 offset:33280\n"
    "v_add_u32 v185, 0x8000, v190\n"
    "v_mfma_f32_16x16x32_bf16 v[84:87], v[160:163], a[196:199], v[84:87]\n"
    "buffer_load_dwordx4 v190, s[44:47], s83 offen offset:3072 sc1 lds\n"
    "v_mfma_f32_16x16x32_bf16 v[84:87], v[164:167], a[212:215], v[84:87]\n"
    "ds_read_b64_tr_b16 v[148:149], v179 offset:36864\n"
    "v_accvgpr_write_b32 a117, 0\n"
    "v_mfma_f32_16x16x32_bf16 v[84:87], v[168:171], a[228:231], v[84:87]\n"
    "v_mfma_f32_16x16x32_bf16 v[84:87], v[172:175], a[244:247], v[84:87]\n"
    "ds_read_b64_tr_b16 v[150:151], v179 offset:37376\n"
    "v_accvgpr_write_b32 a118, 0\n"
    "v_mfma_f32_16x16x32_bf16 v[100:103], v[160:163], a[200:203], v[100:103]\n"
    "v_mfma_f32_16x16x32_bf16 v[100:103], v[164:167], a[216:219], v[100:103]\n"
    "ds_read_b64_tr_b16 v[152:153], v179 offset:40960\n"
    "v_accvgpr_write_b32 a119, 0\n"
    "v_mfma_f32_16x16x32_bf16 v[100:103], v[168:171], a[232:235], v[100:103]\n"
    "v_mfma_f32_16x16x32_bf16 v[100:103], v[172:175], a[248:251], v[100:103]\n"
    "ds_read_b64_tr_b16 v[154:155], v179 offset:41472\n"
    "v_accvgpr_write_b32 a120, 0\n"
    "v_mfma_f32_16x16x32_bf16 v[116:119], v[160:163], a[204:207], v[116:119]\n"
    "v_mfma_f32_16x16x32_bf16 v[116:119], v[164:167], a[220:223], v[116:119]\n"
    "ds_read_b64_tr_b16 v[156:157], v179 offset:45056\n"
    "v_accvgpr_write_b32 a121, 0\n"
    "v_mfma_f32_16x16x32_bf16 v[116:119], v[168:171], a[236:239], v[116:119]\n"
    "v_mfma_f32_16x16x32_bf16 v[116:119], v[172:175], a[252:255], v[116:119]\n"
    "ds_read_b64_tr_b16 v[158:159], v179 offset:45568\n"
    "v_accvgpr_write_b32 a122, 0\n"
    "s_waitcnt lgkmcnt(8)\n"
    "v_mfma_f32_16x16x32_bf16 v[72:75], v[128:131], a[192:195], v[72:75]\n"
    "s_sub_u32 s74, s30, 736\n"
    "v_mfma_f32_16x16x32_bf16 v[72:75], v[132:135], a[208:211], v[72:75]\n"
    "s_mul_i32 s75, s74, 2341\n"
    "ds_read_b64_tr_b16 v[160:161], v176 offset:49152\n"
    "v_accvgpr_write_b32 a123, 0\n"
    "v_mfma_f32_16x16x32_bf16 v[72:75], v[136:139], a[224:227], v[72:75]\n"
    "s_lshr_b32 s75, s75, 16\n"
    "v_mfma_f32_16x16x32_bf16 v[72:75], v[140:143], a[240:243], v[72:75]\n"
    "s_mul_i32 s93, s75, 28\n"
    "ds_read_b64_tr_b16 v[162:163], v176 offset:49664\n"
    "v_accvgpr_write_b32 a124, 0\n"
    "v_mfma_f32_16x16x32_bf16 v[88:91], v[128:131], a[196:199], v[88:91]\n"
    "s_sub_u32 s74, s74, s93\n"
    "v_mfma_f32_16x16x32_bf16 v[88:91], v[132:135], a[212:215], v[88:91]\n"
    "s_lshl_b32 s74, s74, 16\n"
    "ds_read_b64_tr_b16 v[164:165], v176 offset:53248\n"
    "v_accvgpr_write_b32 a125, 0\n"
    "v_mfma_f32_16x16x32_bf16 v[88:91], v[136:139], a[228:231], v[88:91]\n"
    "s_lshl_b32 s75, s75, 7\n"
    "v_mfma_f32_16x16x32_bf16 v[88:91], v[140:143], a[244:247], v[88:91]\n"
    "s_add_u32 s20, s74, s75\n"
    "ds_read_b64_tr_b16 v[166:167], v176 offset:53760\n"
    "v_accvgpr_write_b32 a126, 0\n"
    "v_mfma_f32_16x16x32_bf16 v[104:107], v[128:131], a[200:203], v[104:107]\n"
    "s_sub_u32 s74, s30, 512\n"
    "v_mfma_f32_16x16x32_bf16 v[104:107], v[132:135], a[216:219], v[104:107]\n"
    "s_mul_i32 s75, s74, 2341\n"
    "ds_read_b64_tr_b16 v[168:169], v176 offset:57344\n"
    "v_accvgpr_write_b32 a127, 0\n"
    "v_mfma_f32_16x16x32_bf16 v[104:107], v[136:139], a[232:235], v[104:107]\n"
    "s_lshr_b32 s75, s75, 16\n"
    "v_mfma_f32_16x16x32_bf16 v[104:107], v[140:143], a[248:251], v[104:107]\n"
    "s_mul_i32 s75, s75, 225792\n"
    "ds_read_b64_tr_b16 v[170:171], v176 offset:57856\n"
    "v_mfma_f32_16x16x32_bf16 v[120:123], v[128:131], a[204:207], v[120:123]\n"
    "s_lshl_b32 s74, s74, 7\n"
    "v_mfma_f32_16x16x32_bf16 v[120:123], v[132:135], a[220:223], v[120:123]\n"
    "s_add_u32 s74, s74, s75\n"
    "ds_read_b64_tr_b16 v[172:173], v176 offset:61440\n"
    "v_mfma_f32_16x16x32_bf16 v[120:123], v[136:139], a[236:239], v[120:123]\n"
    "s_cmp_lt_u32 s30, 736\n"
    "v_mfma_f32_16x16x32_bf16 v[120:123], v[140:143], a[252:255], v[120:123]\n"
    "s_cselect_b32 s20, s74, s20\n"
    "ds_read_b64_tr_b16 v[174:175], v176 offset:61952\n"
    "s_waitcnt lgkmcnt(8)\n"
    "v_mfma_f32_16x16x32_bf16 v[76:79], v[144:147], a[192:195], v[76:79]\n"
    "s_cselect_b64 s[36:37], s[26:27], s[28:29]\n"
    "v_mfma_f32_16x16x32_bf16 v[76:79], v[148:151], a[208:211], v[76:79]\n"
    "s_movk_i32 s93, 1024\n"
    "ds_read_b64_tr_b16 v[128:129], v177 offset:49152\n"
    "v_mfma_f32_16x16x32_bf16 v[76:79], v[152:155], a[224:227], v[76:79]\n"
    "s_cselect_b32 s22, 3584, s93\n"
    "v_mfma_f32_16x16x32_bf16 v[76:79], v[156:159], a[240:243], v[76:79]\n"
    "s_and_b32 s74, s30, 63\n"
    "ds_read_b64_tr_b16 v[130:131], v177 offset:49664\n"
    "v_mfma_f32_16x16x32_bf16 v[92:95], v[144:147], a[196:199], v[92:95]\n"
    "s_lshl_b32 s74, s74, 16\n"
    "v_mfma_f32_16x16x32_bf16 v[92:95], v[148:151], a[212:215], v[92:95]\n"
    "s_lshr_b32 s75, s30, 6\n"
    "ds_read_b64_tr_b16 v[132:133], v177 offset:53248\n"
    "v_mfma_f32_16x16x32_bf16 v[92:95], v[152:155], a[228:231], v[92:95]\n"
    "s_lshl_b32 s75, s75, 7\n"
    "v_mfma_f32_16x16x32_bf16 v[92:95], v[156:159], a[244:247], v[92:95]\n"
    "s_add_u32 s74, s74, s75\n"
    "ds_read_b64_tr_b16 v[134:135], v177 offset:53760\n"
    "v_mfma_f32_16x16x32_bf16 v[108:111], v[144:147], a[200:203], v[108:111]\n"
    "s_mov_b32 s75, 1835008\n"
    "v_mfma_f32_16x16x32_bf16 v[108:111], v[148:151], a[216:219], v[108:111]\n"
    "s_cmp_lt_u32 s30, 512\n"
    "ds_read_b64_tr_b16 v[136:137], v177 offset:57344\n"
    "v_mfma_f32_16x16x32_bf16 v[108:111], v[152:155], a[232:235], v[108:111]\n"
    "s_cselect_b32 s20, s74, s20\n"
    "v_mfma_f32_16x16x32_bf16 v[108:111], v[156:159], a[248:251], v[108:111]\n"
    "s_cselect_b64 s[36:37], s[24:25], s[36:37]\n"
    "ds_read_b64_tr_b16 v[138:139], v177 offset:57856\n"
    "v_mfma_f32_16x16x32_bf16 v[124:127], v[144:147], a[204:207], v[124:127]\n"
    "s_cselect_b32 s22, s93, s22\n"
    "v_mfma_f32_16x16x32_bf16 v[124:127], v[148:151], a[220:223], v[124:127]\n"
    "s_cselect_b32 s38, 0x400000, s75\n"
    "ds_read_b64_tr_b16 v[140:141], v177 offset:61440\n"
    "v_mfma_f32_16x16x32_bf16 v[124:127], v[152:155], a[236:239], v[124:127]\n"
    "s_and_b32 s37, s37, 0xffff\n"
    "v_mfma_f32_16x16x32_bf16 v[124:127], v[156:159], a[252:255], v[124:127]\n"
    "s_lshl_b32 s74, s30, 13\n"
    "ds_read_b64_tr_b16 v[142:143], v177 offset:61952\n"
    "s_waitcnt lgkmcnt(8)\n"
    "v_mfma_f32_16x16x32_bf16 v[192:195], v[160:163], a[192:195], v[192:195]\n"
    "s_add_u32 s74, s74, 0x3ffff00\n"
    "v_mfma_f32_16x16x32_bf16 v[192:195], v[164:167], a[208:211], v[192:195]\n"
    "s_cmp_lt_u32 s30, 960\n"
    "ds_read_b64_tr_b16 v[144:145], v178 offset:49152\n"
    "v_mfma_f32_16x16x32_bf16 v[192:195], v[168:171], a[224:227], v[192:195]\n"
    "s_cselect_b32 s21, s74, 0x40000000\n"
    "v_mfma_f32_16x16x32_bf16 v[192:195], v[172:175], a[240:243], v[192:195]\n"
    "s_cselect_b32 s20, s20, 0x40000000\n"
    "ds_read_b64_tr_b16 v[146:147], v178 offset:49664\n"
    "v_cvt_pk_bf16_f32 v0, v64, v65\n"
    "v_mfma_f32_16x16x32_bf16 v[208:211], v[160:163], a[196:199], v[208:211]\n"
    "s_lshl_b32 s74, s18, 2\n"
    "v_cvt_pk_bf16_f32 v1, v66, v67\n"
    "v_cvt_pk_bf16_f32 v2, v68, v69\n"
    "v_mfma_f32_16x16x32_bf16 v[208:211], v[164:167], a[212:215], v[208:211]\n"
    "s_add_u32 s74, s74, s71\n"
    "ds_read_b64_tr_b16 v[148:149], v178 offset:53248\n"
    "v_cvt_pk_bf16_f32 v3, v70, v71\n"
    "v_mfma_f32_16x16x32_bf16 v[208:211], v[168:171], a[228:231], v[208:211]\n"
    "s_lshl2_add_u32 s23, s74, 0x8000\n"
    "v_cvt_pk_bf16_f32 v16, v80, v81\n"
    "v_cvt_pk_bf16_f32 v17, v82, v83\n"
    "v_mfma_f32_16x16x32_bf16 v[208:211], v[172:175], a[244:247], v[208:211]\n"
    "ds_read_b64_tr_b16 v[150:151], v178 offset:53760\n"
    "v_cvt_pk_bf16_f32 v18, v84, v85\n"
    "v_mfma_f32_16x16x32_bf16 v[224:227], v[160:163], a[200:203], v[224:227]\n"
    "v_cvt_pk_bf16_f32 v19, v86, v87\n"
    "v_cvt_pk_bf16_f32 v32, v96, v97\n"
    "v_mfma_f32_16x16x32_bf16 v[224:227], v[164:167], a[216:219], v[224:227]\n"
    "ds_read_b64_tr_b16 v[152:153], v178 offset:57344\n"
    "v_cvt_pk_bf16_f32 v33, v98, v99\n"
    "v_mfma_f32_16x16x32_bf16 v[224:227], v[168:171], a[232:235], v[224:227]\n"
    "v_cvt_pk_bf16_f32 v34, v100, v101\n"
    "v_cvt_pk_bf16_f32 v35, v102, v103\n"
    "v_mfma_f32_16x16x32_bf16 v[224:227], v[172:175], a[248:251], v[224:227]\n"
    "ds_read_b64_tr_b16 v[154:155], v178 offset:57856\n"
    "v_cvt_pk_bf16_f32 v48, v112, v113\n"
    "v_mfma_f32_16x16x32_bf16 v[240:243], v[160:163], a[204:207], v[240:243]\n"
    "v_cvt_pk_bf16_f32 v49, v114, v115\n"
    "v_cvt_pk_bf16_f32 v50, v116, v117\n"
    "v_mfma_f32_16x16x32_bf16 v[240:243], v[164:167], a[220:223], v[240:243]\n"
    "ds_read_b64_tr_b16 v[156:157], v178 offset:61440\n"
    "v_cvt_pk_bf16_f32 v51, v118, v119\n"
    "v_mfma_f32_16x16x32_bf16 v[240:243], v[168:171], a[236:239], v[240:243]\n"
    "v_cvt_pk_bf16_f32 v4, v72, v73\n"
    "v_cvt_pk_bf16_f32 v5, v74, v75\n"
    "v_mfma_f32_16x16x32_bf16 v[240:243], v[172:175], a[252:255], v[240:243]\n"
    "ds_read_b64_tr_b16 v[158:159], v178 offset:61952\n"
    "v_cvt_pk_bf16_f32 v6, v76, v77\n"
    "s_waitcnt lgkmcnt(8)\n"
    "v_mfma_f32_16x16x32_bf16 v[196:199], v[128:131], a[192:195], v[196:199]\n"
    "ds_read_b64_tr_b16 v[160:161], v179 offset:49152\n"
    "v_cvt_pk_bf16_f32 v7, v78, v79\n"
    "v_mfma_f32_16x16x32_bf16 v[196:199], v[132:135], a[208:211], v[196:199]\n"
    "ds_read_b64_tr_b16 v[162:163], v179 offset:49664\n"
    "v_cvt_pk_bf16_f32 v20, v88, v89\n"
    "v_mfma_f32_16x16x32_bf16 v[196:199], v[136:139], a[224:227], v[196:199]\n"
    "ds_read_b64_tr_b16 v[164:165], v179 offset:53248\n"
    "v_cvt_pk_bf16_f32 v21, v90, v91\n"
    "v_mfma_f32_16x16x32_bf16 v[196:199], v[140:143], a[240:243], v[196:199]\n"
    "ds_read_b64_tr_b16 v[166:167], v179 offset:53760\n"
    "v_cvt_pk_bf16_f32 v22, v92, v93\n"
    "v_mfma_f32_16x16x32_bf16 v[212:215], v[128:131], a[196:199], v[212:215]\n"
    "ds_read_b64_tr_b16 v[168:169], v179 offset:57344\n"
    "v_cvt_pk_bf16_f32 v23, v94, v95\n"
    "v_mfma_f32_16x16x32_bf16 v[212:215], v[132:135], a[212:215], v[212:215]\n"
    "ds_read_b64_tr_b16 v[170:171], v179 offset:57856\n"
    "v_cvt_pk_bf16_f32 v36, v104, v105\n"
    "v_mfma_f32_16x16x32_bf16 v[212:215], v[136:139], a[228:231], v[212:215]\n"
    "ds_read_b64_tr_b16 v[172:173], v179 offset:61440\n"
    "v_cvt_pk_bf16_f32 v37, v106, v107\n"
    "v_mfma_f32_16x16x32_bf16 v[212:215], v[140:143], a[244:247], v[212:215]\n"
    "ds_read_b64_tr_b16 v[174:175], v179 offset:61952\n"
    "v_cvt_pk_bf16_f32 v38, v108, v109\n"
    "v_mfma_f32_16x16x32_bf16 v[228:231], v[128:131], a[200:203], v[228:231]\n"
    "v_cvt_pk_bf16_f32 v39, v110, v111\n"
    "v_cvt_pk_bf16_f32 v52, v120, v121\n"
    "v_mfma_f32_16x16x32_bf16 v[228:231], v[132:135], a[216:219], v[228:231]\n"
    "v_cvt_pk_bf16_f32 v53, v122, v123\n"
    "v_cvt_pk_bf16_f32 v54, v124, v125\n"
    "v_mfma_f32_16x16x32_bf16 v[228:231], v[136:139], a[232:235], v[228:231]\n"
    "v_cvt_pk_bf16_f32 v55, v126, v127\n"
    "v_bfe_u32 v181, v190, 7, 2\n"
    "v_mfma_f32_16x16x32_bf16 v[228:231], v[140:143], a[248:251], v[228:231]\n"
    "v_lshrrev_b32 v180, 9, v190\n"
    "v_subrev_u32 v64, 0, v183\n"
    "v_mfma_f32_16x16x32_bf16 v[244:247], v[128:131], a[204:207], v[244:247]\n"
    "v_lshl_or_b32 v181, v180, 4, v181\n"
    "v_subrev_u32 v65, 1, v183\n"
    "v_mfma_f32_16x16x32_bf16 v[244:247], v[132:135], a[220:223], v[244:247]\n"
    "v_mul_u32_u24 v181, s22, v181\n"
    "v_subrev_u32 v66, 2, v183\n"
    "v_subrev_u32 v67, 3, v183\n"
    "v_mfma_f32_16x16x32_bf16 v[244:247], v[136:139], a[236:239], v[244:247]\n"
    "v_and_b32 v182, 0x70, v190\n"
    "v_subrev_u32 v68, 16, v183\n"
    "v_mfma_f32_16x16x32_bf16 v[244:247], v[140:143], a[252:255], v[244:247]\n"
    "v_add_u32 v181, v181, v182\n"
    "v_subrev_u32 v69, 17, v183\n"
    "s_waitcnt lgkmcnt(8)\n"
    "s_waitcnt vmcnt(0) lgkmcnt(0)\n"
    "s_barrier\n"
    "v_mfma_f32_16x16x32_bf16 v[200:203], v[144:147], a[192:195], v[200:203]\n"
    "s_lshl_b32 s89, s71, 13\n"
    "ds_read_b128 a[128:131], v184 offset:0\n"
    "v_min_u32 v180, 1, v190\n"
    "v_subrev_u32 v70, 18, v183\n"
    "v_mfma_f32_16x16x32_bf16 v[200:203], v[148:151], a[208:211], v[200:203]\n"
    "s_lshl_b32 s90, s71, 9\n"
    "v_lshlrev_b32 v180, 30, v180\n"
    "v_subrev_u32 v71, 19, v183\n"
    "v_mfma_f32_16x16x32_bf16 v[200:203], v[152:155], a[224:227], v[200:203]\n"
    "s_add_u32 s90, s90, 0x1c000\n"
    "ds_read_b128 a[132:135], v184 offset:1024\n"
    "v_mov_b32 v182, 1\n"
    "v_subrev_u32 v72, 32, v183\n"
    "v_mfma_f32_16x16x32_bf16 v[200:203], v[156:159], a[240:243], v[200:203]\n"
    "s_lshl_b32 s22, s22, 2\n"
    "v_subrev_u32 v73, 33, v183\n"
    "v_subrev_u32 v74, 34, v183\n"
    "v_mfma_f32_16x16x32_bf16 v[216:219], v[144:147], a[196:199], v[216:219]\n"
    "s_xor_b32 s19, s71, 1\n"
    "ds_read_b128 a[136:139], v184 offset:2048\n"
    "v_subrev_u32 v75, 35, v183\n"
    "v_mfma_f32_16x16x32_bf16 v[216:219], v[148:151], a[212:215], v[216:219]\n"
    "s_lshl_b32 s19, s19, 13\n"
    "v_subrev_u32 v76, 48, v183\n"
    "v_subrev_u32 v77, 49, v183\n"
    "v_subrev_u32 v78, 50, v183\n"
    "v_mfma_f32_16x16x32_bf16 v[216:219], v[152:155], a[228:231], v[216:219]\n"
    "s_add_u32 s19, s19, 7168\n"
    "ds_read_b128 a[140:143], v184 offset:3072\n"
    "v_subrev_u32 v79, 51, v183\n"
    "v_mfma_f32_16x16x32_bf16 v[216:219], v[156:159], a[244:247], v[216:219]\n"
    "v_bfi_b32 v64, s31, v188, v64\n"
    "v_bfi_b32 v65, s31, v188, v65\n"
    "v_mfma_f32_16x16x32_bf16 v[232:235], v[144:147], a[200:203], v[232:235]\n"
    "ds_read_b128 a[144:147], v184 offset:4096\n"
    "v_bfi_b32 v66, s31, v188, v66\n"
    "v_bfi_b32 v67, s31, v188, v67\n"
    "v_mfma_f32_16x16x32_bf16 v[232:235], v[148:151], a[216:219], v[232:235]\n"
    "v_bfi_b32 v68, s31, v188, v68\n"
    "v_bfi_b32 v69, s31, v188, v69\n"
    "v_mfma_f32_16x16x32_bf16 v[232:235], v[152:155], a[232:235], v[232:235]\n"
    "ds_read_b128 a[148:151], v184 offset:5120\n"
    "v_bfi_b32 v70, s31, v188, v70\n"
    "v_bfi_b32 v71, s31, v188, v71\n"
    "v_mfma_f32_16x16x32_bf16 v[232:235], v[156:159], a[248:251], v[232:235]\n"
    "v_bfi_b32 v72, s31, v188, v72\n"
    "v_bfi_b32 v73, s31, v188, v73\n"
    "v_mfma_f32_16x16x32_bf16 v[248:251], v[144:147], a[204:207], v[248:251]\n"
    "ds_read_b128 a[152:155], v184 offset:6144\n"
    "v_bfi_b32 v74, s31, v188, v74\n"
    "v_mfma_f32_16x16x32_bf16 v[248:251], v[148:151], a[220:223], v[248:251]\n"
    "v_bfi_b32 v75, s31, v188, v75\n"
    "v_bfi_b32 v76, s31, v188, v76\n"
    "v_bfi_b32 v77, s31, v188, v77\n"
    "v_mfma_f32_16x16x32_bf16 v[248:251], v[152:155], a[236:239], v[248:251]\n"
    "ds_read_b128 a[156:159], v184 offset:7168\n"
    "v_bfi_b32 v78, s31, v188, v78\n"
    "v_mfma_f32_16x16x32_bf16 v[248:251], v[156:159], a[252:255], v[248:251]\n"
    "v_bfi_b32 v79, s31, v188, v79\n"
    "v_bfe_u32 v84, v190, 6, 2\n"
    "v_mfma_f32_16x16x32_bf16 v[204:207], v[160:163], a[192:195], v[204:207]\n"
    "ds_read_b128 a[160:163], v184 offset:8192\n"
    "v_lshlrev_b32 v84, 7, v84\n"
    "v_lshrrev_b32 v85, 8, v190\n"
    "v_mfma_f32_16x16x32_bf16 v[204:207], v[164:167], a[208:211], v[204:207]\n"
    "v_lshl_or_b32 v84, v85, 5, v84\n"
    "v_bfe_u32 v86, v190, 4, 2\n"
    "v_mfma_f32_16x16x32_bf16 v[204:207], v[168:171], a[224:227], v[204:207]\n"
    "ds_read_b128 a[164:167], v184 offset:9216\n"
    "v_lshl_or_b32 v84, v86, 3, v84\n"
    "v_add_u32 v84, s19, v84\n"
    "v_mfma_f32_16x16x32_bf16 v[204:207], v[172:175], a[240:243], v[204:207]\n"
    "v_mul_u32_u24 v85, 768, v85\n"
    "v_add_u32 v85, v85, v190\n"
    "v_mfma_f32_16x16x32_bf16 v[220:223], v[160:163], a[196:199], v[220:223]\n"
    "ds_read_b128 a[168:171], v184 offset:10240\n"
    "v_lshrrev_b32 v89, 4, v190\n"
    "v_mfma_f32_16x16x32_bf16 v[220:223], v[164:167], a[212:215], v[220:223]\n"
    "v_mul_u32_u24 v89, s22, v89\n"
    "v_lshrrev_b32 v89, 2, v89\n"
    "v_add_u32 v186, s89, v190\n"
    "v_mfma_f32_16x16x32_bf16 v[220:223], v[168:171], a[228:231], v[220:223]\n"
    "ds_read_b128 a[172:175], v184 offset:11264\n"
    "v_lshrrev_b32 v87, 1, v190\n"
    "v_mfma_f32_16x16x32_bf16 v[220:223], v[172:175], a[244:247], v[220:223]\n"
    "v_add_u32 v87, s90, v87\n"
    "v_mov_b32 v86, 0x67800000\n"
    "v_mfma_f32_16x16x32_bf16 v[236:239], v[160:163], a[200:203], v[236:239]\n"
    "ds_read_b128 a[176:179], v184 offset:12288\n"
    "v_bfe_u32 v92, v190, 4, 2\n"
    "v_min_u32 v92, 1, v92\n"
    "v_mfma_f32_16x16x32_bf16 v[236:239], v[164:167], a[216:219], v[236:239]\n"
    "v_subrev_u32 v92, 1, v92\n"
    "v_and_b32 v92, 0x3f803f80, v92\n"
    "v_mfma_f32_16x16x32_bf16 v[236:239], v[168:171], a[232:235], v[236:239]\n"
    "ds_read_b128 a[180:183], v184 offset:13312\n"
    "v_mov_b32 v93, v92\n"
    "v_mov_b64 v[94:95], v[92:93]\n"
    "v_mfma_f32_16x16x32_bf16 v[236:239], v[172:175], a[248:251], v[236:239]\n"
    "v_mov_b64 v[96:97], 0\n"
    "v_mov_b64 v[98:99], 0\n"
    "v_mfma_f32_16x16x32_bf16 v[252:255], v[160:163], a[204:207], v[252:255]\n"
    "ds_read_b128 a[184:187], v184 offset:14336\n"
    "v_mov_b64 v[100:101], 0\n"
    "v_mfma_f32_16x16x32_bf16 v[252:255], v[164:167], a[220:223], v[252:255]\n"
    "v_mov_b64 v[102:103], 0\n"
    "v_mov_b64 v[104:105], 0\n"
    "v_mov_b64 v[106:107], 0\n"
    "v_mfma_f32_16x16x32_bf16 v[252:255], v[168:171], a[236:239], v[252:255]\n"
    "ds_read_b128 a[188:191], v184 offset:15360\n"
    "v_mov_b64 v[108:109], 0\n"
    "v_mfma_f32_16x16x32_bf16 v[252:255], v[172:175], a[252:255], v[252:255]\n"
    "v_mov_b64 v[110:111], 0\n"
    "s_waitcnt lgkmcnt(12)\n"
    "v_mfma_f32_16x16x32_bf16 v[112:115], a[128:131], v[0:3], 0\n"
    "v_cvt_pk_bf16_f32 v8, v192, v193\n"
    "v_cvt_pk_bf16_f32 v9, v194, v195\n"
    "v_cvt_pk_bf16_f32 v10, v196, v197\n"
    "v_mfma_f32_16x16x32_bf16 v[116:119], a[128:131], v[16:19], 0\n"
    "s_or_b32 s74, s71, s72\n"
    "v_cvt_pk_bf16_f32 v11, v198, v199\n"
    "v_cvt_pk_bf16_f32 v24, v208, v209\n"
    "v_cvt_pk_bf16_f32 v25, v210, v211\n"
    "v_mfma_f32_16x16x32_bf16 v[120:123], a[128:131], v[32:35], 0\n"
    "v_cvt_pk_bf16_f32 v26, v212, v213\n"
    "v_cvt_pk_bf16_f32 v27, v214, v215\n"
    "v_cvt_pk_bf16_f32 v40, v224, v225\n"
    "v_mfma_f32_16x16x32_bf16 v[124:127], a[128:131], v[48:51], 0\n"
    "v_cvt_pk_bf16_f32 v41, v226, v227\n"
    "v_cvt_pk_bf16_f32 v42, v228, v229\n"
    "v_cvt_pk_bf16_f32 v43, v230, v231\n"
    "v_mfma_f32_16x16x32_bf16 v[112:115], a[132:135], v[4:7], v[112:115]\n"
    "v_cvt_pk_bf16_f32 v56, v240, v241\n"
    "v_cvt_pk_bf16_f32 v57, v242, v243\n"
    "v_cvt_pk_bf16_f32 v58, v244, v245\n"
    "v_mfma_f32_16x16x32_bf16 v[116:119], a[132:135], v[20:23], v[116:119]\n"
    "v_cvt_pk_bf16_f32 v59, v246, v247\n"
    "v_cvt_pk_bf16_f32 v12, v200, v201\n"
    "v_cvt_pk_bf16_f32 v13, v202, v203\n"
    "v_mfma_f32_16x16x32_bf16 v[120:123], a[132:135], v[36:39], v[120:123]\n"
    "v_cvt_pk_bf16_f32 v14, v204, v205\n"
    "v_cvt_pk_bf16_f32 v15, v206, v207\n"
    "v_cvt_pk_bf16_f32 v28, v216, v217\n"
    "v_mfma_f32_16x16x32_bf16 v[124:127], a[132:135], v[52:55], v[124:127]\n"
    "v_cvt_pk_bf16_f32 v29, v218, v219\n"
    "v_cvt_pk_bf16_f32 v30, v220, v221\n"
    "v_cvt_pk_bf16_f32 v31, v222, v223\n"
    "v_mfma_f32_16x16x32_bf16 v[112:115], a[136:139], v[8:11], v[112:115]\n"
    "v_cvt_pk_bf16_f32 v44, v232, v233\n"
    "v_cvt_pk_bf16_f32 v45, v234, v235\n"
    "v_cvt_pk_bf16_f32 v46, v236, v237\n"
    "v_mfma_f32_16x16x32_bf16 v[116:119], a[136:139], v[24:27], v[116:119]\n"
    "v_cvt_pk_bf16_f32 v47, v238, v239\n"
    "v_cvt_pk_bf16_f32 v60, v248, v249\n"
    "v_cvt_pk_bf16_f32 v61, v250, v251\n"
    "v_mfma_f32_16x16x32_bf16 v[120:123], a[136:139], v[40:43], v[120:123]\n"
    "v_cvt_pk_bf16_f32 v62, v252, v253\n"
    "v_cvt_pk_bf16_f32 v63, v254, v255\n"
    "v_mfma_f32_16x16x32_bf16 v[124:127], a[136:139], v[56:59], v[124:127]\n"
    "s_nop 0\n"
    "v_mfma_f32_16x16x32_bf16 v[112:115], a[140:143], v[12:15], v[112:115]\n"
    "ds_write_b128 v186, v[32:35] offset:0\n"
    "v_mfma_f32_16x16x32_bf16 v[116:119], a[140:143], v[28:31], v[116:119]\n"
    "ds_write_b128 v186, v[36:39] offset:1024\n"
    "v_mfma_f32_16x16x32_bf16 v[120:123], a[140:143], v[44:47], v[120:123]\n"
    "ds_write_b128 v186, v[48:51] offset:4096\n"
    "v_mfma_f32_16x16x32_bf16 v[124:127], a[140:143], v[60:63], v[124:127]\n"
    "ds_write_b128 v186, v[52:55] offset:5120\n"
    "s_cmp_lg_u32 s74, 0\n"
    "s_cbranch_scc1 L_at_p1\n"
    "s_nop 3\n"
    "v_min_f32 v112, v64, v112\n"
    "v_min_f32 v113, v65, v113\n"
    "v_min_f32 v114, v66, v114\n"
    "v_min_f32 v115, v67, v115\n"
    "L_at_p1:\n"
    "s_waitcnt lgkmcnt(4)\n"
    "v_mfma_f32_16x16x32_bf16 v[192:195], a[128:131], v[0:3], 0\n"
    "s_add_u32 s85, s83, 0x80000\n"
    "ds_write_b128 v186, v[40:43] offset:2048\n"
    "v_mfma_f32_16x16x32_bf16 v[192:195], a[132:135], v[4:7], v[192:195]\n"
    "s_add_u32 s86, s84, 0x80000\n"
    "v_xor_b32 v184, 0x30000, v184\n"
    "v_xor_b32 v185, 0x10000, v185\n"
    "v_max3_f32 v224, v112, v113, v114\n"
    "v_mfma_f32_16x16x32_bf16 v[192:195], a[136:139], v[8:11], v[192:195]\n"
    "s_add_u32 s89, s80, 3\n"
    "v_max3_f32 v225, v116, v117, v118\n"
    "v_max3_f32 v226, v120, v121, v122\n"
    "v_max3_f32 v227, v124, v125, v126\n"
    "v_mfma_f32_16x16x32_bf16 v[192:195], a[140:143], v[12:15], v[192:195]\n"
    "s_sub_i32 s90, s89, s81\n"
    "v_max_f32 v224, v224, v115\n"
    "v_max_f32 v225, v225, v119\n"
    "v_max_f32 v226, v226, v123\n"
    "v_mfma_f32_16x16x32_bf16 v[196:199], a[144:147], v[0:3], 0\n"
    "s_max_i32 s90, s90, 0\n"
    "ds_write_b128 v186, v[44:47] offset:3072\n"
    "v_max_f32 v227, v227, v127\n"
    "v_mov_b32 v228, v224\n"
    "v_mfma_f32_16x16x32_bf16 v[196:199], a[148:151], v[4:7], v[196:199]\n"
    "s_add_u32 s90, s90, s89\n"
    "v_mov_b32 v229, v225\n"
    "v_mov_b32 v230, v226\n"
    "v_mov_b32 v231, v227\n"
    "v_mfma_f32_16x16x32_bf16 v[196:199], a[152:155], v[8:11], v[196:199]\n"
    "s_lshl_b32 s90, s90, 14\n"
    "v_permlane32_swap_b32 v224, v228\n"
    "v_mfma_f32_16x16x32_bf16 v[196:199], a[156:159], v[12:15], v[196:199]\n"
    "s_add_u32 s90, s90, s57\n"
    "v_permlane32_swap_b32 v225, v229\n"
    "v_permlane32_swap_b32 v226, v230\n"
    "v_mfma_f32_16x16x32_bf16 v[200:203], a[160:163], v[0:3], 0\n"
    "s_cmp_le_u32 s89, 16\n"
    "ds_write_b128 v186, v[56:59] offset:6144\n"
    "v_permlane32_swap_b32 v227, v231\n"
    "v_mfma_f32_16x16x32_bf16 v[200:203], a[164:167], v[4:7], v[200:203]\n"
    "s_cselect_b32 s83, s90, 0x40000000\n"
    "v_max_f32 v224, v224, v228\n"
    "v_max_f32 v225, v225, v229\n"
    "v_max_f32 v226, v226, v230\n"
    "v_mfma_f32_16x16x32_bf16 v[200:203], a[168:171], v[8:11], v[200:203]\n"
    "s_add_u32 s84, s83, 16384\n"
    "v_max_f32 v227, v227, v231\n"
    "v_mov_b32 v228, v224\n"
    "v_mov_b32 v229, v225\n"
    "v_mfma_f32_16x16x32_bf16 v[200:203], a[172:175], v[12:15], v[200:203]\n"
    "s_cmp_ge_u32 s89, s81\n"
    "v_mov_b32 v230, v226\n"
    "v_mov_b32 v231, v227\n"
    "v_mfma_f32_16x16x32_bf16 v[204:207], a[176:179], v[0:3], 0\n"
    "s_cselect_b32 s84, s84, 0x40000000\n"
    "ds_write_b128 v186, v[60:63] offset:7168\n"
    "v_permlane16_swap_b32 v224, v228\n"
    "v_mfma_f32_16x16x32_bf16 v[204:207], a[180:183], v[4:7], v[204:207]\n"
    "s_xor_b32 s87, s87, 0x30000\n"
    "v_permlane16_swap_b32 v225, v229\n"
    "v_permlane16_swap_b32 v226, v230\n"
    "v_mfma_f32_16x16x32_bf16 v[204:207], a[184:187], v[8:11], v[204:207]\n"
    "s_xor_b32 s88, s88, 0x10000\n"
    "v_permlane16_swap_b32 v227, v231\n"
    "v_max_f32 v224, v224, v228\n"
    "v_mfma_f32_16x16x32_bf16 v[204:207], a[188:191], v[12:15], v[204:207]\n"
    "s_add_u32 s80, s80, 1\n"
    "v_max_f32 v225, v225, v229\n"
    "v_max_f32 v226, v226, v230\n"
    "v_max_f32 v227, v227, v231\n"
    "s_bitcmp1_b32 s13, 0\n"
    "s_cbranch_scc1 L_exact\n"
    "L_at_havemax:\n"
    "v_sub_f32 v232, 0, v224\n"
    "v_sub_f32 v233, 0, v225\n"
    "v_sub_f32 v234, 0, v226\n"
    "v_sub_f32 v235, 0, v227\n"
    "v_mov_b32 v128, v233\n"
    "v_mov_b32 v129, v233\n"
    "v_mov_b64 v[130:131], v[128:129]\n"
    "v_mov_b64 v[132:133], v[128:129]\n"
    "v_mov_b64 v[134:135], v[128:129]\n"
    "v_mov_b64 v[136:137], v[128:129]\n"
    "v_mov_b64 v[138:139], v[128:129]\n"
    "v_mov_b64 v[140:141], v[128:129]\n"
    "v_mov_b64 v[142:143], v[128:129]\n"
    "s_cmp_lg_u32 s72, 0\n"
    "ds_write_b64 v87, v[234:235]\n"
    "s_cbranch_scc1 L_at_p2\n"
    "v_min_f32 v192, v64, v192\n"
    "v_min_f32 v193, v65, v193\n"
    "v_min_f32 v194, v66, v194\n"
    "v_min_f32 v195, v67, v195\n"
    "v_min_f32 v196, v68, v196\n"
    "v_min_f32 v197, v69, v197\n"
    "v_min_f32 v198, v70, v198\n"
    "v_min_f32 v199, v71, v199\n"
    "v_min_f32 v200, v72, v200\n"
    "v_min_f32 v201, v73, v201\n"
    "v_min_f32 v202, v74, v202\n"
    "v_min_f32 v203, v75, v203\n"
    "v_min_f32 v204, v76, v204\n"
    "v_min_f32 v205, v77, v205\n"
    "v_min_f32 v206, v78, v206\n"
    "v_min_f32 v207, v79, v207\n"
    "L_at_p2:\n"
    "v_mfma_f32_16x16x32_bf16 v[208:211], a[128:131], v[16:19], v[128:131]\n"
    "ds_read_b128 a[192:195], v185 offset:0\n"
    "v_add_f32 v192, v192, v232\n"
    "v_exp_f32_e32 v240, v192\n"
    "v_mov_b32 v144, v234\n"
    "v_mov_b32 v145, v234\n"
    "v_mfma_f32_16x16x32_bf16 v[208:211], a[132:135], v[20:23], v[208:211]\n"
    "ds_read_b128 a[196:199], v185 offset:1024\n"
    "v_add_f32 v193, v193, v232\n"
    "v_exp_f32_e32 v241, v193\n"
    "v_mov_b64 v[146:147], v[144:145]\n"
    "v_mov_b64 v[148:149], v[144:145]\n"
    "v_mfma_f32_16x16x32_bf16 v[208:211], a[136:139], v[24:27], v[208:211]\n"
    "ds_read_b128 a[200:203], v185 offset:2048\n"
    "v_add_f32 v194, v194, v232\n"
    "v_exp_f32_e32 v242, v194\n"
    "v_mov_b64 v[150:151], v[144:145]\n"
    ".p2align 12\n"
    "L_pad7:\n"
    "s_cbranch_execz L_pad11\n"
    "v_mov_b64 v[152:153], v[144:145]\n"
    "v_cvt_pk_bf16_f32 v224, v240, v241\n"
    "v_mfma_f32_16x16x32_bf16 v[208:211], a[140:143], v[28:31], v[208:211]\n"
    "ds_read_b128 a[204:207], v185 offset:3072\n"
    "v_add_f32 v195, v195, v232\n"
    "v_exp_f32_e32 v243, v195\n"
    "v_mov_b64 v[154:155], v[144:145]\n"
    "v_mov_b64 v[156:157], v[144:145]\n"
    "v_mfma_f32_16x16x32_bf16 v[212:215], a[144:147], v[16:19], v[132:135]\n"
    "ds_read_b128 a[208:211], v185 offset:4096\n"
    "v_add_f32 v196, v196, v232\n"
    "v_exp_f32_e32 v240, v196\n"
    "v_mov_b64 v[158:159], v[144:145]\n"
    "v_mov_b32 v160, v235\n"
    "v_cvt_pk_bf16_f32 v225, v242, v243\n"
    "v_mfma_f32_16x16x32_bf16 v[212:215], a[148:151], v[20:23], v[212:215]\n"
    "ds_read_b128 a[212:215], v185 offset:5120\n"
    "v_add_f32 v197, v197, v232\n"
    "v_exp_f32_e32 v241, v197\n"
    "v_mov_b32 v161, v235\n"
    "v_mov_b64 v[162:163], v[160:161]\n"
    "v_mfma_f32_16x16x32_bf16 v[212:215], a[152:155], v[24:27], v[212:215]\n"
    "ds_read_b128 a[216:219], v185 offset:6144\n"
    "v_add_f32 v198, v198, v232\n"
    "v_exp_f32_e32 v242, v198\n"
    "v_mov_b64 v[164:165], v[160:161]\n"
    "v_mov_b64 v[166:167], v[160:161]\n"
    "v_cvt_pk_bf16_f32 v226, v240, v241\n"
    "v_mfma_f32_16x16x32_bf16 v[212:215], a[156:159], v[28:31], v[212:215]\n"
    "ds_read_b128 a[220:223], v185 offset:7168\n"
    "v_add_f32 v199, v199, v232\n"
    "v_exp_f32_e32 v243, v199\n"
    "v_mov_b64 v[168:169], v[160:161]\n"
    "v_mov_b64 v[170:171], v[160:161]\n"
    "v_mfma_f32_16x16x32_bf16 v[216:219], a[160:163], v[16:19], v[136:139]\n"
    "ds_read_b128 a[224:227], v185 offset:8192\n"
    "v_add_f32 v200, v200, v232\n"
    "v_exp_f32_e32 v240, v200\n"
    "v_mov_b64 v[172:173], v[160:161]\n"
    "v_mov_b64 v[174:175], v[160:161]\n"
    "v_cvt_pk_bf16_f32 v227, v242, v243\n"
    "v_mfma_f32_16x16x32_bf16 v[216:219], a[164:167], v[20:23], v[216:219]\n"
    "s_cmp_lt_u32 s85, 0x40000000\n"
    "ds_read_b128 a[228:231], v185 offset:9216\n"
    "v_add_f32 v201, v201, v232\n"
    "v_exp_f32_e32 v241, v201\n"
    "v_mov_b32 v112, v232\n"
    "v_mov_b32 v113, v232\n"
    "v_mfma_f32_16x16x32_bf16 v[216:219], a[168:171], v[24:27], v[216:219]\n"
    "s_cselect_b32 m0, s88, s59\n"
    "v_add_f32 v202, v202, v232\n"
    "v_exp_f32_e32 v242, v202\n"
    "v_mov_b64 v[114:115], v[112:113]\n"
    "v_mov_b64 v[116:117], v[112:113]\n"
    "v_cvt_pk_bf16_f32 v228, v240, v241\n"
    "v_mfma_f32_16x16x32_bf16 v[216:219], a[172:175], v[28:31], v[216:219]\n"
    "buffer_load_dwordx4 v190, s[44:47], s85 offen offset:0 sc1 lds\n"
    "v_add_f32 v203, v203, v232\n"
    "v_exp_f32_e32 v243, v203\n"
    "v_mov_b64 v[118:119], v[112:113]\n"
    "v_mov_b64 v[120:121], v[112:113]\n"
    "v_mfma_f32_16x16x32_bf16 v[220:223], a[176:179], v[16:19], v[140:143]\n"
    "v_add_f32 v204, v204, v232\n"
    "v_exp_f32_e32 v240, v204\n"
    "v_mov_b64 v[122:123], v[112:113]\n"
    "v_mov_b64 v[124:125], v[112:113]\n"
    "v_cvt_pk_bf16_f32 v229, v242, v243\n"
    "v_mfma_f32_16x16x32_bf16 v[220:223], a[180:183], v[20:23], v[220:223]\n"
    "buffer_load_dwordx4 v190, s[44:47], s85 offen offset:1024 sc1 lds\n"
    "v_add_f32 v205, v205, v232\n"
    "v_exp_f32_e32 v241, v205\n"
    "v_mov_b64 v[126:127], v[112:113]\n"
    "v_xor_b32 v87, 512, v87\n"
    "v_mfma_f32_16x16x32_bf16 v[220:223], a[184:187], v[24:27], v[220:223]\n"
    "v_add_f32 v206, v206, v232\n"
    "v_exp_f32_e32 v242, v206\n"
    "v_xor_b32 v186, 0x2000, v186\n"
    "v_lshlrev_b32 v252, 0, v190\n"
    "v_cvt_pk_bf16_f32 v230, v240, v241\n"
    "v_mfma_f32_16x16x32_bf16 v[220:223], a[188:191], v[28:31], v[220:223]\n"
    "buffer_load_dwordx4 v190, s[44:47], s85 offen offset:2048 sc1 lds\n"
    "v_add_f32 v207, v207, v232\n"
    "v_exp_f32_e32 v243, v207\n"
    "s_waitcnt lgkmcnt(10)\n"
    "v_cvt_pk_bf16_f32 v231, v242, v243\n"
    "s_barrier\n"
    "ds_read_b64 v[248:249], v87\n"
    "ds_read_b128 v[80:83], v186 offset:7168\n"
    "s_branch L_at_s1\n"
    ".p2align 6\n"
    "L_at_top:\n"
    "v_mfma_f32_16x16x32_bf16 v[192:195], a[128:131], v[0:3], v[112:115]\n"
    "s_cmp_lt_u32 s80, 8\n"
    "v_exp_f32_e32 v240, v208\n"
    "v_mfma_f32_16x16x32_bf16 v[192:195], a[132:135], v[4:7], v[192:195]\n"
    "s_cselect_b32 s75, s20, 0x40000000\n"
    "v_xor_b32 v185, 0x10000, v185\n"
    "v_mfma_f32_16x16x32_bf16 v[192:195], a[136:139], v[8:11], v[192:195]\n"
    "s_cselect_b32 m0, s19, s59\n"
    "v_exp_f32_e32 v241, v209\n"
    "v_mfma_f32_16x16x32_bf16 v[192:195], a[140:143], v[12:15], v[192:195]\n"
    "s_sub_u32 s74, s80, 1\n"
    "v_cvt_pk_bf16_f32 v232, v240, v241\n"
    "v_mfma_f32_16x16x32_bf16 v[196:199], a[144:147], v[0:3], v[116:119]\n"
    "s_cmp_lt_u32 s74, 8\n"
    "v_exp_f32_e32 v242, v210\n"
    "v_mfma_f32_16x16x32_bf16 v[196:199], a[148:151], v[4:7], v[196:199]\n"
    "s_cselect_b32 s74, s21, 0x40000000\n"
    "buffer_load_dwordx4 v181, s[36:39], s75 offen lds\n"
    "v_mfma_f32_16x16x32_bf16 v[196:199], a[152:155], v[8:11], v[196:199]\n"
    "s_cmp_eq_u32 s80, 14\n"
    "v_exp_f32_e32 v243, v211\n"
    "v_mfma_f32_16x16x32_bf16 v[196:199], a[156:159], v[12:15], v[196:199]\n"
    "s_waitcnt lgkmcnt(0)\n"
    "s_cselect_b32 s61, s60, 0x30000\n"
    "v_cvt_pk_bf16_f32 v233, v242, v243\n"
    "v_mfma_f32_16x16x32_bf16 v[200:203], a[160:163], v[0:3], v[120:123]\n"
    "s_cselect_b32 s53, s58, 0x40000000\n"
    "v_exp_f32_e32 v240, v212\n"
    "v_mfma_f32_16x16x32_bf16 v[200:203], a[164:167], v[4:7], v[200:203]\n"
    "s_add_u32 s85, s83, 0x80000\n"
    "buffer_store_dwordx4 v[176:179], v85, s[76:79], s74 offen sc1\n"
    "v_mfma_f32_16x16x32_bf16 v[200:203], a[168:171], v[8:11], v[200:203]\n"
    "s_add_u32 s86, s84, 0x80000\n"
    "v_exp_f32_e32 v241, v213\n"
    "v_mfma_f32_16x16x32_bf16 v[200:203], a[172:175], v[12:15], v[200:203]\n"
    "s_add_u32 s89, s80, 3\n"
    "v_cvt_pk_bf16_f32 v234, v240, v241\n"
    "v_mfma_f32_16x16x32_bf16 v[204:207], a[176:179], v[0:3], v[124:127]\n"
    "s_sub_i32 s90, s89, s81\n"
    "v_exp_f32_e32 v242, v214\n"
    "v_mfma_f32_16x16x32_bf16 v[204:207], a[180:183], v[4:7], v[204:207]\n"
    "s_max_i32 s90, s90, 0\n"
    "buffer_load_dwordx4 v[88:91], v252, s[76:79], s53 offen sc1\n"
    "v_mfma_f32_16x16x32_bf16 v[204:207], a[184:187], v[8:11], v[204:207]\n"
    "s_add_u32 s90, s90, s89\n"
    "v_exp_f32_e32 v243, v215\n"
    "v_mfma_f32_16x16x32_bf16 v[204:207], a[188:191], v[12:15], v[204:207]\n"
    "s_lshl_b32 s90, s90, 14\n"
    "v_cvt_pk_bf16_f32 v235, v242, v243\n"
    "v_mfma_f32_16x16x32_bf16 a[64:67], a[192:195], v[224:227], a[64:67]\n"
    "s_add_u32 s90, s90, s57\n"
    "v_exp_f32_e32 v240, v216\n"
    "v_mfma_f32_16x16x32_bf16 a[64:67], a[196:199], v[228:231], a[64:67]\n"
    "s_cmp_le_u32 s89, 16\n"
    "v_xor_b32 v184, s61, v184\n"
    "v_mfma_f32_16x16x32_bf16 a[68:71], a[200:203], v[224:227], a[68:71]\n"
    "s_cselect_b32 s83, s90, 0x40000000\n"
    "v_exp_f32_e32 v241, v217\n"
    "v_mfma_f32_16x16x32_bf16 a[68:71], a[204:207], v[228:231], a[68:71]\n"
    "s_add_u32 s84, s83, 16384\n"
    "v_cvt_pk_bf16_f32 v236, v240, v241\n"
    "v_mfma_f32_16x16x32_bf16 a[72:75], a[208:211], v[224:227], a[72:75]\n"
    "s_cmp_ge_u32 s89, s81\n"
    "v_exp_f32_e32 v242, v218\n"
    "v_mfma_f32_16x16x32_bf16 a[72:75], a[212:215], v[228:231], a[72:75]\n"
    "s_cselect_b32 s84, s84, 0x40000000\n"
    "v_mfma_f32_16x16x32_bf16 a[76:79], a[216:219], v[224:227], a[76:79]\n"
    "s_xor_b32 s87, s87, 0x30000\n"
    "v_exp_f32_e32 v243, v219\n"
    "v_mfma_f32_16x16x32_bf16 a[76:79], a[220:223], v[228:231], a[76:79]\n"
    "s_xor_b32 s88, s88, 0x10000\n"
    "v_cvt_pk_bf16_f32 v237, v242, v243\n"
    "v_mfma_f32_16x16x32_bf16 a[80:83], a[224:227], v[224:227], a[80:83]\n"
    "s_add_u32 s80, s80, 1\n"
    "v_exp_f32_e32 v240, v220\n"
    "v_mfma_f32_16x16x32_bf16 a[80:83], a[228:231], v[228:231], a[80:83]\n"
    "v_mfma_f32_16x16x32_bf16 a[84:87], a[232:235], v[224:227], a[84:87]\n"
    "v_exp_f32_e32 v241, v221\n"
    "v_mfma_f32_16x16x32_bf16 a[84:87], a[236:239], v[228:231], a[84:87]\n"
    "v_cvt_pk_bf16_f32 v238, v240, v241\n"
    "v_mfma_f32_16x16x32_bf16 a[88:91], a[240:243], v[224:227], a[88:91]\n"
    "v_exp_f32_e32 v242, v222\n"
    "v_mfma_f32_16x16x32_bf16 a[88:91], a[244:247], v[228:231], a[88:91]\n"
    "v_mfma_f32_16x16x32_bf16 a[92:95], a[248:251], v[224:227], a[92:95]\n"
    "v_exp_f32_e32 v243, v223\n"
    "v_mfma_f32_16x16x32_bf16 a[92:95], a[252:255], v[228:231], a[92:95]\n"
    "v_cvt_pk_bf16_f32 v239, v242, v243\n"
    "v_mfma_f32_16x16x32_bf16 v[104:107], v[92:95], v[224:227], v[104:107]\n"
    "v_mfma_f32_16x16x32_bf16 v[104:107], v[92:95], v[228:231], v[104:107]\n"
    "v_mfma_f32_16x16x32_bf16 v[208:211], a[128:131], v[16:19], v[128:131]\n"
    "v_exp_f32_e32 v240, v192\n"
    "v_mfma_f32_16x16x32_bf16 v[208:211], a[132:135], v[20:23], v[208:211]\n"
    "s_cmp_eq_u32 s80, 4\n"
    "v_mfma_f32_16x16x32_bf16 v[208:211], a[136:139], v[24:27], v[208:211]\n"
    "s_cselect_b32 s74, 5, 1\n"
    "v_exp_f32_e32 v241, v193\n"
    "v_mfma_f32_16x16x32_bf16 v[208:211], a[140:143], v[28:31], v[208:211]\n"
    "s_mul_i32 s74, s74, s22\n"
    "v_cvt_pk_bf16_f32 v224, v240, v241\n"
    "v_mfma_f32_16x16x32_bf16 v[212:215], a[144:147], v[16:19], v[132:135]\n"
    "s_add_u32 s20, s20, s74\n"
    "v_exp_f32_e32 v242, v194\n"
    "v_mfma_f32_16x16x32_bf16 v[212:215], a[148:151], v[20:23], v[212:215]\n"
    "s_cmp_eq_u32 s80, 5\n"
    "v_mfma_f32_16x16x32_bf16 v[212:215], a[152:155], v[24:27], v[212:215]\n"
    "s_cselect_b32 s74, 13, 1\n"
    "v_exp_f32_e32 v243, v195\n"
    "v_mfma_f32_16x16x32_bf16 v[212:215], a[156:159], v[28:31], v[212:215]\n"
    "s_lshl_b32 s74, s74, 8\n"
    "v_cvt_pk_bf16_f32 v225, v242, v243\n"
    "v_mfma_f32_16x16x32_bf16 v[216:219], a[160:163], v[16:19], v[136:139]\n"
    "s_add_u32 s21, s21, s74\n"
    "v_exp_f32_e32 v240, v196\n"
    "v_mfma_f32_16x16x32_bf16 v[216:219], a[164:167], v[20:23], v[216:219]\n"
    "v_mfma_f32_16x16x32_bf16 v[216:219], a[168:171], v[24:27], v[216:219]\n"
    "v_exp_f32_e32 v241, v197\n"
    "v_mfma_f32_16x16x32_bf16 v[216:219], a[172:175], v[28:31], v[216:219]\n"
    "v_cvt_pk_bf16_f32 v226, v240, v241\n"
    "v_mfma_f32_16x16x32_bf16 v[220:223], a[176:179], v[16:19], v[140:143]\n"
    "v_exp_f32_e32 v242, v198\n"
    "v_mfma_f32_16x16x32_bf16 v[220:223], a[180:183], v[20:23], v[220:223]\n"
    "v_mfma_f32_16x16x32_bf16 v[220:223], a[184:187], v[24:27], v[220:223]\n"
    "s_cmp_lg_u32 s92, 0\n"
    "v_exp_f32_e32 v243, v199\n"
    "v_mfma_f32_16x16x32_bf16 v[220:223], a[188:191], v[28:31], v[220:223]\n"
    "s_cbranch_scc1 L_at_nowait\n"
    "s_waitcnt vmcnt(3)\n"
    "L_at_nowait:\n"
    "v_cvt_pk_bf16_f32 v227, v242, v243\n"
    "v_mfma_f32_16x16x32_bf16 a[96:99], a[192:195], v[232:235], a[96:99]\n"
    "v_exp_f32_e32 v240, v200\n"
    "s_barrier\n"
    "v_mfma_f32_16x16x32_bf16 a[96:99], a[196:199], v[236:239], a[96:99]\n"
    "s_mov_b32 s92, 0\n"
    "ds_read_b128 a[192:195], v185 offset:0\n"
    "ds_read_b128 a[196:199], v185 offset:1024\n"
    "v_mfma_f32_16x16x32_bf16 a[100:103], a[200:203], v[232:235], a[100:103]\n"
    "v_exp_f32_e32 v241, v201\n"
    "v_mfma_f32_16x16x32_bf16 a[100:103], a[204:207], v[236:239], a[100:103]\n"
    "s_cmp_lt_u32 s85, 0x40000000\n"
    "ds_read_b128 a[200:203], v185 offset:2048\n"
    "v_cvt_pk_bf16_f32 v228, v240, v241\n"
    "v_mfma_f32_16x16x32_bf16 a[104:107], a[208:211], v[232:235], a[104:107]\n"
    "s_cselect_b32 m0, s88, s59\n"
    "v_exp_f32_e32 v242, v202\n"
    "v_mfma_f32_16x16x32_bf16 a[104:107], a[212:215], v[236:239], a[104:107]\n"
    "buffer_load_dwordx4 v190, s[44:47], s85 offen offset:0 sc1 lds\n"
    "v_mfma_f32_16x16x32_bf16 a[108:111], a[216:219], v[232:235], a[108:111]\n"
    "v_exp_f32_e32 v243, v203\n"
    "v_mfma_f32_16x16x32_bf16 a[108:111], a[220:223], v[236:239], a[108:111]\n"
    "ds_read_b128 a[204:207], v185 offset:3072\n"
    "v_cvt_pk_bf16_f32 v229, v242, v243\n"
    "v_mfma_f32_16x16x32_bf16 a[112:115], a[224:227], v[232:235], a[112:115]\n"
    "v_exp_f32_e32 v240, v204\n"
    "v_mfma_f32_16x16x32_bf16 a[112:115], a[228:231], v[236:239], a[112:115]\n"
    "ds_read_b128 a[208:211], v185 offset:4096\n"
    "ds_read_b128 a[212:215], v185 offset:5120\n"
    "v_mfma_f32_16x16x32_bf16 a[116:119], a[232:235], v[232:235], a[116:119]\n"
    "v_exp_f32_e32 v241, v205\n"
    "v_mfma_f32_16x16x32_bf16 a[116:119], a[236:239], v[236:239], a[116:119]\n"
    "ds_read_b128 a[216:219], v185 offset:6144\n"
    "v_cvt_pk_bf16_f32 v230, v240, v241\n"
    "v_mfma_f32_16x16x32_bf16 a[120:123], a[240:243], v[232:235], a[120:123]\n"
    "v_exp_f32_e32 v242, v206\n"
    "v_mfma_f32_16x16x32_bf16 a[120:123], a[244:247], v[236:239], a[120:123]\n"
    "buffer_load_dwordx4 v190, s[44:47], s85 offen offset:1024 sc1 lds\n"
    "v_mfma_f32_16x16x32_bf16 a[124:127], a[248:251], v[232:235], a[124:127]\n"
    "v_exp_f32_e32 v243, v207\n"
    "v_mfma_f32_16x16x32_bf16 a[124:127], a[252:255], v[236:239], a[124:127]\n"
    "ds_read_b128 a[220:223], v185 offset:7168\n"
    "v_cvt_pk_bf16_f32 v231, v242, v243\n"
    "v_mfma_f32_16x16x32_bf16 v[108:111], v[92:95], v[232:235], v[108:111]\n"
    "ds_read_b128 a[224:227], v185 offset:8192\n"
    "ds_read_b128 a[228:231], v185 offset:9216\n"
    "v_mfma_f32_16x16x32_bf16 v[108:111], v[92:95], v[236:239], v[108:111]\n"
    "buffer_load_dwordx4 v190, s[44:47], s85 offen offset:2048 sc1 lds\n"
    "L_at_s1:\n"
    "v_mfma_f32_16x16x32_bf16 v[192:195], a[128:131], v[32:35], v[144:147]\n"
    "v_exp_f32_e32 v240, v208\n"
    "v_mfma_f32_16x16x32_bf16 v[192:195], a[132:135], v[36:39], v[192:195]\n"
    "ds_read_b128 a[232:235], v185 offset:10240\n"
    "ds_read_b128 a[236:239], v185 offset:11264\n"
    "v_mfma_f32_16x16x32_bf16 v[192:195], a[136:139], v[40:43], v[192:195]\n"
    "v_exp_f32_e32 v241, v209\n"
    "v_mfma_f32_16x16x32_bf16 v[192:195], a[140:143], v[44:47], v[192:195]\n"
    "ds_read_b128 a[240:243], v185 offset:12288\n"
    "v_cvt_pk_bf16_f32 v232, v240, v241\n"
    "v_mfma_f32_16x16x32_bf16 v[196:199], a[144:147], v[32:35], v[148:151]\n"
    "v_exp_f32_e32 v242, v210\n"
    "v_mfma_f32_16x16x32_bf16 v[196:199], a[148:151], v[36:39], v[196:199]\n"
    "buffer_load_dwordx4 v190, s[44:47], s85 offen offset:3072 sc1 lds\n"
    "v_mfma_f32_16x16x32_bf16 v[196:199], a[152:155], v[40:43], v[196:199]\n"
    "v_exp_f32_e32 v243, v211\n"
    "v_mfma_f32_16x16x32_bf16 v[196:199], a[156:159], v[44:47], v[196:199]\n"
    "s_cmp_lt_u32 s83, 0x40000000\n"
    "ds_read_b128 a[244:247], v185 offset:13312\n"
    "v_cvt_pk_bf16_f32 v233, v242, v243\n"
    "v_mfma_f32_16x16x32_bf16 v[200:203], a[160:163], v[32:35], v[152:155]\n"
    "s_cselect_b32 m0, s87, s59\n"
    "v_exp_f32_e32 v240, v212\n"
    "v_mfma_f32_16x16x32_bf16 v[200:203], a[164:167], v[36:39], v[200:203]\n"
    "buffer_load_dwordx4 v190, s[44:47], s83 offen offset:0 sc1 lds\n"
    "v_mfma_f32_16x16x32_bf16 v[200:203], a[168:171], v[40:43], v[200:203]\n"
    "s_cmp_eq_u32 s80, 9\n"
    "v_exp_f32_e32 v241, v213\n"
    "v_mfma_f32_16x16x32_bf16 v[200:203], a[172:175], v[44:47], v[200:203]\n"
    "s_cselect_b32 s93, s23, 0x40000000\n"
    "ds_read_b128 a[248:251], v185 offset:14336\n"
    "v_cvt_pk_bf16_f32 v234, v240, v241\n"
    "v_mfma_f32_16x16x32_bf16 v[204:207], a[176:179], v[32:35], v[156:159]\n"
    "s_cmp_eq_u32 s80, s81\n"
    "v_exp_f32_e32 v242, v214\n"
    "v_mfma_f32_16x16x32_bf16 v[204:207], a[180:183], v[36:39], v[204:207]\n"
    "s_cselect_b32 s93, s15, s93\n"
    "buffer_load_dwordx4 v190, s[44:47], s83 offen offset:1024 sc1 lds\n"
    "v_mfma_f32_16x16x32_bf16 v[204:207], a[184:187], v[40:43], v[204:207]\n"
    "v_exp_f32_e32 v243, v215\n"
    "v_mfma_f32_16x16x32_bf16 v[204:207], a[188:191], v[44:47], v[204:207]\n"
    "s_waitcnt lgkmcnt(7)\n"
    "ds_read_b128 a[252:255], v185 offset:15360\n"
    "v_cvt_pk_bf16_f32 v235, v242, v243\n"
    "v_mfma_f32_16x16x32_bf16 a[0:3], a[192:195], v[224:227], a[0:3]\n"
    "v_exp_f32_e32 v240, v216\n"
    "v_mfma_f32_16x16x32_bf16 a[0:3], a[196:199], v[228:231], a[0:3]\n"
    "buffer_load_dwordx4 v190, s[44:47], s83 offen offset:2048 sc1 lds\n"
    "v_mfma_f32_16x16x32_bf16 a[4:7], a[200:203], v[224:227], a[4:7]\n"
    "v_exp_f32_e32 v241, v217\n"
    "v_mfma_f32_16x16x32_bf16 a[4:7], a[204:207], v[228:231], a[4:7]\n"
    "v_cvt_pk_bf16_f32 v236, v240, v241\n"
    "v_mfma_f32_16x16x32_bf16 a[8:11], a[208:211], v[224:227], a[8:11]\n"
    "v_exp_f32_e32 v242, v218\n"
    "v_mfma_f32_16x16x32_bf16 a[8:11], a[212:215], v[228:231], a[8:11]\n"
    "buffer_load_dwordx4 v190, s[44:47], s83 offen offset:3072 sc1 lds\n"
    "v_mfma_f32_16x16x32_bf16 a[12:15], a[216:219], v[224:227], a[12:15]\n"
    "s_add_u32 s89, s88, 16384\n"
    "v_exp_f32_e32 v243, v219\n"
    "v_mfma_f32_16x16x32_bf16 a[12:15], a[220:223], v[228:231], a[12:15]\n"
    "s_waitcnt lgkmcnt(2)\n"
    "s_cmp_lt_u32 s86, 0x40000000\n"
    "v_cvt_pk_bf16_f32 v237, v242, v243\n"
    "v_mfma_f32_16x16x32_bf16 a[16:19], a[224:227], v[224:227], a[16:19]\n"
    "s_cselect_b32 m0, s89, s59\n"
    "v_exp_f32_e32 v240, v220\n"
    "v_mfma_f32_16x16x32_bf16 a[16:19], a[228:231], v[228:231], a[16:19]\n"
    "buffer_load_dwordx4 v190, s[44:47], s86 offen offset:0 sc1 lds\n"
    "v_mfma_f32_16x16x32_bf16 a[20:23], a[232:235], v[224:227], a[20:23]\n"
    "s_waitcnt vmcnt(9)\n"
    "v_exp_f32_e32 v241, v221\n"
    "v_mfma_f32_16x16x32_bf16 a[20:23], a[236:239], v[228:231], a[20:23]\n"
    "ds_read_b64_tr_b16 v[176:177], v84 offset:0\n"
    "v_cvt_pk_bf16_f32 v238, v240, v241\n"
    "v_mfma_f32_16x16x32_bf16 a[24:27], a[240:243], v[224:227], a[24:27]\n"
    "v_exp_f32_e32 v242, v222\n"
    "v_mfma_f32_16x16x32_bf16 a[24:27], a[244:247], v[228:231], a[24:27]\n"
    "s_waitcnt lgkmcnt(1)\n"
    "buffer_load_dwordx4 v190, s[44:47], s86 offen offset:1024 sc1 lds\n"
    "v_mfma_f32_16x16x32_bf16 a[28:31], a[248:251], v[224:227], a[28:31]\n"
    "v_exp_f32_e32 v243, v223\n"
    "v_mfma_f32_16x16x32_bf16 a[28:31], a[252:255], v[228:231], a[28:31]\n"
    "ds_read_b64_tr_b16 v[178:179], v84 offset:512\n"
    "v_cvt_pk_bf16_f32 v239, v242, v243\n"
    "v_mfma_f32_16x16x32_bf16 v[96:99], v[92:95], v[224:227], v[96:99]\n"
    "buffer_store_dword v182, v180, s[76:79], s93 offen sc1\n"
    "v_mfma_f32_16x16x32_bf16 v[96:99], v[92:95], v[228:231], v[96:99]\n"
    "buffer_load_dwordx4 v190, s[44:47], s86 offen offset:2048 sc1 lds\n"
    "v_mfma_f32_16x16x32_bf16 v[208:211], a[128:131], v[48:51], v[160:163]\n"
    "v_exp_f32_e32 v240, v192\n"
    "v_mfma_f32_16x16x32_bf16 v[208:211], a[132:135], v[52:55], v[208:211]\n"
    "ds_read_b128 a[128:131], v184 offset:0\n"
    "ds_read_b128 a[132:135], v184 offset:1024\n"
    "v_mfma_f32_16x16x32_bf16 v[208:211], a[136:139], v[56:59], v[208:211]\n"
    "v_exp_f32_e32 v241, v193\n"
    "v_mfma_f32_16x16x32_bf16 v[208:211], a[140:143], v[60:63], v[208:211]\n"
    "ds_read_b128 a[136:139], v184 offset:2048\n"
    "v_cvt_pk_bf16_f32 v224, v240, v241\n"
    "v_mfma_f32_16x16x32_bf16 v[212:215], a[144:147], v[48:51], v[164:167]\n"
    "v_exp_f32_e32 v242, v194\n"
    "v_mfma_f32_16x16x32_bf16 v[212:215], a[148:151], v[52:55], v[212:215]\n"
    "buffer_load_dwordx4 v190, s[44:47], s86 offen offset:3072 sc1 lds\n"
    "v_mfma_f32_16x16x32_bf16 v[212:215], a[152:155], v[56:59], v[212:215]\n"
    "v_exp_f32_e32 v243, v195\n"
    "v_mfma_f32_16x16x32_bf16 v[212:215], a[156:159], v[60:63], v[212:215]\n"
    "ds_read_b128 a[140:143], v184 offset:3072\n"
    "v_cvt_pk_bf16_f32 v225, v242, v243\n"
    "v_mfma_f32_16x16x32_bf16 v[216:219], a[160:163], v[48:51], v[168:171]\n"
    "v_exp_f32_e32 v240, v196\n"
    "v_mfma_f32_16x16x32_bf16 v[216:219], a[164:167], v[52:55], v[216:219]\n"
    "ds_read_b128 a[144:147], v184 offset:4096\n"
    "ds_read_b128 a[148:151], v184 offset:5120\n"
    "v_mfma_f32_16x16x32_bf16 v[216:219], a[168:171], v[56:59], v[216:219]\n"
    "s_add_u32 s89, s87, 16384\n"
    "v_exp_f32_e32 v241, v197\n"
    "v_mfma_f32_16x16x32_bf16 v[216:219], a[172:175], v[60:63], v[216:219]\n"
    "s_cmp_lt_u32 s84, 0x40000000\n"
    "ds_read_b128 a[152:155], v184 offset:6144\n"
    "v_cvt_pk_bf16_f32 v226, v240, v241\n"
    "v_mfma_f32_16x16x32_bf16 v[220:223], a[176:179], v[48:51], v[172:175]\n"
    "s_cselect_b32 m0, s89, s59\n"
    "v_exp_f32_e32 v242, v198\n"
    "v_mfma_f32_16x16x32_bf16 v[220:223], a[180:183], v[52:55], v[220:223]\n"
    "buffer_load_dwordx4 v190, s[44:47], s84 offen offset:0 sc1 lds\n"
    "v_mfma_f32_16x16x32_bf16 v[220:223], a[184:187], v[56:59], v[220:223]\n"
    "v_exp_f32_e32 v243, v199\n"
    "v_mfma_f32_16x16x32_bf16 v[220:223], a[188:191], v[60:63], v[220:223]\n"
    "ds_read_b128 a[156:159], v184 offset:7168\n"
    "v_cvt_pk_bf16_f32 v227, v242, v243\n"
    "v_mfma_f32_16x16x32_bf16 a[32:35], a[192:195], v[232:235], a[32:35]\n"
    "v_exp_f32_e32 v240, v200\n"
    "v_mfma_f32_16x16x32_bf16 a[32:35], a[196:199], v[236:239], a[32:35]\n"
    "ds_read_b128 a[160:163], v184 offset:8192\n"
    "ds_read_b128 a[164:167], v184 offset:9216\n"
    "v_mfma_f32_16x16x32_bf16 a[36:39], a[200:203], v[232:235], a[36:39]\n"
    "v_exp_f32_e32 v241, v201\n"
    "v_mfma_f32_16x16x32_bf16 a[36:39], a[204:207], v[236:239], a[36:39]\n"
    "ds_read_b128 a[168:171], v184 offset:10240\n"
    "v_cvt_pk_bf16_f32 v228, v240, v241\n"
    "v_mfma_f32_16x16x32_bf16 a[40:43], a[208:211], v[232:235], a[40:43]\n"
    "v_exp_f32_e32 v242, v202\n"
    "v_mfma_f32_16x16x32_bf16 a[40:43], a[212:215], v[236:239], a[40:43]\n"
    "buffer_load_dwordx4 v190, s[44:47], s84 offen offset:1024 sc1 lds\n"
    "v_mfma_f32_16x16x32_bf16 a[44:47], a[216:219], v[232:235], a[44:47]\n"
    "v_exp_f32_e32 v243, v203\n"
    "v_mfma_f32_16x16x32_bf16 a[44:47], a[220:223], v[236:239], a[44:47]\n"
    "ds_read_b128 a[172:175], v184 offset:11264\n"
    "v_cvt_pk_bf16_f32 v229, v242, v243\n"
    "v_mfma_f32_16x16x32_bf16 a[48:51], a[224:227], v[232:235], a[48:51]\n"
    "v_exp_f32_e32 v240, v204\n"
    "v_mfma_f32_16x16x32_bf16 a[48:51], a[228:231], v[236:239], a[48:51]\n"
    "ds_read_b128 a[176:179], v184 offset:12288\n"
    "ds_read_b128 a[180:183], v184 offset:13312\n"
    "v_mfma_f32_16x16x32_bf16 a[52:55], a[232:235], v[232:235], a[52:55]\n"
    "v_exp_f32_e32 v241, v205\n"
    "v_mfma_f32_16x16x32_bf16 a[52:55], a[236:239], v[236:239], a[52:55]\n"
    "ds_read_b128 a[184:187], v184 offset:14336\n"
    "v_cvt_pk_bf16_f32 v230, v240, v241\n"
    "v_mfma_f32_16x16x32_bf16 a[56:59], a[240:243], v[232:235], a[56:59]\n"
    "v_exp_f32_e32 v242, v206\n"
    "v_mfma_f32_16x16x32_bf16 a[56:59], a[244:247], v[236:239], a[56:59]\n"
    "buffer_load_dwordx4 v190, s[44:47], s84 offen offset:2048 sc1 lds\n"
    "v_mfma_f32_16x16x32_bf16 a[60:63], a[248:251], v[232:235], a[60:63]\n"
    "v_exp_f32_e32 v243, v207\n"
    "v_mfma_f32_16x16x32_bf16 a[60:63], a[252:255], v[236:239], a[60:63]\n"
    "ds_read_b128 a[188:191], v184 offset:15360\n"
    "v_cvt_pk_bf16_f32 v231, v242, v243\n"
    "v_mfma_f32_16x16x32_bf16 v[100:103], v[92:95], v[232:235], v[100:103]\n"
    "s_waitcnt lgkmcnt(8)\n"
    "v_mfma_f32_16x16x32_bf16 v[100:103], v[92:95], v[236:239], v[100:103]\n"
    "s_bitcmp1_b32 s82, s80\n"
    "buffer_load_dwordx4 v190, s[44:47], s84 offen offset:3072 sc1 lds\n"
    "s_cbranch_scc0 L_at_top\n"
    "s_sub_i32 s90, s81, s80\n"
    "s_cmp_lt_i32 s90, 2\n"
    "s_cbranch_scc1 L_at_e3\n"
    "s_cmp_eq_u32 s90, 2\n"
    "s_cbranch_scc1 L_at_e2\n"
    "v_min_f32 v112, v64, v112\n"
    "v_min_f32 v113, v65, v113\n"
    "v_min_f32 v114, v66, v114\n"
    "v_min_f32 v115, v67, v115\n"
    "v_min_f32 v116, v68, v116\n"
    "v_min_f32 v117, v69, v117\n"
    "v_min_f32 v118, v70, v118\n"
    "v_min_f32 v119, v71, v119\n"
    "v_min_f32 v120, v72, v120\n"
    "v_min_f32 v121, v73, v121\n"
    "v_min_f32 v122, v74, v122\n"
    "v_min_f32 v123, v75, v123\n"
    "v_min_f32 v124, v76, v124\n"
    "v_min_f32 v125, v77, v125\n"
    "v_min_f32 v126, v78, v126\n"
    "v_min_f32 v127, v79, v127\n"
    "s_branch L_at_top\n"
    "L_at_e2:\n"
    "v_mov_b64 v[0:1], 0\n"
    "v_mov_b64 v[2:3], 0\n"
    "v_mov_b64 v[4:5], 0\n"
    "v_mov_b64 v[6:7], 0\n"
    "v_mov_b64 v[8:9], 0\n"
    "v_mov_b64 v[10:11], 0\n"
    "v_mov_b64 v[12:13], 0\n"
    "v_mov_b64 v[14:15], 0\n"
    "v_mov_b64 v[112:113], v[188:189]\n"
    "v_mov_b64 v[114:115], v[188:189]\n"
    "v_mov_b64 v[116:117], v[188:189]\n"
    "v_mov_b64 v[118:119], v[188:189]\n"
    "v_mov_b64 v[120:121], v[188:189]\n"
    "v_mov_b64 v[122:123], v[188:189]\n"
    "v_mov_b64 v[124:125], v[188:189]\n"
    "v_mov_b64 v[126:127], v[188:189]\n"
    "v_min_f32 v128, v64, v128\n"
    "v_min_f32 v129, v65, v129\n"
    "v_min_f32 v130, v66, v130\n"
    "v_min_f32 v131, v67, v131\n"
    "v_min_f32 v132, v68, v132\n"
    "v_min_f32 v133, v69, v133\n"
    "v_min_f32 v134, v70, v134\n"
    "v_min_f32 v135, v71, v135\n"
    "v_min_f32 v136, v72, v136\n"
    "v_min_f32 v137, v73, v137\n"
    "v_min_f32 v138, v74, v138\n"
    "v_min_f32 v139, v75, v139\n"
    "v_min_f32 v140, v76, v140\n"
    "v_min_f32 v141, v77, v141\n"
    "v_min_f32 v142, v78, v142\n"
    "v_min_f32 v143, v79, v143\n"
    "v_add_u32 v184, s91, v184\n"
    "s_branch L_at_top\n"
    "L_at_e3:\n"
    "s_cmp_lg_u32 s90, 1\n"
    "s_cbranch_scc1 L_at_post\n"
    "ds_read_b128 v[0:3], v186 offset:0\n"
    "ds_read_b128 v[4:7], v186 offset:1024\n"
    "ds_read_b128 v[8:11], v186 offset:2048\n"
    "ds_read_b128 v[12:15], v186 offset:3072\n"
    "ds_read_b128 v[16:19], v186 offset:4096\n"
    "ds_read_b128 v[20:23], v186 offset:5120\n"
    "ds_read_b128 v[24:27], v186 offset:6144\n"
    "v_mov_b64 v[28:29], v[80:81]\n"
    "v_mov_b64 v[30:31], v[82:83]\n"
    "v_add_u32 v185, s91, v185\n"
    "s_bitcmp1_b32 s13, 1\n"
    "s_cselect_b32 s90, 0x40000000, s62\n"
    "s_add_u32 s89, s90, 0x80000\n"
    "v_cmp_nle_f32 vcc, v96, v86\n"
    "v_rcp_f32 v250, v96\n"
    "s_or_b64 s[94:95], s[94:95], vcc\n"
    "v_accvgpr_read_b32 v112, a0\n"
    "v_accvgpr_read_b32 v113, a1\n"
    "v_accvgpr_read_b32 v114, a2\n"
    "v_accvgpr_read_b32 v115, a3\n"
    "v_accvgpr_read_b32 v116, a4\n"
    "v_accvgpr_read_b32 v117, a5\n"
    "v_accvgpr_read_b32 v118, a6\n"
    "v_accvgpr_read_b32 v119, a7\n"
    "v_pk_mul_f32 v[112:113], v[112:113], v[250:251] op_sel_hi:[1,0]\n"
    "v_pk_mul_f32 v[114:115], v[114:115], v[250:251] op_sel_hi:[1,0]\n"
    "v_pk_mul_f32 v[116:117], v[116:117], v[250:251] op_sel_hi:[1,0]\n"
    "v_pk_mul_f32 v[118:119], v[118:119], v[250:251] op_sel_hi:[1,0]\n"
    "v_cvt_pk_bf16_f32 v112, v112, v113\n"
    "v_cvt_pk_bf16_f32 v113, v114, v115\n"
    "v_cvt_pk_bf16_f32 v114, v116, v117\n"
    "v_cvt_pk_bf16_f32 v115, v118, v119\n"
    "buffer_store_dwordx4 v[112:115], v190, s[76:79], s90 offen offset:0 sc1\n"
    "v_accvgpr_read_b32 v120, a8\n"
    "v_accvgpr_read_b32 v121, a9\n"
    "v_accvgpr_read_b32 v122, a10\n"
    "v_accvgpr_read_b32 v123, a11\n"
    "v_accvgpr_read_b32 v124, a12\n"
    "v_accvgpr_read_b32 v125, a13\n"
    "v_accvgpr_read_b32 v126, a14\n"
    "v_accvgpr_read_b32 v127, a15\n"
    "v_pk_mul_f32 v[120:121], v[120:121], v[250:251] op_sel_hi:[1,0]\n"
    "v_pk_mul_f32 v[122:123], v[122:123], v[250:251] op_sel_hi:[1,0]\n"
    "v_pk_mul_f32 v[124:125], v[124:125], v[250:251] op_sel_hi:[1,0]\n"
    "v_pk_mul_f32 v[126:127], v[126:127], v[250:251] op_sel_hi:[1,0]\n"
    "v_cvt_pk_bf16_f32 v120, v120, v121\n"
    "v_cvt_pk_bf16_f32 v121, v122, v123\n"
    "v_cvt_pk_bf16_f32 v122, v124, v125\n"
    "v_cvt_pk_bf16_f32 v123, v126, v127\n"
    "buffer_store_dwordx4 v[120:123], v190, s[76:79], s90 offen offset:1024 sc1\n"
    "v_accvgpr_read_b32 v112, a16\n"
    "v_accvgpr_read_b32 v113, a17\n"
    "v_accvgpr_read_b32 v114, a18\n"
    "v_accvgpr_read_b32 v115, a19\n"
    "v_accvgpr_read_b32 v116, a20\n"
    "v_accvgpr_read_b32 v117, a21\n"
    "v_accvgpr_read_b32 v118, a22\n"
    "v_accvgpr_read_b32 v119, a23\n"
    "v_pk_mul_f32 v[112:113], v[112:113], v[250:251] op_sel_hi:[1,0]\n"
    "v_pk_mul_f32 v[114:115], v[114:115], v[250:251] op_sel_hi:[1,0]\n"
    "v_pk_mul_f32 v[116:117], v[116:117], v[250:251] op_sel_hi:[1,0]\n"
    "v_pk_mul_f32 v[118:119], v[118:119], v[250:251] op_sel_hi:[1,0]\n"
    "v_cvt_pk_bf16_f32 v112, v112, v113\n"
    "v_cvt_pk_bf16_f32 v113, v114, v115\n"
    "v_cvt_pk_bf16_f32 v114, v116, v117\n"
    "v_cvt_pk_bf16_f32 v115, v118, v119\n"
    "buffer_store_dwordx4 v[112:115], v190, s[76:79], s90 offen offset:2048 sc1\n"
    "v_accvgpr_read_b32 v120, a24\n"
    "v_accvgpr_read_b32 v121, a25\n"
    "v_accvgpr_read_b32 v122, a26\n"
    "v_accvgpr_read_b32 v123, a27\n"
    "v_accvgpr_read_b32 v124, a28\n"
    "v_accvgpr_read_b32 v125, a29\n"
    "v_accvgpr_read_b32 v126, a30\n"
    "v_accvgpr_read_b32 v127, a31\n"
    "v_pk_mul_f32 v[120:121], v[120:121], v[250:251] op_sel_hi:[1,0]\n"
    "v_pk_mul_f32 v[122:123], v[122:123], v[250:251] op_sel_hi:[1,0]\n"
    "v_pk_mul_f32 v[124:125], v[124:125], v[250:251] op_sel_hi:[1,0]\n"
    "v_pk_mul_f32 v[126:127], v[126:127], v[250:251] op_sel_hi:[1,0]\n"
    "v_cvt_pk_bf16_f32 v120, v120, v121\n"
    "v_cvt_pk_bf16_f32 v121, v122, v123\n"
    "v_cvt_pk_bf16_f32 v122, v124, v125\n"
    "v_cvt_pk_bf16_f32 v123, v126, v127\n"
    "buffer_store_dwordx4 v[120:123], v190, s[76:79], s90 offen offset:3072 sc1\n"
    "v_cmp_nle_f32 vcc, v100, v86\n"
    "v_rcp_f32 v250, v100\n"
    "s_or_b64 s[94:95], s[94:95], vcc\n"
    "v_accvgpr_read_b32 v128, a32\n"
    "v_accvgpr_read_b32 v129, a33\n"
    "v_accvgpr_read_b32 v130, a34\n"
    "v_accvgpr_read_b32 v131, a35\n"
    "v_accvgpr_read_b32 v132, a36\n"
    "v_accvgpr_read_b32 v133, a37\n"
    "v_accvgpr_read_b32 v134, a38\n"
    "v_accvgpr_read_b32 v135, a39\n"
    "v_pk_mul_f32 v[128:129], v[128:129], v[250:251] op_sel_hi:[1,0]\n"
    "v_pk_mul_f32 v[130:131], v[130:131], v[250:251] op_sel_hi:[1,0]\n"
    "v_pk_mul_f32 v[132:133], v[132:133], v[250:251] op_sel_hi:[1,0]\n"
    "v_pk_mul_f32 v[134:135], v[134:135], v[250:251] op_sel_hi:[1,0]\n"
    "v_cvt_pk_bf16_f32 v128, v128, v129\n"
    "v_cvt_pk_bf16_f32 v129, v130, v131\n"
    "v_cvt_pk_bf16_f32 v130, v132, v133\n"
    "v_cvt_pk_bf16_f32 v131, v134, v135\n"
    "buffer_store_dwordx4 v[128:131], v190, s[76:79], s89 offen offset:0 sc1\n"
    "v_accvgpr_read_b32 v136, a40\n"
    "v_accvgpr_read_b32 v137, a41\n"
    "v_accvgpr_read_b32 v138, a42\n"
    "v_accvgpr_read_b32 v139, a43\n"
    "v_accvgpr_read_b32 v140, a44\n"
    "v_accvgpr_read_b32 v141, a45\n"
    "v_accvgpr_read_b32 v142, a46\n"
    "v_accvgpr_read_b32 v143, a47\n"
    "v_pk_mul_f32 v[136:137], v[136:137], v[250:251] op_sel_hi:[1,0]\n"
    "v_pk_mul_f32 v[138:139], v[138:139], v[250:251] op_sel_hi:[1,0]\n"
    "v_pk_mul_f32 v[140:141], v[140:141], v[250:251] op_sel_hi:[1,0]\n"
    "v_pk_mul_f32 v[142:143], v[142:143], v[250:251] op_sel_hi:[1,0]\n"
    "v_cvt_pk_bf16_f32 v136, v136, v137\n"
    "v_cvt_pk_bf16_f32 v137, v138, v139\n"
    "v_cvt_pk_bf16_f32 v138, v140, v141\n"
    "v_cvt_pk_bf16_f32 v139, v142, v143\n"
    "buffer_store_dwordx4 v[136:139], v190, s[76:79], s89 offen offset:1024 sc1\n"
    "v_accvgpr_read_b32 v128, a48\n"
    "v_accvgpr_read_b32 v129, a49\n"
    "v_accvgpr_read_b32 v130, a50\n"
    "v_accvgpr_read_b32 v131, a51\n"
    "v_accvgpr_read_b32 v132, a52\n"
    "v_accvgpr_read_b32 v133, a53\n"
    "v_accvgpr_read_b32 v134, a54\n"
    "v_accvgpr_read_b32 v135, a55\n"
    "v_pk_mul_f32 v[128:129], v[128:129], v[250:251] op_sel_hi:[1,0]\n"
    "v_pk_mul_f32 v[130:131], v[130:131], v[250:251] op_sel_hi:[1,0]\n"
    "v_pk_mul_f32 v[132:133], v[132:133], v[250:251] op_sel_hi:[1,0]\n"
    "v_pk_mul_f32 v[134:135], v[134:135], v[250:251] op_sel_hi:[1,0]\n"
    "v_cvt_pk_bf16_f32 v128, v128, v129\n"
    "v_cvt_pk_bf16_f32 v129, v130, v131\n"
    ".p2align 12\n"
    "L_pad8:\n"
    "s_cbranch_execz L_pad12\n"
    "v_cvt_pk_bf16_f32 v130, v132, v133\n"
    "v_cvt_pk_bf16_f32 v131, v134, v135\n"
    "buffer_store_dwordx4 v[128:131], v190, s[76:79], s89 offen offset:2048 sc1\n"
    "v_accvgpr_read_b32 v136, a56\n"
    "v_accvgpr_read_b32 v137, a57\n"
    "v_accvgpr_read_b32 v138, a58\n"
    "v_accvgpr_read_b32 v139, a59\n"
    "v_accvgpr_read_b32 v140, a60\n"
    "v_accvgpr_read_b32 v141, a61\n"
    "v_accvgpr_read_b32 v142, a62\n"
    "v_accvgpr_read_b32 v143, a63\n"
    "v_pk_mul_f32 v[136:137], v[136:137], v[250:251] op_sel_hi:[1,0]\n"
    "v_pk_mul_f32 v[138:139], v[138:139], v[250:251] op_sel_hi:[1,0]\n"
    "v_pk_mul_f32 v[140:141], v[140:141], v[250:251] op_sel_hi:[1,0]\n"
    "v_pk_mul_f32 v[142:143], v[142:143], v[250:251] op_sel_hi:[1,0]\n"
    "v_cvt_pk_bf16_f32 v136, v136, v137\n"
    "v_cvt_pk_bf16_f32 v137, v138, v139\n"
    "v_cvt_pk_bf16_f32 v138, v140, v141\n"
    "v_cvt_pk_bf16_f32 v139, v142, v143\n"
    "buffer_store_dwordx4 v[136:139], v190, s[76:79], s89 offen offset:3072 sc1\n"
    "s_bitcmp1_b32 s13, 0\n"
    "s_cselect_b64 s[94:95], 0, s[94:95]\n"
    "s_cmp_eq_u64 s[94:95], 0\n"
    "s_cselect_b32 s15, s15, 0x40000000\n"
    "v_mov_b64 v[96:97], 0\n"
    "v_mov_b64 v[98:99], 0\n"
    "v_mov_b64 v[100:101], 0\n"
    "v_mov_b64 v[102:103], 0\n"
    "s_mov_b32 s92, 1\n"
    "s_nop 0\n"
    "v_mfma_f32_32x32x16_bf16 a[0:15], v[96:99], v[96:99], 0\n"
    "v_mov_b32 v112, v248\n"
    "v_mov_b32 v113, v248\n"
    "v_mov_b64 v[114:115], v[112:113]\n"
    "v_mov_b64 v[116:117], v[112:113]\n"
    "v_mov_b64 v[118:119], v[112:113]\n"
    "v_mfma_f32_32x32x16_bf16 a[16:31], v[96:99], v[96:99], 0\n"
    "v_mov_b64 v[120:121], v[112:113]\n"
    "v_mov_b64 v[122:123], v[112:113]\n"
    "v_mov_b64 v[124:125], v[112:113]\n"
    "v_mov_b64 v[126:127], v[112:113]\n"
    "v_mov_b32 v128, v249\n"
    "v_mfma_f32_32x32x16_bf16 a[32:47], v[96:99], v[96:99], 0\n"
    "v_mov_b32 v129, v249\n"
    "v_mov_b64 v[130:131], v[128:129]\n"
    "v_mov_b64 v[132:133], v[128:129]\n"
    "v_mov_b64 v[134:135], v[128:129]\n"
    "v_mov_b64 v[136:137], v[128:129]\n"
    "v_mfma_f32_32x32x16_bf16 a[48:63], v[96:99], v[96:99], 0\n"
    "v_mov_b64 v[138:139], v[128:129]\n"
    "v_mov_b64 v[140:141], v[128:129]\n"
    "v_mov_b64 v[142:143], v[128:129]\n"
    "s_waitcnt vmcnt(8) lgkmcnt(0)\n"
    "s_cmp_lg_u32 s80, 15\n"
    "s_cbranch_scc1 L_at_top\n"
    "L_at_post:\n"
    "v_xor_b32 v88, 1, v88\n"
    "v_xor_b32 v89, 1, v89\n"
    "v_xor_b32 v90, 1, v90\n"
    "v_xor_b32 v91, 1, v91\n"
    "v_or3_b32 v88, v88, v89, v90\n"
    "v_or_b32 v88, v88, v91\n"
    "v_cmp_ne_u32 vcc, 0, v88\n"
    "s_cbranch_vccz L_wt_ok\n"
    "s_mov_b32 s14, 0x40000\n"
    "L_wt_spin:\n"
    "buffer_load_dwordx4 v[88:91], v252, s[76:79], s58 offen sc1\n"
    "s_sub_u32 s14, s14, 1\n"
    "s_waitcnt vmcnt(0)\n"
    "s_cbranch_scc1 L_wt_ok\n"
    "v_xor_b32 v88, 1, v88\n"
    "v_xor_b32 v89, 1, v89\n"
    "v_xor_b32 v90, 1, v90\n"
    "v_xor_b32 v91, 1, v91\n"
    "v_or3_b32 v88, v88, v89, v90\n"
    "v_or_b32 v88, v88, v91\n"
    "v_cmp_ne_u32 vcc, 0, v88\n"
    "s_cbranch_vccz L_wt_ok\n"
    "s_sleep 3\n"
    "s_branch L_wt_spin\n"
    "L_wt_ok:\n"
    "s_lshr_b32 s69, s71, 1\n"
    "s_mul_i32 s69, s69, 0x20000\n"
    "s_add_u32 s69, s69, s91\n"
    "v_min_f32 v144, v64, v144\n"
    "v_min_f32 v145, v65, v145\n"
    "v_min_f32 v146, v66, v146\n"
    "v_min_f32 v147, v67, v147\n"
    "v_add_u32 v186, s69, v190\n"
    "v_add_u32 v185, 0x18000, v190\n"
    "v_mfma_f32_16x16x32_bf16 v[192:195], a[128:131], v[32:35], v[144:147]\n"
    "v_exp_f32_e32 v240, v208\n"
    "v_mfma_f32_16x16x32_bf16 v[192:195], a[132:135], v[36:39], v[192:195]\n"
    "s_sub_u32 s85, s49, s48\n"
    "v_min_f32 v148, v68, v148\n"
    "v_min_f32 v149, v69, v149\n"
    "v_min_f32 v150, v70, v150\n"
    "v_min_f32 v151, v71, v151\n"
    "v_mfma_f32_16x16x32_bf16 v[192:195], a[136:139], v[40:43], v[192:195]\n"
    "s_sub_u32 s86, s50, s48\n"
    "v_exp_f32_e32 v241, v209\n"
    "v_mfma_f32_16x16x32_bf16 v[192:195], a[140:143], v[44:47], v[192:195]\n"
    "s_sub_u32 s87, s51, s48\n"
    "ds_write_b128 v186, a[0:3] offset:0\n"
    "v_cvt_pk_bf16_f32 v232, v240, v241\n"
    "v_min_f32 v152, v72, v152\n"
    "v_mfma_f32_16x16x32_bf16 v[196:199], a[144:147], v[32:35], v[148:151]\n"
    "s_add_u32 s23, s57, 0x14000\n"
    "v_exp_f32_e32 v242, v210\n"
    "v_mfma_f32_16x16x32_bf16 v[196:199], a[148:151], v[36:39], v[196:199]\n"
    "s_add_u32 s24, s63, 0x80000\n"
    "v_min_f32 v153, v73, v153\n"
    "v_min_f32 v154, v74, v154\n"
    "v_min_f32 v155, v75, v155\n"
    "v_mfma_f32_16x16x32_bf16 v[196:199], a[152:155], v[40:43], v[196:199]\n"
    "v_exp_f32_e32 v243, v211\n"
    "v_mfma_f32_16x16x32_bf16 v[196:199], a[156:159], v[44:47], v[196:199]\n"
    "s_waitcnt lgkmcnt(1)\n"
    "ds_write_b128 v186, a[4:7] offset:1024\n"
    "v_cvt_pk_bf16_f32 v233, v242, v243\n"
    "v_min_f32 v156, v76, v156\n"
    "v_mfma_f32_16x16x32_bf16 v[200:203], a[160:163], v[32:35], v[152:155]\n"
    "v_exp_f32_e32 v240, v212\n"
    "v_mfma_f32_16x16x32_bf16 v[200:203], a[164:167], v[36:39], v[200:203]\n"
    "v_min_f32 v157, v77, v157\n"
    "v_min_f32 v158, v78, v158\n"
    "v_min_f32 v159, v79, v159\n"
    "v_mfma_f32_16x16x32_bf16 v[200:203], a[168:171], v[40:43], v[200:203]\n"
    "v_exp_f32_e32 v241, v213\n"
    "v_mfma_f32_16x16x32_bf16 v[200:203], a[172:175], v[44:47], v[200:203]\n"
    "ds_write_b128 v186, a[8:11] offset:2048\n"
    "v_cvt_pk_bf16_f32 v234, v240, v241\n"
    "v_mfma_f32_16x16x32_bf16 v[204:207], a[176:179], v[32:35], v[156:159]\n"
    "v_exp_f32_e32 v242, v214\n"
    "v_mfma_f32_16x16x32_bf16 v[204:207], a[180:183], v[36:39], v[204:207]\n"
    "ds_write_b128 v186, a[12:15] offset:3072\n"
    "v_mfma_f32_16x16x32_bf16 v[204:207], a[184:187], v[40:43], v[204:207]\n"
    "v_exp_f32_e32 v243, v215\n"
    "v_mfma_f32_16x16x32_bf16 v[204:207], a[188:191], v[44:47], v[204:207]\n"
    "v_cvt_pk_bf16_f32 v235, v242, v243\n"
    "v_mfma_f32_16x16x32_bf16 a[64:67], a[192:195], v[224:227], a[64:67]\n"
    "v_exp_f32_e32 v240, v216\n"
    "v_mfma_f32_16x16x32_bf16 a[64:67], a[196:199], v[228:231], a[64:67]\n"
    "ds_write_b128 v186, a[16:19] offset:4096\n"
    "v_mfma_f32_16x16x32_bf16 a[68:71], a[200:203], v[224:227], a[68:71]\n"
    "v_exp_f32_e32 v241, v217\n"
    "v_mfma_f32_16x16x32_bf16 a[68:71], a[204:207], v[228:231], a[68:71]\n"
    "ds_write_b128 v186, a[20:23] offset:5120\n"
    "v_cvt_pk_bf16_f32 v236, v240, v241\n"
    "v_mfma_f32_16x16x32_bf16 a[72:75], a[208:211], v[224:227], a[72:75]\n"
    "v_exp_f32_e32 v242, v218\n"
    "v_mfma_f32_16x16x32_bf16 a[72:75], a[212:215], v[228:231], a[72:75]\n"
    "ds_write_b128 v186, a[24:27] offset:6144\n"
    "v_mfma_f32_16x16x32_bf16 a[76:79], a[216:219], v[224:227], a[76:79]\n"
    "v_exp_f32_e32 v243, v219\n"
    "v_mfma_f32_16x16x32_bf16 a[76:79], a[220:223], v[228:231], a[76:79]\n"
    "v_cvt_pk_bf16_f32 v237, v242, v243\n"
    "v_mfma_f32_16x16x32_bf16 a[80:83], a[224:227], v[224:227], a[80:83]\n"
    "v_exp_f32_e32 v240, v220\n"
    "v_mfma_f32_16x16x32_bf16 a[80:83], a[228:231], v[228:231], a[80:83]\n"
    "ds_write_b128 v186, a[28:31] offset:7168\n"
    "v_mfma_f32_16x16x32_bf16 a[84:87], a[232:235], v[224:227], a[84:87]\n"
    "v_exp_f32_e32 v241, v221\n"
    "v_mfma_f32_16x16x32_bf16 a[84:87], a[236:239], v[228:231], a[84:87]\n"
    "ds_write_b128 v186, a[32:35] offset:8192\n"
    "v_cvt_pk_bf16_f32 v238, v240, v241\n"
    "v_mfma_f32_16x16x32_bf16 a[88:91], a[240:243], v[224:227], a[88:91]\n"
    "v_exp_f32_e32 v242, v222\n"
    "v_mfma_f32_16x16x32_bf16 a[88:91], a[244:247], v[228:231], a[88:91]\n"
    "ds_write_b128 v186, a[36:39] offset:9216\n"
    "v_mfma_f32_16x16x32_bf16 a[92:95], a[248:251], v[224:227], a[92:95]\n"
    "v_exp_f32_e32 v243, v223\n"
    "v_mfma_f32_16x16x32_bf16 a[92:95], a[252:255], v[228:231], a[92:95]\n"
    "v_cvt_pk_bf16_f32 v239, v242, v243\n"
    "v_mov_b32 v97, v100\n"
    "v_mfma_f32_16x16x32_bf16 v[104:107], v[92:95], v[224:227], v[104:107]\n"
    "ds_write_b128 v186, a[40:43] offset:10240\n"
    "v_mfma_f32_16x16x32_bf16 v[104:107], v[92:95], v[228:231], v[104:107]\n"
    "v_min_f32 v112, v64, v160\n"
    "v_min_f32 v113, v65, v161\n"
    "v_mfma_f32_16x16x32_bf16 v[208:211], a[128:131], v[48:51], v[160:163]\n"
    "v_exp_f32_e32 v240, v192\n"
    "v_mfma_f32_16x16x32_bf16 v[208:211], a[132:135], v[52:55], v[208:211]\n"
    "ds_write_b128 v186, a[44:47] offset:11264\n"
    "v_min_f32 v114, v66, v162\n"
    "v_min_f32 v115, v67, v163\n"
    "v_mfma_f32_16x16x32_bf16 v[208:211], a[136:139], v[56:59], v[208:211]\n"
    "v_exp_f32_e32 v241, v193\n"
    "v_mfma_f32_16x16x32_bf16 v[208:211], a[140:143], v[60:63], v[208:211]\n"
    "ds_read_b128 a[128:131], v184 offset:16384\n"
    "ds_read_b128 a[132:135], v184 offset:17408\n"
    "v_cvt_pk_bf16_f32 v224, v240, v241\n"
    "v_mfma_f32_16x16x32_bf16 v[212:215], a[144:147], v[48:51], v[164:167]\n"
    "ds_write_b128 v186, a[48:51] offset:12288\n"
    "v_exp_f32_e32 v242, v194\n"
    "v_mfma_f32_16x16x32_bf16 v[212:215], a[148:151], v[52:55], v[212:215]\n"
    "ds_read_b128 a[136:139], v184 offset:18432\n"
    "ds_read_b128 a[140:143], v184 offset:19456\n"
    "v_mfma_f32_16x16x32_bf16 v[212:215], a[152:155], v[56:59], v[212:215]\n"
    "v_exp_f32_e32 v243, v195\n"
    "v_mfma_f32_16x16x32_bf16 v[212:215], a[156:159], v[60:63], v[212:215]\n"
    "ds_write_b128 v186, a[52:55] offset:13312\n"
    "ds_read_b128 a[144:147], v184 offset:20480\n"
    "v_cvt_pk_bf16_f32 v225, v242, v243\n"
    "v_mfma_f32_16x16x32_bf16 v[216:219], a[160:163], v[48:51], v[168:171]\n"
    "v_exp_f32_e32 v240, v196\n"
    "v_mfma_f32_16x16x32_bf16 v[216:219], a[164:167], v[52:55], v[216:219]\n"
    "ds_read_b128 a[148:151], v184 offset:21504\n"
    "ds_read_b128 a[152:155], v184 offset:22528\n"
    "v_mfma_f32_16x16x32_bf16 v[216:219], a[168:171], v[56:59], v[216:219]\n"
    "ds_write_b128 v186, a[56:59] offset:14336\n"
    "v_exp_f32_e32 v241, v197\n"
    "v_mfma_f32_16x16x32_bf16 v[216:219], a[172:175], v[60:63], v[216:219]\n"
    "ds_read_b128 a[156:159], v184 offset:23552\n"
    "v_cvt_pk_bf16_f32 v226, v240, v241\n"
    "v_mfma_f32_16x16x32_bf16 v[220:223], a[176:179], v[48:51], v[172:175]\n"
    "ds_write_b128 v186, a[60:63] offset:15360\n"
    "v_exp_f32_e32 v242, v198\n"
    "v_mfma_f32_16x16x32_bf16 v[220:223], a[180:183], v[52:55], v[220:223]\n"
    "ds_read_b128 a[160:163], v184 offset:24576\n"
    "ds_read_b128 a[164:167], v184 offset:25600\n"
    "v_mfma_f32_16x16x32_bf16 v[220:223], a[184:187], v[56:59], v[220:223]\n"
    "v_exp_f32_e32 v243, v199\n"
    "v_mfma_f32_16x16x32_bf16 v[220:223], a[188:191], v[60:63], v[220:223]\n"
    "s_waitcnt vmcnt(0) lgkmcnt(2)\n"
    "ds_read_b128 a[168:171], v184 offset:26624\n"
    "v_cvt_pk_bf16_f32 v227, v242, v243\n"
    "v_mfma_f32_16x16x32_bf16 a[96:99], a[192:195], v[232:235], a[96:99]\n"
    "v_exp_f32_e32 v240, v200\n"
    "s_barrier\n"
    "v_mfma_f32_16x16x32_bf16 a[96:99], a[196:199], v[236:239], a[96:99]\n"
    "ds_read_b128 a[192:195], v185 offset:0\n"
    "ds_read_b128 a[196:199], v185 offset:1024\n"
    "v_mfma_f32_16x16x32_bf16 a[100:103], a[200:203], v[232:235], a[100:103]\n"
    "v_exp_f32_e32 v241, v201\n"
    "v_mfma_f32_16x16x32_bf16 a[100:103], a[204:207], v[236:239], a[100:103]\n"
    "ds_read_b128 a[172:175], v184 offset:27648\n"
    "v_cvt_pk_bf16_f32 v228, v240, v241\n"
    "v_mfma_f32_16x16x32_bf16 a[104:107], a[208:211], v[232:235], a[104:107]\n"
    "buffer_store_dword v182, v180, s[76:79], s15 offen sc1\n"
    "v_exp_f32_e32 v242, v202\n"
    "v_mfma_f32_16x16x32_bf16 a[104:107], a[212:215], v[236:239], a[104:107]\n"
    "ds_read_b128 a[200:203], v185 offset:2048\n"
    "ds_read_b128 a[204:207], v185 offset:3072\n"
    "v_mfma_f32_16x16x32_bf16 a[108:111], a[216:219], v[232:235], a[108:111]\n"
    "v_exp_f32_e32 v243, v203\n"
    "v_mfma_f32_16x16x32_bf16 a[108:111], a[220:223], v[236:239], a[108:111]\n"
    "s_add_u32 m0, s57, 32768\n"
    "ds_read_b128 a[176:179], v184 offset:28672\n"
    "v_cvt_pk_bf16_f32 v229, v242, v243\n"
    "v_mfma_f32_16x16x32_bf16 a[112:115], a[224:227], v[232:235], a[112:115]\n"
    "s_add_u32 s68, s48, 0x0\n"
    "buffer_load_dwordx4 v190, s[76:79], s68 offen offset:0 sc1 lds\n"
    "v_exp_f32_e32 v240, v204\n"
    "v_mfma_f32_16x16x32_bf16 a[112:115], a[228:231], v[236:239], a[112:115]\n"
    "ds_read_b128 a[208:211], v185 offset:4096\n"
    "ds_read_b128 a[212:215], v185 offset:5120\n"
    "v_mfma_f32_16x16x32_bf16 a[116:119], a[232:235], v[232:235], a[116:119]\n"
    "v_exp_f32_e32 v241, v205\n"
    "v_mfma_f32_16x16x32_bf16 a[116:119], a[236:239], v[236:239], a[116:119]\n"
    "ds_read_b128 a[180:183], v184 offset:29696\n"
    "v_cvt_pk_bf16_f32 v230, v240, v241\n"
    "v_mfma_f32_16x16x32_bf16 a[120:123], a[240:243], v[232:235], a[120:123]\n"
    "buffer_load_dwordx4 v190, s[76:79], s68 offen offset:1024 sc1 lds\n"
    "v_exp_f32_e32 v242, v206\n"
    "v_mfma_f32_16x16x32_bf16 a[120:123], a[244:247], v[236:239], a[120:123]\n"
    "ds_read_b128 a[216:219], v185 offset:6144\n"
    "ds_read_b128 a[220:223], v185 offset:7168\n"
    "v_mfma_f32_16x16x32_bf16 a[124:127], a[248:251], v[232:235], a[124:127]\n"
    "v_exp_f32_e32 v243, v207\n"
    "v_mfma_f32_16x16x32_bf16 a[124:127], a[252:255], v[236:239], a[124:127]\n"
    "ds_read_b128 a[184:187], v184 offset:30720\n"
    "v_cvt_pk_bf16_f32 v231, v242, v243\n"
    "v_mfma_f32_16x16x32_bf16 v[108:111], v[92:95], v[232:235], v[108:111]\n"
    "ds_read_b128 a[224:227], v185 offset:8192\n"
    "ds_read_b128 a[228:231], v185 offset:9216\n"
    "v_mfma_f32_16x16x32_bf16 v[108:111], v[92:95], v[236:239], v[108:111]\n"
    "ds_read_b128 a[188:191], v184 offset:31744\n"
    "ds_read_b128 a[232:235], v185 offset:10240\n"
    "v_mfma_f32_16x16x32_bf16 v[192:195], a[128:131], v[48:51], v[112:115]\n"
    "v_exp_f32_e32 v240, v208\n"
    "v_mfma_f32_16x16x32_bf16 v[192:195], a[132:135], v[52:55], v[192:195]\n"
    "v_min_f32 v116, v68, v164\n"
    "v_min_f32 v117, v69, v165\n"
    "v_min_f32 v118, v70, v166\n"
    "v_min_f32 v119, v71, v167\n"
    "v_mfma_f32_16x16x32_bf16 v[192:195], a[136:139], v[56:59], v[192:195]\n"
    "buffer_load_dwordx4 v190, s[76:79], s68 offen offset:2048 sc1 lds\n"
    "v_exp_f32_e32 v241, v209\n"
    "v_mfma_f32_16x16x32_bf16 v[192:195], a[140:143], v[60:63], v[192:195]\n"
    "v_cvt_pk_bf16_f32 v232, v240, v241\n"
    "v_min_f32 v120, v72, v168\n"
    "v_mfma_f32_16x16x32_bf16 v[196:199], a[144:147], v[48:51], v[116:119]\n"
    "v_exp_f32_e32 v242, v210\n"
    "v_mfma_f32_16x16x32_bf16 v[196:199], a[148:151], v[52:55], v[196:199]\n"
    "v_min_f32 v121, v73, v169\n"
    "v_min_f32 v122, v74, v170\n"
    "v_min_f32 v123, v75, v171\n"
    "v_mfma_f32_16x16x32_bf16 v[196:199], a[152:155], v[56:59], v[196:199]\n"
    "buffer_load_dwordx4 v190, s[76:79], s68 offen offset:3072 sc1 lds\n"
    "v_exp_f32_e32 v243, v211\n"
    "v_mfma_f32_16x16x32_bf16 v[196:199], a[156:159], v[60:63], v[196:199]\n"
    "s_waitcnt lgkmcnt(1)\n"
    "v_cvt_pk_bf16_f32 v233, v242, v243\n"
    "v_min_f32 v124, v76, v172\n"
    "v_mfma_f32_16x16x32_bf16 v[200:203], a[160:163], v[48:51], v[120:123]\n"
    "v_exp_f32_e32 v240, v212\n"
    "s_barrier\n"
    "v_mfma_f32_16x16x32_bf16 v[200:203], a[164:167], v[52:55], v[200:203]\n"
    "s_add_u32 m0, s57, 49152\n"
    "v_min_f32 v125, v77, v173\n"
    "v_min_f32 v126, v78, v174\n"
    "v_min_f32 v127, v79, v175\n"
    "v_mfma_f32_16x16x32_bf16 v[200:203], a[168:171], v[56:59], v[200:203]\n"
    "s_add_u32 s68, s48, 0x80000\n"
    "buffer_load_dwordx4 v190, s[76:79], s68 offen offset:0 sc1 lds\n"
    "v_exp_f32_e32 v241, v213\n"
    "v_mfma_f32_16x16x32_bf16 v[200:203], a[172:175], v[60:63], v[200:203]\n"
    "ds_read_b128 a[236:239], v185 offset:11264\n"
    "v_cvt_pk_bf16_f32 v234, v240, v241\n"
    "v_mfma_f32_16x16x32_bf16 v[204:207], a[176:179], v[48:51], v[124:127]\n"
    "v_exp_f32_e32 v242, v214\n"
    "v_mfma_f32_16x16x32_bf16 v[204:207], a[180:183], v[52:55], v[204:207]\n"
    "ds_read_b128 a[240:243], v185 offset:12288\n"
    "ds_read_b128 a[244:247], v185 offset:13312\n"
    "v_mfma_f32_16x16x32_bf16 v[204:207], a[184:187], v[56:59], v[204:207]\n"
    "buffer_load_dwordx4 v190, s[76:79], s68 offen offset:1024 sc1 lds\n"
    "v_exp_f32_e32 v243, v215\n"
    "v_mfma_f32_16x16x32_bf16 v[204:207], a[188:191], v[60:63], v[204:207]\n"
    "s_waitcnt lgkmcnt(3)\n"
    "ds_read_b128 a[248:251], v185 offset:14336\n"
    "v_cvt_pk_bf16_f32 v235, v242, v243\n"
    "v_mfma_f32_16x16x32_bf16 a[64:67], a[192:195], v[224:227], a[64:67]\n"
    "v_exp_f32_e32 v240, v216\n"
    "v_mfma_f32_16x16x32_bf16 a[64:67], a[196:199], v[228:231], a[64:67]\n"
    "ds_read_b128 a[252:255], v185 offset:15360\n"
    "v_mfma_f32_16x16x32_bf16 a[68:71], a[200:203], v[224:227], a[68:71]\n"
    "buffer_load_dwordx4 v190, s[76:79], s68 offen offset:2048 sc1 lds\n"
    "v_exp_f32_e32 v241, v217\n"
    "v_mfma_f32_16x16x32_bf16 a[68:71], a[204:207], v[228:231], a[68:71]\n"
    "v_cvt_pk_bf16_f32 v236, v240, v241\n"
    "v_add_u32 v176, 0x10000, v190\n"
    "v_mfma_f32_16x16x32_bf16 a[72:75], a[208:211], v[224:227], a[72:75]\n"
    "v_exp_f32_e32 v242, v218\n"
    "v_mfma_f32_16x16x32_bf16 a[72:75], a[212:215], v[228:231], a[72:75]\n"
    "buffer_load_dwordx4 v190, s[76:79], s68 offen offset:3072 sc1 lds\n"
    "v_mfma_f32_16x16x32_bf16 a[76:79], a[216:219], v[224:227], a[76:79]\n"
    "s_add_u32 m0, s57, 65536\n"
    "v_exp_f32_e32 v243, v219\n"
    "v_mfma_f32_16x16x32_bf16 a[76:79], a[220:223], v[228:231], a[76:79]\n"
    "s_add_u32 s68, s49, 0x0\n"
    "buffer_load_dwordx4 v190, s[76:79], s68 offen offset:0 sc1 lds\n"
    "v_cvt_pk_bf16_f32 v237, v242, v243\n"
    "v_mfma_f32_16x16x32_bf16 a[80:83], a[224:227], v[224:227], a[80:83]\n"
    "v_exp_f32_e32 v240, v220\n"
    "v_mfma_f32_16x16x32_bf16 a[80:83], a[228:231], v[228:231], a[80:83]\n"
    "buffer_load_dwordx4 v190, s[76:79], s68 offen offset:1024 sc1 lds\n"
    "v_mfma_f32_16x16x32_bf16 a[84:87], a[232:235], v[224:227], a[84:87]\n"
    "s_waitcnt lgkmcnt(0)\n"
    "v_exp_f32_e32 v241, v221\n"
    "v_mfma_f32_16x16x32_bf16 a[84:87], a[236:239], v[228:231], a[84:87]\n"
    "buffer_load_dwordx4 v190, s[76:79], s68 offen offset:2048 sc1 lds\n"
    "v_cvt_pk_bf16_f32 v238, v240, v241\n"
    "v_mfma_f32_16x16x32_bf16 a[88:91], a[240:243], v[224:227], a[88:91]\n"
    "v_exp_f32_e32 v242, v222\n"
    "v_mfma_f32_16x16x32_bf16 a[88:91], a[244:247], v[228:231], a[88:91]\n"
    "buffer_load_dwordx4 v190, s[76:79], s68 offen offset:3072 sc1 lds\n"
    "v_mfma_f32_16x16x32_bf16 a[92:95], a[248:251], v[224:227], a[92:95]\n"
    "v_exp_f32_e32 v243, v223\n"
    "v_mfma_f32_16x16x32_bf16 a[92:95], a[252:255], v[228:231], a[92:95]\n"
    "v_cvt_pk_bf16_f32 v239, v242, v243\n"
    "v_bfe_u32 v181, v190, 2, 4\n"
    "v_mfma_f32_16x16x32_bf16 v[104:107], v[92:95], v[224:227], v[104:107]\n"
    "s_add_u32 s69, s62, 0\n"
    "buffer_load_dwordx4 a[0:3], v190, s[76:79], s69 offen offset:0 sc1\n"
    "v_lshlrev_b32 v181, 5, v181\n"
    "v_mfma_f32_16x16x32_bf16 v[104:107], v[92:95], v[228:231], v[104:107]\n"
    "buffer_load_dwordx4 a[4:7], v190, s[76:79], s69 offen offset:1024 sc1\n"
    "v_xor_b32 v186, 0x4000, v186\n"
    "v_mfma_f32_16x16x32_bf16 a[96:99], a[192:195], v[232:235], a[96:99]\n"
    "ds_read_b128 a[192:195], v185 offset:16384\n"
    "v_exp_f32_e32 v240, v192\n"
    "v_mfma_f32_16x16x32_bf16 a[96:99], a[196:199], v[236:239], a[96:99]\n"
    "ds_read_b128 a[196:199], v185 offset:17408\n"
    "v_exp_f32_e32 v241, v193\n"
    "v_mfma_f32_16x16x32_bf16 a[100:103], a[200:203], v[232:235], a[100:103]\n"
    "ds_read_b128 a[200:203], v185 offset:18432\n"
    "v_exp_f32_e32 v242, v194\n"
    "v_cvt_pk_bf16_f32 v224, v240, v241\n"
    "v_mfma_f32_16x16x32_bf16 a[100:103], a[204:207], v[236:239], a[100:103]\n"
    "ds_read_b128 a[204:207], v185 offset:19456\n"
    "v_exp_f32_e32 v243, v195\n"
    "v_mfma_f32_16x16x32_bf16 a[104:107], a[208:211], v[232:235], a[104:107]\n"
    "ds_read_b128 a[208:211], v185 offset:20480\n"
    "v_exp_f32_e32 v240, v196\n"
    "v_cvt_pk_bf16_f32 v225, v242, v243\n"
    "v_mfma_f32_16x16x32_bf16 a[104:107], a[212:215], v[236:239], a[104:107]\n"
    "ds_read_b128 a[212:215], v185 offset:21504\n"
    "v_exp_f32_e32 v241, v197\n"
    "v_mfma_f32_16x16x32_bf16 a[108:111], a[216:219], v[232:235], a[108:111]\n"
    "ds_read_b128 a[216:219], v185 offset:22528\n"
    "v_exp_f32_e32 v242, v198\n"
    "v_cvt_pk_bf16_f32 v226, v240, v241\n"
    "v_mfma_f32_16x16x32_bf16 a[108:111], a[220:223], v[236:239], a[108:111]\n"
    "ds_read_b128 a[220:223], v185 offset:23552\n"
    "v_exp_f32_e32 v243, v199\n"
    "v_mfma_f32_16x16x32_bf16 a[112:115], a[224:227], v[232:235], a[112:115]\n"
    "ds_read_b128 a[224:227], v185 offset:24576\n"
    "v_exp_f32_e32 v240, v200\n"
    "v_cvt_pk_bf16_f32 v227, v242, v243\n"
    "v_mfma_f32_16x16x32_bf16 a[112:115], a[228:231], v[236:239], a[112:115]\n"
    "ds_read_b128 a[228:231], v185 offset:25600\n"
    "v_exp_f32_e32 v241, v201\n"
    "v_mfma_f32_16x16x32_bf16 a[116:119], a[232:235], v[232:235], a[116:119]\n"
    "ds_read_b128 a[232:235], v185 offset:26624\n"
    "v_exp_f32_e32 v242, v202\n"
    "v_cvt_pk_bf16_f32 v228, v240, v241\n"
    "v_mfma_f32_16x16x32_bf16 a[116:119], a[236:239], v[236:239], a[116:119]\n"
    "ds_read_b128 a[236:239], v185 offset:27648\n"
    "v_exp_f32_e32 v243, v203\n"
    "v_mfma_f32_16x16x32_bf16 a[120:123], a[240:243], v[232:235], a[120:123]\n"
    "ds_read_b128 a[240:243], v185 offset:28672\n"
    "v_exp_f32_e32 v240, v204\n"
    "v_cvt_pk_bf16_f32 v229, v242, v243\n"
    "v_mfma_f32_16x16x32_bf16 a[120:123], a[244:247], v[236:239], a[120:123]\n"
    "ds_read_b128 a[244:247], v185 offset:29696\n"
    "v_exp_f32_e32 v241, v205\n"
    "v_mfma_f32_16x16x32_bf16 a[124:127], a[248:251], v[232:235], a[124:127]\n"
    "ds_read_b128 a[248:251], v185 offset:30720\n"
    "v_exp_f32_e32 v242, v206\n"
    "v_cvt_pk_bf16_f32 v230, v240, v241\n"
    "v_mfma_f32_16x16x32_bf16 a[124:127], a[252:255], v[236:239], a[124:127]\n"
    "ds_read_b128 a[252:255], v185 offset:31744\n"
    "v_exp_f32_e32 v243, v207\n"
    "v_mfma_f32_16x16x32_bf16 v[108:111], v[92:95], v[232:235], v[108:111]\n"
    "v_cvt_pk_bf16_f32 v231, v242, v243\n"
    "v_mfma_f32_16x16x32_bf16 v[108:111], v[92:95], v[236:239], v[108:111]\n"
    "buffer_load_dwordx4 a[8:11], v190, s[76:79], s69 offen offset:2048 sc1\n"
    "v_add_u32 v184, s23, v190\n"
    "v_mfma_f32_16x16x32_bf16 v[108:111], v[92:95], v[224:227], v[108:111]\n"
    "buffer_load_dwordx4 a[12:15], v190, s[76:79], s69 offen offset:3072 sc1\n"
    "v_cmp_nle_f32 vcc, v96, v86\n"
    "v_mfma_f32_16x16x32_bf16 v[108:111], v[92:95], v[228:231], v[108:111]\n"
    "s_waitcnt lgkmcnt(8)\n"
    "s_or_b64 s[94:95], s[94:95], vcc\n"
    "ds_read_b128 v[0:3], v186 offset:0\n"
    "ds_read_b128 v[4:7], v186 offset:1024\n"
    "v_mfma_f32_16x16x32_bf16 a[96:99], a[192:195], v[224:227], a[96:99]\n"
    "s_lshl_b32 s4, s52, 5\n"
    "s_add_u32 s4, s4, 0x5800000\n"
    "buffer_load_dword v187, v181, s[76:79], s4 offen sc1\n"
    "v_cmp_nle_f32 vcc, v100, v86\n"
    "v_mfma_f32_16x16x32_bf16 a[96:99], a[196:199], v[228:231], a[96:99]\n"
    "s_or_b64 s[94:95], s[94:95], vcc\n"
    "ds_read_b128 v[8:11], v186 offset:2048\n"
    "ds_read_b128 v[12:15], v186 offset:3072\n"
    "v_mfma_f32_16x16x32_bf16 a[100:103], a[200:203], v[224:227], a[100:103]\n"
    "s_add_u32 s69, s69, 0x80000\n"
    "buffer_load_dwordx4 a[16:19], v190, s[76:79], s69 offen offset:0 sc1\n"
    "v_cmp_nle_f32 vcc, v104, v86\n"
    "v_mfma_f32_16x16x32_bf16 a[100:103], a[204:207], v[228:231], a[100:103]\n"
    "s_or_b64 s[94:95], s[94:95], vcc\n"
    "ds_read_b128 v[16:19], v186 offset:4096\n"
    "ds_read_b128 v[20:23], v186 offset:5120\n"
    "v_mfma_f32_16x16x32_bf16 a[104:107], a[208:211], v[224:227], a[104:107]\n"
    "ds_read_b128 v[24:27], v186 offset:6144\n"
    "v_cmp_nle_f32 vcc, v108, v86\n"
    "v_mfma_f32_16x16x32_bf16 a[104:107], a[212:215], v[228:231], a[104:107]\n"
    "s_or_b64 s[94:95], s[94:95], vcc\n"
    "ds_read_b128 v[28:31], v186 offset:7168\n"
    "ds_read_b128 v[32:35], v186 offset:8192\n"
    "v_mfma_f32_16x16x32_bf16 a[108:111], a[216:219], v[224:227], a[108:111]\n"
    "buffer_load_dwordx4 a[20:23], v190, s[76:79], s69 offen offset:1024 sc1\n"
    "v_lshlrev_b32 v185, 5, v181\n"
    "v_mfma_f32_16x16x32_bf16 a[108:111], a[220:223], v[228:231], a[108:111]\n"
    "s_waitcnt lgkmcnt(9)\n"
    "ds_read_b128 v[36:39], v186 offset:9216\n"
    "ds_read_b128 v[40:43], v186 offset:10240\n"
    "v_mfma_f32_16x16x32_bf16 a[112:115], a[224:227], v[224:227], a[112:115]\n"
    "s_cmp_lg_u64 s[94:95], 0\n"
    "ds_read_b128 v[44:47], v186 offset:11264\n"
    "v_add_u32 v185, 0x14008, v185\n"
    "v_mfma_f32_16x16x32_bf16 a[112:115], a[228:231], v[228:231], a[112:115]\n"
    "s_cselect_b32 s22, 1, 0\n"
    "ds_read_b128 v[48:51], v186 offset:12288\n"
    "ds_read_b128 v[52:55], v186 offset:13312\n"
    "v_mfma_f32_16x16x32_bf16 a[116:119], a[232:235], v[224:227], a[116:119]\n"
    "s_andn2_b32 s22, s22, s13\n"
    "buffer_load_dwordx4 a[24:27], v190, s[76:79], s69 offen offset:2048 sc1\n"
    "v_mfma_f32_16x16x32_bf16 a[116:119], a[236:239], v[228:231], a[116:119]\n"
    "ds_read_b128 v[56:59], v186 offset:14336\n"
    "ds_read_b128 v[60:63], v186 offset:15360\n"
    "v_mov_b32 v98, s22\n"
    "v_mfma_f32_16x16x32_bf16 a[120:123], a[240:243], v[224:227], a[120:123]\n"
    "ds_write_b128 v184, v[96:99]\n"
    "v_mfma_f32_16x16x32_bf16 a[120:123], a[244:247], v[228:231], a[120:123]\n"
    "v_xor_b32 v184, 0x1000, v184\n"
    "v_mfma_f32_16x16x32_bf16 a[124:127], a[248:251], v[224:227], a[124:127]\n"
    "buffer_load_dwordx4 a[28:31], v190, s[76:79], s69 offen offset:3072 sc1\n"
    "v_mfma_f32_16x16x32_bf16 a[124:127], a[252:255], v[228:231], a[124:127]\n"
    "s_waitcnt vmcnt(13) lgkmcnt(0)\n"
    "s_barrier\n"
    "ds_read_b32 v100, v185\n"
    "ds_read_b64 v[96:97], v184\n"
    "s_add_u32 m0, s57, 0\n"
    "s_add_u32 s68, s49, 0x80000\n"
    "buffer_load_dwordx4 v190, s[76:79], s68 offen offset:0 sc1 lds\n"
    "buffer_load_dwordx4 v190, s[76:79], s68 offen offset:1024 sc1 lds\n"
    "s_waitcnt lgkmcnt(0)\n"
    "v_cmp_ne_u32 vcc, 0, v100\n"
    "v_add_f32 v104, v104, v96\n"
    "v_add_f32 v108, v108, v97\n"
    "s_cbranch_vccnz L_redo\n"
    "v_rcp_f32 v250, v104\n"
    "v_accvgpr_read_b32 v192, a64\n"
    "v_accvgpr_read_b32 v193, a65\n"
    "v_accvgpr_read_b32 v194, a66\n"
    "v_accvgpr_read_b32 v195, a67\n"
    "v_accvgpr_read_b32 v196, a68\n"
    "v_accvgpr_read_b32 v197, a69\n"
    "v_accvgpr_read_b32 v198, a70\n"
    "buffer_load_dwordx4 v190, s[76:79], s68 offen offset:2048 sc1 lds\n"
    "v_accvgpr_read_b32 v199, a71\n"
    "v_pk_add_f32 v[192:193], v[192:193], v[0:1]\n"
    "v_pk_add_f32 v[194:195], v[194:195], v[2:3]\n"
    "v_pk_add_f32 v[196:197], v[196:197], v[4:5]\n"
    "v_pk_add_f32 v[198:199], v[198:199], v[6:7]\n"
    "v_pk_mul_f32 v[192:193], v[192:193], v[250:251] op_sel_hi:[1,0]\n"
    "v_pk_mul_f32 v[194:195], v[194:195], v[250:251] op_sel_hi:[1,0]\n"
    "v_pk_mul_f32 v[196:197], v[196:197], v[250:251] op_sel_hi:[1,0]\n"
    "buffer_load_dwordx4 v190, s[76:79], s68 offen offset:3072 sc1 lds\n"
    "s_waitcnt vmcnt(8)\n"
    "v_cmp_ne_u32 vcc, 1, v187\n"
    "s_cbranch_vccz L_ca_ok\n"
    "s_mov_b32 s14, 0x40000\n"
    "L_ca_spin:\n"
    "buffer_load_dword v187, v181, s[76:79], s4 offen sc1\n"
    "s_sub_u32 s14, s14, 1\n"
    "s_waitcnt vmcnt(0)\n"
    "s_cbranch_scc1 L_ca_ok\n"
    "v_cmp_ne_u32 vcc, 1, v187\n"
    "s_cbranch_vccz L_ca_ok\n"
    "s_sleep 3\n"
    "s_branch L_ca_spin\n"
    "L_ca_ok:\n"
    "v_pk_mul_f32 v[198:199], v[198:199], v[250:251] op_sel_hi:[1,0]\n"
    "v_cvt_pk_bf16_f32 v64, v192, v193\n"
    "v_cvt_pk_bf16_f32 v65, v194, v195\n"
    "v_cvt_pk_bf16_f32 v66, v196, v197\n"
    "v_cvt_pk_bf16_f32 v67, v198, v199\n"
    "v_accvgpr_read_b32 v200, a72\n"
    "v_accvgpr_read_b32 v201, a73\n"
    "v_accvgpr_read_b32 v202, a74\n"
    "s_add_u32 s69, s62, s85\n"
    "buffer_load_dwordx4 a[32:35], v190, s[76:79], s69 offen offset:0 sc1\n"
    "v_accvgpr_read_b32 v203, a75\n"
    "v_accvgpr_read_b32 v204, a76\n"
    "v_accvgpr_read_b32 v205, a77\n"
    "v_accvgpr_read_b32 v206, a78\n"
    "v_accvgpr_read_b32 v207, a79\n"
    "v_pk_add_f32 v[200:201], v[200:201], v[8:9]\n"
    "v_pk_add_f32 v[202:203], v[202:203], v[10:11]\n"
    "v_pk_add_f32 v[204:205], v[204:205], v[12:13]\n"
    "buffer_load_dwordx4 a[36:39], v190, s[76:79], s69 offen offset:1024 sc1\n"
    "v_pk_add_f32 v[206:207], v[206:207], v[14:15]\n"
    "v_pk_mul_f32 v[200:201], v[200:201], v[250:251] op_sel_hi:[1,0]\n"
    "v_pk_mul_f32 v[202:203], v[202:203], v[250:251] op_sel_hi:[1,0]\n"
    "v_pk_mul_f32 v[204:205], v[204:205], v[250:251] op_sel_hi:[1,0]\n"
    "v_pk_mul_f32 v[206:207], v[206:207], v[250:251] op_sel_hi:[1,0]\n"
    "v_cvt_pk_bf16_f32 v68, v200, v201\n"
    "v_cvt_pk_bf16_f32 v69, v202, v203\n"
    "v_cvt_pk_bf16_f32 v70, v204, v205\n"
    "buffer_load_dwordx4 a[40:43], v190, s[76:79], s69 offen offset:2048 sc1\n"
    "v_cvt_pk_bf16_f32 v71, v206, v207\n"
    "v_accvgpr_read_b32 v192, a80\n"
    "v_accvgpr_read_b32 v193, a81\n"
    "v_accvgpr_read_b32 v194, a82\n"
    "v_accvgpr_read_b32 v195, a83\n"
    "v_accvgpr_read_b32 v196, a84\n"
    "v_accvgpr_read_b32 v197, a85\n"
    "v_accvgpr_read_b32 v198, a86\n"
    "buffer_load_dwordx4 a[44:47], v190, s[76:79], s69 offen offset:3072 sc1\n"
    "v_accvgpr_read_b32 v199, a87\n"
    "v_pk_add_f32 v[192:193], v[192:193], v[16:17]\n"
    "v_pk_add_f32 v[194:195], v[194:195], v[18:19]\n"
    "v_pk_add_f32 v[196:197], v[196:197], v[20:21]\n"
    "v_pk_add_f32 v[198:199], v[198:199], v[22:23]\n"
    "v_pk_mul_f32 v[192:193], v[192:193], v[250:251] op_sel_hi:[1,0]\n"
    "v_pk_mul_f32 v[194:195], v[194:195], v[250:251] op_sel_hi:[1,0]\n"
    "v_pk_mul_f32 v[196:197], v[196:197], v[250:251] op_sel_hi:[1,0]\n"
    "s_add_u32 s69, s69, 0x80000\n"
    "buffer_load_dwordx4 a[48:51], v190, s[76:79], s69 offen offset:0 sc1\n"
    "v_pk_mul_f32 v[198:199], v[198:199], v[250:251] op_sel_hi:[1,0]\n"
    "v_cvt_pk_bf16_f32 v72, v192, v193\n"
    "v_cvt_pk_bf16_f32 v73, v194, v195\n"
    "v_cvt_pk_bf16_f32 v74, v196, v197\n"
    "v_cvt_pk_bf16_f32 v75, v198, v199\n"
    "v_accvgpr_read_b32 v200, a88\n"
    "v_accvgpr_read_b32 v201, a89\n"
    "v_accvgpr_read_b32 v202, a90\n"
    "buffer_load_dwordx4 a[52:55], v190, s[76:79], s69 offen offset:1024 sc1\n"
    "v_accvgpr_read_b32 v203, a91\n"
    "v_accvgpr_read_b32 v204, a92\n"
    "v_accvgpr_read_b32 v205, a93\n"
    "v_accvgpr_read_b32 v206, a94\n"
    "v_accvgpr_read_b32 v207, a95\n"
    "v_pk_add_f32 v[200:201], v[200:201], v[24:25]\n"
    "v_pk_add_f32 v[202:203], v[202:203], v[26:27]\n"
    "v_pk_add_f32 v[204:205], v[204:205], v[28:29]\n"
    "buffer_load_dwordx4 a[56:59], v190, s[76:79], s69 offen offset:2048 sc1\n"
    "v_pk_add_f32 v[206:207], v[206:207], v[30:31]\n"
    "v_pk_mul_f32 v[200:201], v[200:201], v[250:251] op_sel_hi:[1,0]\n"
    "v_pk_mul_f32 v[202:203], v[202:203], v[250:251] op_sel_hi:[1,0]\n"
    "v_pk_mul_f32 v[204:205], v[204:205], v[250:251] op_sel_hi:[1,0]\n"
    "v_pk_mul_f32 v[206:207], v[206:207], v[250:251] op_sel_hi:[1,0]\n"
    "v_cvt_pk_bf16_f32 v76, v200, v201\n"
    "v_cvt_pk_bf16_f32 v77, v202, v203\n"
    ".p2align 12\n"
    "L_pad9:\n"
    "s_cbranch_execz L_pad13\n"
    "v_cvt_pk_bf16_f32 v78, v204, v205\n"
    "buffer_load_dwordx4 a[60:63], v190, s[76:79], s69 offen offset:3072 sc1\n"
    "v_cvt_pk_bf16_f32 v79, v206, v207\n"
    "v_rcp_f32 v250, v108\n"
    "v_accvgpr_read_b32 v208, a96\n"
    "v_accvgpr_read_b32 v209, a97\n"
    "v_accvgpr_read_b32 v210, a98\n"
    "v_accvgpr_read_b32 v211, a99\n"
    "v_accvgpr_read_b32 v212, a100\n"
    "v_accvgpr_read_b32 v213, a101\n"
    "v_accvgpr_read_b32 v214, a102\n"
    "v_accvgpr_read_b32 v215, a103\n"
    "v_pk_add_f32 v[208:209], v[208:209], v[32:33]\n"
    "v_pk_add_f32 v[210:211], v[210:211], v[34:35]\n"
    "v_pk_add_f32 v[212:213], v[212:213], v[36:37]\n"
    "v_pk_add_f32 v[214:215], v[214:215], v[38:39]\n"
    "v_pk_mul_f32 v[208:209], v[208:209], v[250:251] op_sel_hi:[1,0]\n"
    "v_pk_mul_f32 v[210:211], v[210:211], v[250:251] op_sel_hi:[1,0]\n"
    "v_pk_mul_f32 v[212:213], v[212:213], v[250:251] op_sel_hi:[1,0]\n"
    "v_pk_mul_f32 v[214:215], v[214:215], v[250:251] op_sel_hi:[1,0]\n"
    "v_cvt_pk_bf16_f32 v80, v208, v209\n"
    "v_cvt_pk_bf16_f32 v81, v210, v211\n"
    "v_cvt_pk_bf16_f32 v82, v212, v213\n"
    "ds_read_b128 v[112:115], v190 offset:32768\n"
    "ds_read_b128 v[116:119], v190 offset:36864\n"
    "ds_read_b128 v[120:123], v190 offset:40960\n"
    "ds_read_b128 v[124:127], v190 offset:45056\n"
    "buffer_store_dwordx4 v[64:67], v190, s[76:79], s63 offen offset:0 sc1\n"
    "v_cvt_pk_bf16_f32 v83, v214, v215\n"
    "v_accvgpr_read_b32 v216, a104\n"
    "v_accvgpr_read_b32 v217, a105\n"
    "v_accvgpr_read_b32 v218, a106\n"
    "v_accvgpr_read_b32 v219, a107\n"
    "v_accvgpr_read_b32 v220, a108\n"
    "v_accvgpr_read_b32 v221, a109\n"
    "v_accvgpr_read_b32 v222, a110\n"
    "v_accvgpr_read_b32 v223, a111\n"
    "v_pk_add_f32 v[216:217], v[216:217], v[40:41]\n"
    "v_pk_add_f32 v[218:219], v[218:219], v[42:43]\n"
    "v_pk_add_f32 v[220:221], v[220:221], v[44:45]\n"
    "v_pk_add_f32 v[222:223], v[222:223], v[46:47]\n"
    "v_pk_mul_f32 v[216:217], v[216:217], v[250:251] op_sel_hi:[1,0]\n"
    "v_pk_mul_f32 v[218:219], v[218:219], v[250:251] op_sel_hi:[1,0]\n"
    "v_pk_mul_f32 v[220:221], v[220:221], v[250:251] op_sel_hi:[1,0]\n"
    "v_pk_mul_f32 v[222:223], v[222:223], v[250:251] op_sel_hi:[1,0]\n"
    "v_cvt_pk_bf16_f32 v84, v216, v217\n"
    "v_cvt_pk_bf16_f32 v85, v218, v219\n"
    "v_cvt_pk_bf16_f32 v86, v220, v221\n"
    "ds_read_b128 v[128:131], v190 offset:33792\n"
    "ds_read_b128 v[132:135], v190 offset:37888\n"
    "ds_read_b128 v[136:139], v190 offset:41984\n"
    "ds_read_b128 v[140:143], v190 offset:46080\n"
    "buffer_store_dwordx4 v[68:71], v190, s[76:79], s63 offen offset:1024 sc1\n"
    "v_cvt_pk_bf16_f32 v87, v222, v223\n"
    "v_accvgpr_read_b32 v208, a112\n"
    "v_accvgpr_read_b32 v209, a113\n"
    "v_accvgpr_read_b32 v210, a114\n"
    "v_accvgpr_read_b32 v211, a115\n"
    "v_accvgpr_read_b32 v212, a116\n"
    "v_accvgpr_read_b32 v213, a117\n"
    "v_accvgpr_read_b32 v214, a118\n"
    "v_accvgpr_read_b32 v215, a119\n"
    "v_pk_add_f32 v[208:209], v[208:209], v[48:49]\n"
    "v_pk_add_f32 v[210:211], v[210:211], v[50:51]\n"
    "v_pk_add_f32 v[212:213], v[212:213], v[52:53]\n"
    "v_pk_add_f32 v[214:215], v[214:215], v[54:55]\n"
    "v_pk_mul_f32 v[208:209], v[208:209], v[250:251] op_sel_hi:[1,0]\n"
    "v_pk_mul_f32 v[210:211], v[210:211], v[250:251] op_sel_hi:[1,0]\n"
    "v_pk_mul_f32 v[212:213], v[212:213], v[250:251] op_sel_hi:[1,0]\n"
    "v_pk_mul_f32 v[214:215], v[214:215], v[250:251] op_sel_hi:[1,0]\n"
    "v_cvt_pk_bf16_f32 v88, v208, v209\n"
    "v_cvt_pk_bf16_f32 v89, v210, v211\n"
    "v_cvt_pk_bf16_f32 v90, v212, v213\n"
    "ds_read_b128 v[144:147], v190 offset:34816\n"
    "ds_read_b128 v[148:151], v190 offset:38912\n"
    "ds_read_b128 v[152:155], v190 offset:43008\n"
    "ds_read_b128 v[156:159], v190 offset:47104\n"
    "buffer_store_dwordx4 v[72:75], v190, s[76:79], s63 offen offset:2048 sc1\n"
    "v_cvt_pk_bf16_f32 v91, v214, v215\n"
    "v_accvgpr_read_b32 v216, a120\n"
    "v_accvgpr_read_b32 v217, a121\n"
    "v_accvgpr_read_b32 v218, a122\n"
    "v_accvgpr_read_b32 v219, a123\n"
    "v_accvgpr_read_b32 v220, a124\n"
    "v_accvgpr_read_b32 v221, a125\n"
    "v_accvgpr_read_b32 v222, a126\n"
    "v_accvgpr_read_b32 v223, a127\n"
    "v_pk_add_f32 v[216:217], v[216:217], v[56:57]\n"
    "v_pk_add_f32 v[218:219], v[218:219], v[58:59]\n"
    "v_pk_add_f32 v[220:221], v[220:221], v[60:61]\n"
    "v_pk_add_f32 v[222:223], v[222:223], v[62:63]\n"
    "v_pk_mul_f32 v[216:217], v[216:217], v[250:251] op_sel_hi:[1,0]\n"
    "v_pk_mul_f32 v[218:219], v[218:219], v[250:251] op_sel_hi:[1,0]\n"
    "v_pk_mul_f32 v[220:221], v[220:221], v[250:251] op_sel_hi:[1,0]\n"
    "v_pk_mul_f32 v[222:223], v[222:223], v[250:251] op_sel_hi:[1,0]\n"
    "v_cvt_pk_bf16_f32 v92, v216, v217\n"
    "v_cvt_pk_bf16_f32 v93, v218, v219\n"
    "v_cvt_pk_bf16_f32 v94, v220, v221\n"
    "v_cvt_pk_bf16_f32 v95, v222, v223\n"
    "s_waitcnt lgkmcnt(8)\n"
    "v_mfma_f32_16x16x32_bf16 v[192:195], v[112:115], v[64:67], 0\n"
    "buffer_store_dwordx4 v[76:79], v190, s[76:79], s63 offen offset:3072 sc1\n"
    "v_mfma_f32_16x16x32_bf16 v[192:195], v[116:119], v[68:71], v[192:195]\n"
    "ds_read_b128 v[160:163], v190 offset:35840\n"
    "v_mfma_f32_16x16x32_bf16 v[192:195], v[120:123], v[72:75], v[192:195]\n"
    "v_mfma_f32_16x16x32_bf16 v[192:195], v[124:127], v[76:79], v[192:195]\n"
    "v_mfma_f32_16x16x32_bf16 v[224:227], v[112:115], v[80:83], 0\n"
    "v_mfma_f32_16x16x32_bf16 v[224:227], v[116:119], v[84:87], v[224:227]\n"
    "ds_read_b128 v[164:167], v190 offset:39936\n"
    "v_mfma_f32_16x16x32_bf16 v[224:227], v[120:123], v[88:91], v[224:227]\n"
    "v_mfma_f32_16x16x32_bf16 v[224:227], v[124:127], v[92:95], v[224:227]\n"
    "s_waitcnt vmcnt(16)\n"
    "v_mfma_f32_16x16x32_bf16 v[0:3], v[112:115], a[0:3], 0\n"
    "v_mfma_f32_16x16x32_bf16 v[0:3], v[116:119], a[4:7], v[0:3]\n"
    "s_lshl_b32 s29, s71, 3\n"
    "ds_read_b128 v[168:171], v190 offset:44032\n"
    "v_mfma_f32_16x16x32_bf16 v[0:3], v[120:123], a[8:11], v[0:3]\n"
    "s_add_u32 s29, s29, s72\n"
    "v_mfma_f32_16x16x32_bf16 v[0:3], v[124:127], a[12:15], v[0:3]\n"
    "s_and_b32 s70, s73, 1\n"
    "v_mfma_f32_16x16x32_bf16 v[32:35], v[112:115], a[16:19], 0\n"
    "s_lshl1_add_u32 s29, s29, s70\n"
    "buffer_store_dwordx4 v[80:83], v190, s[76:79], s24 offen offset:0 sc1\n"
    "v_mfma_f32_16x16x32_bf16 v[32:35], v[116:119], a[20:23], v[32:35]\n"
    "s_lshr_b32 s82, s73, 1\n"
    "ds_read_b128 v[172:175], v190 offset:48128\n"
    "v_mfma_f32_16x16x32_bf16 v[32:35], v[120:123], a[24:27], v[32:35]\n"
    "s_lshl2_add_u32 s82, s82, s71\n"
    "v_mfma_f32_16x16x32_bf16 v[32:35], v[124:127], a[28:31], v[32:35]\n"
    "s_waitcnt lgkmcnt(8)\n"
    "v_mfma_f32_16x16x32_bf16 v[196:199], v[128:131], v[64:67], 0\n"
    "s_lshl2_add_u32 s83, s72, s71\n"
    "v_mfma_f32_16x16x32_bf16 v[196:199], v[132:135], v[68:71], v[196:199]\n"
    "s_mul_i32 s83, s83, 57344\n"
    "ds_read_b128 v[112:115], v190 offset:49152\n"
    "v_mfma_f32_16x16x32_bf16 v[196:199], v[136:139], v[72:75], v[196:199]\n"
    "s_add_u32 s83, s83, 0x45c0000\n"
    "v_mfma_f32_16x16x32_bf16 v[196:199], v[140:143], v[76:79], v[196:199]\n"
    "s_lshl_b32 s22, s73, 1\n"
    "v_mfma_f32_16x16x32_bf16 v[228:231], v[128:131], v[80:83], 0\n"
    "s_lshr_b32 s23, s71, 1\n"
    "v_mfma_f32_16x16x32_bf16 v[228:231], v[132:135], v[84:87], v[228:231]\n"
    "s_add_u32 s22, s22, s23\n"
    "ds_read_b128 v[116:119], v190 offset:53248\n"
    "v_mfma_f32_16x16x32_bf16 v[228:231], v[136:139], v[88:91], v[228:231]\n"
    "s_lshl_b32 s23, s72, 1\n"
    "v_mfma_f32_16x16x32_bf16 v[228:231], v[140:143], v[92:95], v[228:231]\n"
    "s_and_b32 s70, s71, 1\n"
    "v_mfma_f32_16x16x32_bf16 v[4:7], v[128:131], a[0:3], 0\n"
    "s_add_u32 s23, s23, s70\n"
    "buffer_store_dwordx4 v[84:87], v190, s[76:79], s24 offen offset:1024 sc1\n"
    "v_mfma_f32_16x16x32_bf16 v[4:7], v[132:135], a[4:7], v[4:7]\n"
    "s_lshl_b32 s16, s22, 14\n"
    "ds_read_b128 v[120:123], v190 offset:57344\n"
    "v_mfma_f32_16x16x32_bf16 v[4:7], v[136:139], a[8:11], v[4:7]\n"
    "s_lshl_b32 s70, s23, 6\n"
    "v_mfma_f32_16x16x32_bf16 v[4:7], v[140:143], a[12:15], v[4:7]\n"
    "s_add_u32 s16, s16, s70\n"
    "v_mfma_f32_16x16x32_bf16 v[36:39], v[128:131], a[16:19], 0\n"
    "s_lshl_b32 s17, s23, 7\n"
    "v_mfma_f32_16x16x32_bf16 v[36:39], v[132:135], a[20:23], v[36:39]\n"
    "s_add_u32 s17, s17, 0x11000\n"
    "ds_read_b128 v[124:127], v190 offset:61440\n"
    "v_mfma_f32_16x16x32_bf16 v[36:39], v[136:139], a[24:27], v[36:39]\n"
    "s_mov_b32 s44, s34\n"
    "v_mfma_f32_16x16x32_bf16 v[36:39], v[140:143], a[28:31], v[36:39]\n"
    "s_waitcnt lgkmcnt(8)\n"
    "v_mfma_f32_16x16x32_bf16 v[200:203], v[144:147], v[64:67], 0\n"
    "s_and_b32 s45, s35, 0xffff\n"
    "v_mfma_f32_16x16x32_bf16 v[200:203], v[148:151], v[68:71], v[200:203]\n"
    "s_mov_b32 s46, 0x200000\n"
    "ds_read_b128 v[128:131], v190 offset:50176\n"
    "v_mfma_f32_16x16x32_bf16 v[200:203], v[152:155], v[72:75], v[200:203]\n"
    "s_and_b32 s70, s73, 3\n"
    "v_mfma_f32_16x16x32_bf16 v[200:203], v[156:159], v[76:79], v[200:203]\n"
    "s_lshl_b32 s70, s70, 5\n"
    "v_mfma_f32_16x16x32_bf16 v[232:235], v[144:147], v[80:83], 0\n"
    "s_lshr_b32 s28, s73, 2\n"
    "buffer_store_dwordx4 v[88:91], v190, s[76:79], s24 offen offset:2048 sc1\n"
    "v_mfma_f32_16x16x32_bf16 v[232:235], v[148:151], v[84:87], v[232:235]\n"
    "s_lshl_b32 s58, s28, 2\n"
    "ds_read_b128 v[132:135], v190 offset:54272\n"
    "v_mfma_f32_16x16x32_bf16 v[232:235], v[152:155], v[88:91], v[232:235]\n"
    "s_add_u32 s58, s58, s70\n"
    "v_mfma_f32_16x16x32_bf16 v[232:235], v[156:159], v[92:95], v[232:235]\n"
    "s_add_u32 s58, s58, 0x4000\n"
    "v_mfma_f32_16x16x32_bf16 v[8:11], v[144:147], a[0:3], 0\n"
    "s_add_u32 s70, s70, s28\n"
    "v_mfma_f32_16x16x32_bf16 v[8:11], v[148:151], a[4:7], v[8:11]\n"
    "s_lshl_b32 s70, s70, 10\n"
    "ds_read_b128 v[136:139], v190 offset:58368\n"
    "v_mfma_f32_16x16x32_bf16 v[8:11], v[152:155], a[8:11], v[8:11]\n"
    "s_add_u32 s70, s70, 0x4800000\n"
    "v_mfma_f32_16x16x32_bf16 v[8:11], v[156:159], a[12:15], v[8:11]\n"
    "s_lshl_b32 s88, s64, 5\n"
    "v_mfma_f32_16x16x32_bf16 v[40:43], v[144:147], a[16:19], 0\n"
    "s_add_u32 s88, s88, s70\n"
    "v_mfma_f32_16x16x32_bf16 v[40:43], v[148:151], a[20:23], v[40:43]\n"
    "s_lshl_b32 s89, s66, 5\n"
    "ds_read_b128 v[140:143], v190 offset:62464\n"
    "v_mfma_f32_16x16x32_bf16 v[40:43], v[152:155], a[24:27], v[40:43]\n"
    "s_add_u32 s89, s89, s70\n"
    "v_mfma_f32_16x16x32_bf16 v[40:43], v[156:159], a[28:31], v[40:43]\n"
    "s_waitcnt lgkmcnt(8)\n"
    "v_mfma_f32_16x16x32_bf16 v[204:207], v[160:163], v[64:67], 0\n"
    "s_lshr_b32 s20, s29, 1\n"
    "buffer_store_dwordx4 v[92:95], v190, s[76:79], s24 offen offset:3072 sc1\n"
    "v_mfma_f32_16x16x32_bf16 v[204:207], v[164:167], v[68:71], v[204:207]\n"
    "s_and_b32 s70, s29, 1\n"
    "ds_read_b128 v[144:147], v190 offset:51200\n"
    "v_mfma_f32_16x16x32_bf16 v[204:207], v[168:171], v[72:75], v[204:207]\n"
    "s_lshl2_add_u32 s20, s20, s70\n"
    "v_mfma_f32_16x16x32_bf16 v[204:207], v[172:175], v[76:79], v[204:207]\n"
    "s_lshl_b32 s20, s20, 11\n"
    "v_mfma_f32_16x16x32_bf16 v[236:239], v[160:163], v[80:83], 0\n"
    "s_add_u32 s20, s20, 0x4400000\n"
    "v_mfma_f32_16x16x32_bf16 v[236:239], v[164:167], v[84:87], v[236:239]\n"
    "s_lshl_b32 s80, s22, 7\n"
    "ds_read_b128 v[148:151], v190 offset:55296\n"
    "v_mfma_f32_16x16x32_bf16 v[236:239], v[168:171], v[88:91], v[236:239]\n"
    "s_lshr_b32 s70, s23, 2\n"
    "v_mfma_f32_16x16x32_bf16 v[236:239], v[172:175], v[92:95], v[236:239]\n"
    "s_lshl_b32 s70, s70, 5\n"
    "v_mfma_f32_16x16x32_bf16 v[12:15], v[160:163], a[0:3], 0\n"
    "s_add_u32 s80, s80, s70\n"
    "v_mfma_f32_16x16x32_bf16 v[12:15], v[164:167], a[4:7], v[12:15]\n"
    "s_add_u32 s80, s80, 0x4000\n"
    "ds_read_b128 v[152:155], v190 offset:59392\n"
    "v_mfma_f32_16x16x32_bf16 v[12:15], v[168:171], a[8:11], v[12:15]\n"
    "s_lshl_b32 s81, s22, 4\n"
    "v_mfma_f32_16x16x32_bf16 v[12:15], v[172:175], a[12:15], v[12:15]\n"
    "s_add_u32 s81, s81, s23\n"
    "s_add_u32 m0, s57, 98304\n"
    "v_mfma_f32_16x16x32_bf16 v[44:47], v[160:163], a[16:19], 0\n"
    "s_min_u32 s28, s29, 55\n"
    "s_add_u32 s68, s50, 0x0\n"
    "buffer_load_dwordx4 v190, s[76:79], s68 offen offset:0 sc1 lds\n"
    "v_mfma_f32_16x16x32_bf16 v[44:47], v[164:167], a[20:23], v[44:47]\n"
    "s_lshl_b32 s28, s28, 6\n"
    "ds_read_b128 v[156:159], v190 offset:63488\n"
    "v_mfma_f32_16x16x32_bf16 v[44:47], v[168:171], a[24:27], v[44:47]\n"
    "s_cmp_lt_u32 s29, 56\n"
    "s_cselect_b32 s20, s20, 0x40000000\n"
    "v_mfma_f32_16x16x32_bf16 v[44:47], v[172:175], a[28:31], v[44:47]\n"
    "s_waitcnt lgkmcnt(8)\n"
    "v_mfma_f32_16x16x32_bf16 v[208:211], v[112:115], v[64:67], 0\n"
    "buffer_load_dwordx4 v190, s[76:79], s68 offen offset:1024 sc1 lds\n"
    "v_mfma_f32_16x16x32_bf16 v[208:211], v[116:119], v[68:71], v[208:211]\n"
    "ds_read_b128 v[160:163], v190 offset:52224\n"
    "v_and_b32 v108, 0xf0, v190\n"
    "v_mfma_f32_16x16x32_bf16 v[208:211], v[120:123], v[72:75], v[208:211]\n"
    "v_mfma_f32_16x16x32_bf16 v[208:211], v[124:127], v[76:79], v[208:211]\n"
    "v_mfma_f32_16x16x32_bf16 v[240:243], v[112:115], v[80:83], 0\n"
    "buffer_load_dwordx4 v190, s[76:79], s68 offen offset:2048 sc1 lds\n"
    "v_mfma_f32_16x16x32_bf16 v[240:243], v[116:119], v[84:87], v[240:243]\n"
    "ds_read_b128 v[164:167], v190 offset:56320\n"
    "v_lshlrev_b32 v108, 6, v108\n"
    "v_mfma_f32_16x16x32_bf16 v[240:243], v[120:123], v[88:91], v[240:243]\n"
    "v_mfma_f32_16x16x32_bf16 v[240:243], v[124:127], v[92:95], v[240:243]\n"
    "v_mfma_f32_16x16x32_bf16 v[16:19], v[112:115], a[0:3], 0\n"
    "buffer_load_dwordx4 v190, s[76:79], s68 offen offset:3072 sc1 lds\n"
    "v_mfma_f32_16x16x32_bf16 v[16:19], v[116:119], a[4:7], v[16:19]\n"
    "ds_read_b128 v[168:171], v190 offset:60416\n"
    "v_bfe_u32 v109, v190, 8, 1\n"
    "v_mfma_f32_16x16x32_bf16 v[16:19], v[120:123], a[8:11], v[16:19]\n"
    "v_mfma_f32_16x16x32_bf16 v[16:19], v[124:127], a[12:15], v[16:19]\n"
    "s_add_u32 m0, s57, 114688\n"
    "v_mfma_f32_16x16x32_bf16 v[48:51], v[112:115], a[16:19], 0\n"
    "s_add_u32 s68, s50, 0x80000\n"
    "buffer_load_dwordx4 v190, s[76:79], s68 offen offset:0 sc1 lds\n"
    "v_mfma_f32_16x16x32_bf16 v[48:51], v[116:119], a[20:23], v[48:51]\n"
    "ds_read_b128 v[172:175], v190 offset:64512\n"
    "v_lshl_or_b32 v108, v109, 5, v108\n"
    "v_mfma_f32_16x16x32_bf16 v[48:51], v[120:123], a[24:27], v[48:51]\n"
    "v_mfma_f32_16x16x32_bf16 v[48:51], v[124:127], a[28:31], v[48:51]\n"
    "s_waitcnt lgkmcnt(8)\n"
    "s_waitcnt vmcnt(21)\n"
    "s_barrier\n"
    "v_mfma_f32_16x16x32_bf16 v[212:215], v[128:131], v[64:67], 0\n"
    "buffer_load_dwordx4 v190, s[76:79], s68 offen offset:1024 sc1 lds\n"
    "v_mfma_f32_16x16x32_bf16 v[212:215], v[132:135], v[68:71], v[212:215]\n"
    "ds_read_b128 v[112:115], v176 offset:0\n"
    "v_bfe_u32 v109, v190, 9, 1\n"
    "v_mfma_f32_16x16x32_bf16 v[212:215], v[136:139], v[72:75], v[212:215]\n"
    "v_mfma_f32_16x16x32_bf16 v[212:215], v[140:143], v[76:79], v[212:215]\n"
    "v_mfma_f32_16x16x32_bf16 v[244:247], v[128:131], v[80:83], 0\n"
    "buffer_load_dwordx4 v190, s[76:79], s68 offen offset:2048 sc1 lds\n"
    "v_mfma_f32_16x16x32_bf16 v[244:247], v[132:135], v[84:87], v[244:247]\n"
    "ds_read_b128 v[116:119], v176 offset:4096\n"
    "v_lshl_or_b32 v111, v109, 4, v108\n"
    "v_mfma_f32_16x16x32_bf16 v[244:247], v[136:139], v[88:91], v[244:247]\n"
    "v_mfma_f32_16x16x32_bf16 v[244:247], v[140:143], v[92:95], v[244:247]\n"
    "v_mfma_f32_16x16x32_bf16 v[20:23], v[128:131], a[0:3], 0\n"
    "buffer_load_dwordx4 v190, s[76:79], s68 offen offset:3072 sc1 lds\n"
    "v_mfma_f32_16x16x32_bf16 v[20:23], v[132:135], a[4:7], v[20:23]\n"
    "ds_read_b128 v[120:123], v176 offset:8192\n"
    "v_lshrrev_b32 v183, 8, v190\n"
    "v_mfma_f32_16x16x32_bf16 v[20:23], v[136:139], a[8:11], v[20:23]\n"
    "v_mfma_f32_16x16x32_bf16 v[20:23], v[140:143], a[12:15], v[20:23]\n"
    "s_add_u32 s56, s56, 0x1000\n"
    "v_mfma_f32_16x16x32_bf16 v[52:55], v[128:131], a[16:19], 0\n"
    "s_add_u32 s52, s52, 0x1000\n"
    "s_waitcnt vmcnt(8)\n"
    "s_lshl_b32 s5, s56, 5\n"
    "s_add_u32 s5, s5, 0x5800000\n"
    "buffer_store_dword v182, v180, s[76:79], s5 offen sc1\n"
    "v_mfma_f32_16x16x32_bf16 v[52:55], v[132:135], a[20:23], v[52:55]\n"
    "ds_read_b128 v[124:127], v176 offset:12288\n"
    "v_lshlrev_b32 v108, 4, v183\n"
    "v_mfma_f32_16x16x32_bf16 v[52:55], v[136:139], a[24:27], v[52:55]\n"
    "s_add_u32 s69, s62, s86\n"
    "buffer_load_dwordx4 a[128:131], v190, s[76:79], s69 offen offset:0 sc1\n"
    "v_mfma_f32_16x16x32_bf16 v[52:55], v[140:143], a[28:31], v[52:55]\n"
    "s_waitcnt lgkmcnt(8)\n"
    "v_mfma_f32_16x16x32_bf16 v[216:219], v[144:147], v[64:67], 0\n"
    "v_mfma_f32_16x16x32_bf16 v[216:219], v[148:151], v[68:71], v[216:219]\n"
    "ds_read_b128 v[128:131], v176 offset:1024\n"
    "v_lshl_add_u32 v109, v183, 3, s28\n"
    "v_mfma_f32_16x16x32_bf16 v[216:219], v[152:155], v[72:75], v[216:219]\n"
    "buffer_load_dwordx4 a[132:135], v190, s[76:79], s69 offen offset:1024 sc1\n"
    "v_mfma_f32_16x16x32_bf16 v[216:219], v[156:159], v[76:79], v[216:219]\n"
    "v_mfma_f32_16x16x32_bf16 v[248:251], v[144:147], v[80:83], 0\n"
    "v_mfma_f32_16x16x32_bf16 v[248:251], v[148:151], v[84:87], v[248:251]\n"
    "ds_read_b128 v[132:135], v176 offset:5120\n"
    "v_lshrrev_b32 v184, 5, v190\n"
    "v_mfma_f32_16x16x32_bf16 v[248:251], v[152:155], v[88:91], v[248:251]\n"
    "buffer_load_dwordx4 a[136:139], v190, s[76:79], s69 offen offset:2048 sc1\n"
    "v_mfma_f32_16x16x32_bf16 v[248:251], v[156:159], v[92:95], v[248:251]\n"
    "v_mfma_f32_16x16x32_bf16 v[24:27], v[144:147], a[0:3], 0\n"
    "v_mfma_f32_16x16x32_bf16 v[24:27], v[148:151], a[4:7], v[24:27]\n"
    "ds_read_b128 v[136:139], v176 offset:9216\n"
    "v_min_u32 v184, 1, v184\n"
    "v_mfma_f32_16x16x32_bf16 v[24:27], v[152:155], a[8:11], v[24:27]\n"
    "buffer_load_dwordx4 a[140:143], v190, s[76:79], s69 offen offset:3072 sc1\n"
    "v_mfma_f32_16x16x32_bf16 v[24:27], v[156:159], a[12:15], v[24:27]\n"
    "v_mfma_f32_16x16x32_bf16 v[56:59], v[144:147], a[16:19], 0\n"
    "v_mfma_f32_16x16x32_bf16 v[56:59], v[148:151], a[20:23], v[56:59]\n"
    "ds_read_b128 v[140:143], v176 offset:13312\n"
    "v_lshlrev_b32 v184, 20, v184\n"
    "v_mfma_f32_16x16x32_bf16 v[56:59], v[152:155], a[24:27], v[56:59]\n"
    "s_add_u32 s69, s69, 0x80000\n"
    "buffer_load_dwordx4 a[144:147], v190, s[76:79], s69 offen offset:0 sc1\n"
    "v_mfma_f32_16x16x32_bf16 v[56:59], v[156:159], a[28:31], v[56:59]\n"
    "s_waitcnt lgkmcnt(8)\n"
    "v_mfma_f32_16x16x32_bf16 v[220:223], v[160:163], v[64:67], 0\n"
    "v_mfma_f32_16x16x32_bf16 v[220:223], v[164:167], v[68:71], v[220:223]\n"
    "ds_read_b128 v[144:147], v176 offset:2048\n"
    "v_or_b32 v184, v184, v190\n"
    "v_mfma_f32_16x16x32_bf16 v[220:223], v[168:171], v[72:75], v[220:223]\n"
    "buffer_load_dwordx4 a[148:151], v190, s[76:79], s69 offen offset:1024 sc1\n"
    "v_mfma_f32_16x16x32_bf16 v[220:223], v[172:175], v[76:79], v[220:223]\n"
    "v_mfma_f32_16x16x32_bf16 v[252:255], v[160:163], v[80:83], 0\n"
    "v_mfma_f32_16x16x32_bf16 v[252:255], v[164:167], v[84:87], v[252:255]\n"
    "ds_read_b128 v[148:151], v176 offset:6144\n"
    "v_lshlrev_b32 v184, 10, v184\n"
    "v_mfma_f32_16x16x32_bf16 v[252:255], v[168:171], v[88:91], v[252:255]\n"
    "buffer_load_dwordx4 a[152:155], v190, s[76:79], s69 offen offset:2048 sc1\n"
    "v_mfma_f32_16x16x32_bf16 v[252:255], v[172:175], v[92:95], v[252:255]\n"
    "v_mfma_f32_16x16x32_bf16 v[28:31], v[160:163], a[0:3], 0\n"
    "v_mfma_f32_16x16x32_bf16 v[28:31], v[164:167], a[4:7], v[28:31]\n"
    "ds_read_b128 v[152:155], v176 offset:10240\n"
    "v_bfe_u32 v185, v190, 2, 5\n"
    "v_mfma_f32_16x16x32_bf16 v[28:31], v[168:171], a[8:11], v[28:31]\n"
    "buffer_load_dwordx4 a[156:159], v190, s[76:79], s69 offen offset:3072 sc1\n"
    "v_mfma_f32_16x16x32_bf16 v[28:31], v[172:175], a[12:15], v[28:31]\n"
    "v_mfma_f32_16x16x32_bf16 v[60:63], v[160:163], a[16:19], 0\n"
    "v_mfma_f32_16x16x32_bf16 v[60:63], v[164:167], a[20:23], v[60:63]\n"
    "s_add_u32 m0, s57, 16384\n"
    "ds_read_b128 v[156:159], v176 offset:14336\n"
    "v_lshlrev_b32 v185, 5, v185\n"
    "v_mfma_f32_16x16x32_bf16 v[60:63], v[168:171], a[24:27], v[60:63]\n"
    "s_add_u32 s68, s51, 0x0\n"
    "buffer_load_dwordx4 v190, s[76:79], s68 offen offset:0 sc1 lds\n"
    "v_mfma_f32_16x16x32_bf16 v[60:63], v[172:175], a[28:31], v[60:63]\n"
    "s_waitcnt lgkmcnt(8)\n"
    "v_mfma_f32_16x16x32_bf16 v[0:3], v[112:115], a[32:35], v[0:3]\n"
    "v_mfma_f32_16x16x32_bf16 v[0:3], v[116:119], a[36:39], v[0:3]\n"
    "ds_read_b128 v[160:163], v176 offset:3072\n"
    "v_mfma_f32_16x16x32_bf16 v[0:3], v[120:123], a[40:43], v[0:3]\n"
    "buffer_load_dwordx4 v190, s[76:79], s68 offen offset:1024 sc1 lds\n"
    "v_mfma_f32_16x16x32_bf16 v[0:3], v[124:127], a[44:47], v[0:3]\n"
    "ds_read_b128 v[164:167], v176 offset:7168\n"
    "v_mfma_f32_16x16x32_bf16 v[32:35], v[112:115], a[48:51], v[32:35]\n"
    "v_mfma_f32_16x16x32_bf16 v[32:35], v[116:119], a[52:55], v[32:35]\n"
    "ds_read_b128 v[168:171], v176 offset:11264\n"
    "v_mfma_f32_16x16x32_bf16 v[32:35], v[120:123], a[56:59], v[32:35]\n"
    "buffer_load_dwordx4 v190, s[76:79], s68 offen offset:2048 sc1 lds\n"
    "v_mfma_f32_16x16x32_bf16 v[32:35], v[124:127], a[60:63], v[32:35]\n"
    "s_waitcnt lgkmcnt(7)\n"
    "ds_read_b128 v[172:175], v176 offset:15360\n"
    "v_mfma_f32_16x16x32_bf16 v[4:7], v[128:131], a[32:35], v[4:7]\n"
    "v_mfma_f32_16x16x32_bf16 v[4:7], v[132:135], a[36:39], v[4:7]\n"
    "ds_read_b128 v[112:115], v190 offset:0\n"
    "v_mfma_f32_16x16x32_bf16 v[4:7], v[136:139], a[40:43], v[4:7]\n"
    "buffer_load_dwordx4 v190, s[76:79], s68 offen offset:3072 sc1 lds\n"
    "v_mfma_f32_16x16x32_bf16 v[4:7], v[140:143], a[44:47], v[4:7]\n"
    "ds_read_b128 v[116:119], v190 offset:4096\n"
    "v_mfma_f32_16x16x32_bf16 v[36:39], v[128:131], a[48:51], v[36:39]\n"
    "v_mfma_f32_16x16x32_bf16 v[36:39], v[132:135], a[52:55], v[36:39]\n"
    "s_add_u32 m0, s57, 81920\n"
    "ds_read_b128 v[120:123], v190 offset:8192\n"
    "v_mfma_f32_16x16x32_bf16 v[36:39], v[136:139], a[56:59], v[36:39]\n"
    "s_add_u32 s68, s51, 0x80000\n"
    "buffer_load_dwordx4 v190, s[76:79], s68 offen offset:0 sc1 lds\n"
    "v_mfma_f32_16x16x32_bf16 v[36:39], v[140:143], a[60:63], v[36:39]\n"
    "s_waitcnt lgkmcnt(7)\n"
    "ds_read_b128 v[124:127], v190 offset:12288\n"
    "v_mfma_f32_16x16x32_bf16 v[8:11], v[144:147], a[32:35], v[8:11]\n"
    "v_mfma_f32_16x16x32_bf16 v[8:11], v[148:151], a[36:39], v[8:11]\n"
    "ds_read_b128 v[128:131], v190 offset:1024\n"
    "v_mfma_f32_16x16x32_bf16 v[8:11], v[152:155], a[40:43], v[8:11]\n"
    "buffer_load_dwordx4 v190, s[76:79], s68 offen offset:1024 sc1 lds\n"
    "v_mfma_f32_16x16x32_bf16 v[8:11], v[156:159], a[44:47], v[8:11]\n"
    "ds_read_b128 v[132:135], v190 offset:5120\n"
    "v_mfma_f32_16x16x32_bf16 v[40:43], v[144:147], a[48:51], v[40:43]\n"
    "v_mfma_f32_16x16x32_bf16 v[40:43], v[148:151], a[52:55], v[40:43]\n"
    "ds_read_b128 v[136:139], v190 offset:9216\n"
    "v_mfma_f32_16x16x32_bf16 v[40:43], v[152:155], a[56:59], v[40:43]\n"
    "buffer_load_dwordx4 v190, s[76:79], s68 offen offset:2048 sc1 lds\n"
    "v_mfma_f32_16x16x32_bf16 v[40:43], v[156:159], a[60:63], v[40:43]\n"
    "s_waitcnt lgkmcnt(7)\n"
    "ds_read_b128 v[140:143], v190 offset:13312\n"
    "v_mfma_f32_16x16x32_bf16 v[12:15], v[160:163], a[32:35], v[12:15]\n"
    "v_mfma_f32_16x16x32_bf16 v[12:15], v[164:167], a[36:39], v[12:15]\n"
    "ds_read_b128 v[144:147], v190 offset:2048\n"
    "v_mfma_f32_16x16x32_bf16 v[12:15], v[168:171], a[40:43], v[12:15]\n"
    "buffer_load_dwordx4 v190, s[76:79], s68 offen offset:3072 sc1 lds\n"
    "v_mfma_f32_16x16x32_bf16 v[12:15], v[172:175], a[44:47], v[12:15]\n"
    "ds_read_b128 v[148:151], v190 offset:6144\n"
    "v_mfma_f32_16x16x32_bf16 v[44:47], v[160:163], a[48:51], v[44:47]\n"
    "v_mfma_f32_16x16x32_bf16 v[44:47], v[164:167], a[52:55], v[44:47]\n"
    "ds_read_b128 v[152:155], v190 offset:10240\n"
    "v_mfma_f32_16x16x32_bf16 v[44:47], v[168:171], a[56:59], v[44:47]\n"
    "s_add_u32 s69, s62, s87\n"
    "buffer_load_dwordx4 a[160:163], v190, s[76:79], s69 offen offset:0 sc1\n"
    "v_mfma_f32_16x16x32_bf16 v[44:47], v[172:175], a[60:63], v[44:47]\n"
    "s_waitcnt lgkmcnt(7)\n"
    "ds_read_b128 v[156:159], v190 offset:14336\n"
    "v_mfma_f32_16x16x32_bf16 v[16:19], v[112:115], a[32:35], v[16:19]\n"
    "v_mfma_f32_16x16x32_bf16 v[16:19], v[116:119], a[36:39], v[16:19]\n"
    "ds_read_b128 v[160:163], v190 offset:3072\n"
    "v_mfma_f32_16x16x32_bf16 v[16:19], v[120:123], a[40:43], v[16:19]\n"
    "buffer_load_dwordx4 a[164:167], v190, s[76:79], s69 offen offset:1024 sc1\n"
    "v_mfma_f32_16x16x32_bf16 v[16:19], v[124:127], a[44:47], v[16:19]\n"
    "ds_read_b128 v[164:167], v190 offset:7168\n"
    "v_mfma_f32_16x16x32_bf16 v[48:51], v[112:115], a[48:51], v[48:51]\n"
    "v_mfma_f32_16x16x32_bf16 v[48:51], v[116:119], a[52:55], v[48:51]\n"
    "ds_read_b128 v[168:171], v190 offset:11264\n"
    "v_mfma_f32_16x16x32_bf16 v[48:51], v[120:123], a[56:59], v[48:51]\n"
    "s_lshl_b32 s4, s52, 5\n"
    "s_add_u32 s4, s4, 0x5800000\n"
    "buffer_load_dword v187, v181, s[76:79], s4 offen sc1\n"
    "v_mfma_f32_16x16x32_bf16 v[48:51], v[124:127], a[60:63], v[48:51]\n"
    "s_waitcnt lgkmcnt(7)\n"
    "ds_read_b128 v[172:175], v190 offset:15360\n"
    "s_waitcnt vmcnt(20)\n"
    "s_barrier\n"
    "v_mfma_f32_16x16x32_bf16 v[20:23], v[128:131], a[32:35], v[20:23]\n"
    "buffer_load_dwordx4 a[168:171], v190, s[76:79], s69 offen offset:2048 sc1\n"
    "v_mfma_f32_16x16x32_bf16 v[20:23], v[132:135], a[36:39], v[20:23]\n"
    "ds_read_b128 v[112:115], v176 offset:32768\n"
    "v_mfma_f32_16x16x32_bf16 v[20:23], v[136:139], a[40:43], v[20:23]\n"
    "v_mfma_f32_16x16x32_bf16 v[20:23], v[140:143], a[44:47], v[20:23]\n"
    "ds_read_b128 v[116:119], v176 offset:36864\n"
    "v_mfma_f32_16x16x32_bf16 v[52:55], v[128:131], a[48:51], v[52:55]\n"
    "buffer_load_dwordx4 a[172:175], v190, s[76:79], s69 offen offset:3072 sc1\n"
    "v_mfma_f32_16x16x32_bf16 v[52:55], v[132:135], a[52:55], v[52:55]\n"
    "ds_read_b128 v[120:123], v176 offset:40960\n"
    "v_mfma_f32_16x16x32_bf16 v[52:55], v[136:139], a[56:59], v[52:55]\n"
    "v_mfma_f32_16x16x32_bf16 v[52:55], v[140:143], a[60:63], v[52:55]\n"
    "s_waitcnt lgkmcnt(7)\n"
    "ds_read_b128 v[124:127], v176 offset:45056\n"
    "v_mfma_f32_16x16x32_bf16 v[24:27], v[144:147], a[32:35], v[24:27]\n"
    "s_add_u32 s69, s69, 0x80000\n"
    "buffer_load_dwordx4 a[176:179], v190, s[76:79], s69 offen offset:0 sc1\n"
    "v_mfma_f32_16x16x32_bf16 v[24:27], v[148:151], a[36:39], v[24:27]\n"
    "ds_read_b128 v[128:131], v176 offset:33792\n"
    "v_mfma_f32_16x16x32_bf16 v[24:27], v[152:155], a[40:43], v[24:27]\n"
    "v_mfma_f32_16x16x32_bf16 v[24:27], v[156:159], a[44:47], v[24:27]\n"
    "ds_read_b128 v[132:135], v176 offset:37888\n"
    "v_mfma_f32_16x16x32_bf16 v[56:59], v[144:147], a[48:51], v[56:59]\n"
    "buffer_load_dwordx4 a[180:183], v190, s[76:79], s69 offen offset:1024 sc1\n"
    "v_mfma_f32_16x16x32_bf16 v[56:59], v[148:151], a[52:55], v[56:59]\n"
    "ds_read_b128 v[136:139], v176 offset:41984\n"
    "v_mfma_f32_16x16x32_bf16 v[56:59], v[152:155], a[56:59], v[56:59]\n"
    "v_mfma_f32_16x16x32_bf16 v[56:59], v[156:159], a[60:63], v[56:59]\n"
    "s_waitcnt lgkmcnt(7)\n"
    "ds_read_b128 v[140:143], v176 offset:46080\n"
    "v_mfma_f32_16x16x32_bf16 v[28:31], v[160:163], a[32:35], v[28:31]\n"
    "buffer_load_dwordx4 a[184:187], v190, s[76:79], s69 offen offset:2048 sc1\n"
    "v_mfma_f32_16x16x32_bf16 v[28:31], v[164:167], a[36:39], v[28:31]\n"
    "ds_read_b128 v[144:147], v176 offset:34816\n"
    "v_mfma_f32_16x16x32_bf16 v[28:31], v[168:171], a[40:43], v[28:31]\n"
    "v_mfma_f32_16x16x32_bf16 v[28:31], v[172:175], a[44:47], v[28:31]\n"
    "ds_read_b128 v[148:151], v176 offset:38912\n"
    "v_mfma_f32_16x16x32_bf16 v[60:63], v[160:163], a[48:51], v[60:63]\n"
    "buffer_load_dwordx4 a[188:191], v190, s[76:79], s69 offen offset:3072 sc1\n"
    "v_mfma_f32_16x16x32_bf16 v[60:63], v[164:167], a[52:55], v[60:63]\n"
    "ds_read_b128 v[152:155], v176 offset:43008\n"
    "v_mfma_f32_16x16x32_bf16 v[60:63], v[168:171], a[56:59], v[60:63]\n"
    "v_mfma_f32_16x16x32_bf16 v[60:63], v[172:175], a[60:63], v[60:63]\n"
    "s_waitcnt vmcnt(17) lgkmcnt(7)\n"
    "ds_read_b128 v[156:159], v176 offset:47104\n"
    "v_mfma_f32_16x16x32_bf16 v[0:3], v[112:115], a[128:131], v[0:3]\n"
    "buffer_load_dwordx4 v[64:67], v111, s[44:47], s16 offen\n"
    "v_mfma_f32_16x16x32_bf16 v[0:3], v[116:119], a[132:135], v[0:3]\n"
    "ds_read_b128 v[160:163], v176 offset:35840\n"
    "v_mfma_f32_16x16x32_bf16 v[0:3], v[120:123], a[136:139], v[0:3]\n"
    "s_add_u32 s70, s16, 0x100000\n"
    "buffer_load_dwordx4 v[68:71], v111, s[44:47], s70 offen\n"
    "v_mfma_f32_16x16x32_bf16 v[0:3], v[124:127], a[140:143], v[0:3]\n"
    "ds_read_b128 v[164:167], v176 offset:39936\n"
    "v_mfma_f32_16x16x32_bf16 v[32:35], v[112:115], a[144:147], v[32:35]\n"
    "s_waitcnt vmcnt(8)\n"
    "v_cmp_ne_u32 vcc, 1, v187\n"
    "s_cbranch_vccz L_cb_ok\n"
    "s_mov_b32 s14, 0x40000\n"
    "L_cb_spin:\n"
    "buffer_load_dword v187, v181, s[76:79], s4 offen sc1\n"
    "s_sub_u32 s14, s14, 1\n"
    "s_waitcnt vmcnt(0)\n"
    "s_cbranch_scc1 L_cb_ok\n"
    "v_cmp_ne_u32 vcc, 1, v187\n"
    "s_cbranch_vccz L_cb_ok\n"
    "s_sleep 3\n"
    "s_branch L_cb_spin\n"
    "L_cb_ok:\n"
    "v_mfma_f32_16x16x32_bf16 v[32:35], v[116:119], a[148:151], v[32:35]\n"
    "ds_read_b128 v[168:171], v176 offset:44032\n"
    "v_mfma_f32_16x16x32_bf16 v[32:35], v[120:123], a[152:155], v[32:35]\n"
    "s_add_u32 s69, s63, s85\n"
    "buffer_load_dwordx4 a[192:195], v190, s[76:79], s69 offen offset:0 sc1\n"
    "v_mfma_f32_16x16x32_bf16 v[32:35], v[124:127], a[156:159], v[32:35]\n"
    "s_waitcnt lgkmcnt(7)\n"
    "ds_read_b128 v[172:175], v176 offset:48128\n"
    "v_mfma_f32_16x16x32_bf16 v[4:7], v[128:131], a[128:131], v[4:7]\n"
    "v_mfma_f32_16x16x32_bf16 v[4:7], v[132:135], a[132:135], v[4:7]\n"
    "ds_read_b128 v[112:115], v176 offset:49152\n"
    "v_mfma_f32_16x16x32_bf16 v[4:7], v[136:139], a[136:139], v[4:7]\n"
    "buffer_load_dwordx4 a[196:199], v190, s[76:79], s69 offen offset:1024 sc1\n"
    "v_mfma_f32_16x16x32_bf16 v[4:7], v[140:143], a[140:143], v[4:7]\n"
    "ds_read_b128 v[116:119], v176 offset:53248\n"
    "v_mfma_f32_16x16x32_bf16 v[36:39], v[128:131], a[144:147], v[36:39]\n"
    "v_mfma_f32_16x16x32_bf16 v[36:39], v[132:135], a[148:151], v[36:39]\n"
    "ds_read_b128 v[120:123], v176 offset:57344\n"
    "v_mfma_f32_16x16x32_bf16 v[36:39], v[136:139], a[152:155], v[36:39]\n"
    "buffer_load_dwordx4 a[200:203], v190, s[76:79], s69 offen offset:2048 sc1\n"
    "v_mfma_f32_16x16x32_bf16 v[36:39], v[140:143], a[156:159], v[36:39]\n"
    "s_waitcnt lgkmcnt(7)\n"
    "ds_read_b128 v[124:127], v176 offset:61440\n"
    "v_mfma_f32_16x16x32_bf16 v[8:11], v[144:147], a[128:131], v[8:11]\n"
    "v_mfma_f32_16x16x32_bf16 v[8:11], v[148:151], a[132:135], v[8:11]\n"
    "ds_read_b128 v[128:131], v176 offset:50176\n"
    "v_mfma_f32_16x16x32_bf16 v[8:11], v[152:155], a[136:139], v[8:11]\n"
    "buffer_load_dwordx4 a[204:207], v190, s[76:79], s69 offen offset:3072 sc1\n"
    "v_mfma_f32_16x16x32_bf16 v[8:11], v[156:159], a[140:143], v[8:11]\n"
    "ds_read_b128 v[132:135], v176 offset:54272\n"
    "v_mfma_f32_16x16x32_bf16 v[40:43], v[144:147], a[144:147], v[40:43]\n"
    "v_mfma_f32_16x16x32_bf16 v[40:43], v[148:151], a[148:151], v[40:43]\n"
    "ds_read_b128 v[136:139], v176 offset:58368\n"
    "v_mfma_f32_16x16x32_bf16 v[40:43], v[152:155], a[152:155], v[40:43]\n"
    ".p2align 12\n"
    "L_pad10:\n"
    "s_cbranch_execz L_pad14\n"
    "s_add_u32 s69, s69, 0x80000\n"
    "buffer_load_dwordx4 a[208:211], v190, s[76:79], s69 offen offset:0 sc1\n"
    "v_mfma_f32_16x16x32_bf16 v[40:43], v[156:159], a[156:159], v[40:43]\n"
    "s_waitcnt lgkmcnt(7)\n"
    "ds_read_b128 v[140:143], v176 offset:62464\n"
    "v_mfma_f32_16x16x32_bf16 v[12:15], v[160:163], a[128:131], v[12:15]\n"
    "v_mfma_f32_16x16x32_bf16 v[12:15], v[164:167], a[132:135], v[12:15]\n"
    "ds_read_b128 v[144:147], v176 offset:51200\n"
    "v_mfma_f32_16x16x32_bf16 v[12:15], v[168:171], a[136:139], v[12:15]\n"
    "buffer_load_dwordx4 a[212:215], v190, s[76:79], s69 offen offset:1024 sc1\n"
    "v_mfma_f32_16x16x32_bf16 v[12:15], v[172:175], a[140:143], v[12:15]\n"
    "ds_read_b128 v[148:151], v176 offset:55296\n"
    "v_mfma_f32_16x16x32_bf16 v[44:47], v[160:163], a[144:147], v[44:47]\n"
    "v_mfma_f32_16x16x32_bf16 v[44:47], v[164:167], a[148:151], v[44:47]\n"
    "ds_read_b128 v[152:155], v176 offset:59392\n"
    "v_mfma_f32_16x16x32_bf16 v[44:47], v[168:171], a[152:155], v[44:47]\n"
    "buffer_load_dwordx4 a[216:219], v190, s[76:79], s69 offen offset:2048 sc1\n"
    "v_mfma_f32_16x16x32_bf16 v[44:47], v[172:175], a[156:159], v[44:47]\n"
    "s_waitcnt lgkmcnt(7)\n"
    "ds_read_b128 v[156:159], v176 offset:63488\n"
    "v_mfma_f32_16x16x32_bf16 v[16:19], v[112:115], a[128:131], v[16:19]\n"
    "v_mfma_f32_16x16x32_bf16 v[16:19], v[116:119], a[132:135], v[16:19]\n"
    "ds_read_b128 v[160:163], v176 offset:52224\n"
    "v_mfma_f32_16x16x32_bf16 v[16:19], v[120:123], a[136:139], v[16:19]\n"
    "buffer_load_dwordx4 a[220:223], v190, s[76:79], s69 offen offset:3072 sc1\n"
    "v_mfma_f32_16x16x32_bf16 v[16:19], v[124:127], a[140:143], v[16:19]\n"
    "ds_read_b128 v[164:167], v176 offset:56320\n"
    "v_mfma_f32_16x16x32_bf16 v[48:51], v[112:115], a[144:147], v[48:51]\n"
    "v_mfma_f32_16x16x32_bf16 v[48:51], v[116:119], a[148:151], v[48:51]\n"
    "ds_read_b128 v[168:171], v176 offset:60416\n"
    "v_mfma_f32_16x16x32_bf16 v[48:51], v[120:123], a[152:155], v[48:51]\n"
    "s_add_u32 s69, s63, s86\n"
    "buffer_load_dwordx4 a[224:227], v190, s[76:79], s69 offen offset:0 sc1\n"
    "v_mfma_f32_16x16x32_bf16 v[48:51], v[124:127], a[156:159], v[48:51]\n"
    "s_waitcnt lgkmcnt(7)\n"
    "ds_read_b128 v[172:175], v176 offset:64512\n"
    "s_barrier\n"
    "v_mfma_f32_16x16x32_bf16 v[20:23], v[128:131], a[128:131], v[20:23]\n"
    "v_mfma_f32_16x16x32_bf16 v[20:23], v[132:135], a[132:135], v[20:23]\n"
    "ds_read_b128 v[112:115], v190 offset:16384\n"
    "v_mfma_f32_16x16x32_bf16 v[20:23], v[136:139], a[136:139], v[20:23]\n"
    "buffer_load_dwordx4 a[228:231], v190, s[76:79], s69 offen offset:1024 sc1\n"
    "v_mfma_f32_16x16x32_bf16 v[20:23], v[140:143], a[140:143], v[20:23]\n"
    "ds_read_b128 v[116:119], v190 offset:20480\n"
    "v_mfma_f32_16x16x32_bf16 v[52:55], v[128:131], a[144:147], v[52:55]\n"
    "v_mfma_f32_16x16x32_bf16 v[52:55], v[132:135], a[148:151], v[52:55]\n"
    "ds_read_b128 v[120:123], v190 offset:24576\n"
    "v_mfma_f32_16x16x32_bf16 v[52:55], v[136:139], a[152:155], v[52:55]\n"
    "buffer_load_dwordx4 a[232:235], v190, s[76:79], s69 offen offset:2048 sc1\n"
    "v_mfma_f32_16x16x32_bf16 v[52:55], v[140:143], a[156:159], v[52:55]\n"
    "s_waitcnt lgkmcnt(7)\n"
    "ds_read_b128 v[124:127], v190 offset:28672\n"
    "v_mfma_f32_16x16x32_bf16 v[24:27], v[144:147], a[128:131], v[24:27]\n"
    "v_mfma_f32_16x16x32_bf16 v[24:27], v[148:151], a[132:135], v[24:27]\n"
    "ds_read_b128 v[128:131], v190 offset:17408\n"
    "v_mfma_f32_16x16x32_bf16 v[24:27], v[152:155], a[136:139], v[24:27]\n"
    "buffer_load_dwordx4 a[236:239], v190, s[76:79], s69 offen offset:3072 sc1\n"
    "v_mfma_f32_16x16x32_bf16 v[24:27], v[156:159], a[140:143], v[24:27]\n"
    "ds_read_b128 v[132:135], v190 offset:21504\n"
    "v_mfma_f32_16x16x32_bf16 v[56:59], v[144:147], a[144:147], v[56:59]\n"
    "v_mfma_f32_16x16x32_bf16 v[56:59], v[148:151], a[148:151], v[56:59]\n"
    "ds_read_b128 v[136:139], v190 offset:25600\n"
    "v_mfma_f32_16x16x32_bf16 v[56:59], v[152:155], a[152:155], v[56:59]\n"
    "s_add_u32 s69, s69, 0x80000\n"
    "buffer_load_dwordx4 a[240:243], v190, s[76:79], s69 offen offset:0 sc1\n"
    "v_mfma_f32_16x16x32_bf16 v[56:59], v[156:159], a[156:159], v[56:59]\n"
    "s_waitcnt lgkmcnt(7)\n"
    "ds_read_b128 v[140:143], v190 offset:29696\n"
    "v_mfma_f32_16x16x32_bf16 v[28:31], v[160:163], a[128:131], v[28:31]\n"
    "v_mfma_f32_16x16x32_bf16 v[28:31], v[164:167], a[132:135], v[28:31]\n"
    "ds_read_b128 v[144:147], v190 offset:18432\n"
    "v_mfma_f32_16x16x32_bf16 v[28:31], v[168:171], a[136:139], v[28:31]\n"
    "buffer_load_dwordx4 a[244:247], v190, s[76:79], s69 offen offset:1024 sc1\n"
    "v_mfma_f32_16x16x32_bf16 v[28:31], v[172:175], a[140:143], v[28:31]\n"
    "ds_read_b128 v[148:151], v190 offset:22528\n"
    "v_mfma_f32_16x16x32_bf16 v[60:63], v[160:163], a[144:147], v[60:63]\n"
    "v_mfma_f32_16x16x32_bf16 v[60:63], v[164:167], a[148:151], v[60:63]\n"
    "ds_read_b128 v[152:155], v190 offset:26624\n"
    "v_mfma_f32_16x16x32_bf16 v[60:63], v[168:171], a[152:155], v[60:63]\n"
    "buffer_load_dwordx4 a[248:251], v190, s[76:79], s69 offen offset:2048 sc1\n"
    "v_mfma_f32_16x16x32_bf16 v[60:63], v[172:175], a[156:159], v[60:63]\n"
    "s_waitcnt vmcnt(17) lgkmcnt(7)\n"
    "ds_read_b128 v[156:159], v190 offset:30720\n"
    "v_mfma_f32_16x16x32_bf16 v[0:3], v[112:115], a[160:163], v[0:3]\n"
    "v_mfma_f32_16x16x32_bf16 v[0:3], v[116:119], a[164:167], v[0:3]\n"
    "ds_read_b128 v[160:163], v190 offset:19456\n"
    "v_mfma_f32_16x16x32_bf16 v[0:3], v[120:123], a[168:171], v[0:3]\n"
    "buffer_load_dwordx4 a[252:255], v190, s[76:79], s69 offen offset:3072 sc1\n"
    "v_mfma_f32_16x16x32_bf16 v[0:3], v[124:127], a[172:175], v[0:3]\n"
    "ds_read_b128 v[164:167], v190 offset:23552\n"
    "v_mfma_f32_16x16x32_bf16 v[32:35], v[112:115], a[176:179], v[32:35]\n"
    "v_mfma_f32_16x16x32_bf16 v[32:35], v[116:119], a[180:183], v[32:35]\n"
    "ds_read_b128 v[168:171], v190 offset:27648\n"
    "v_mfma_f32_16x16x32_bf16 v[32:35], v[120:123], a[184:187], v[32:35]\n"
    "s_add_u32 s69, s63, s87\n"
    "buffer_load_dwordx4 a[64:67], v190, s[76:79], s69 offen offset:0 sc1\n"
    "v_mfma_f32_16x16x32_bf16 v[32:35], v[124:127], a[188:191], v[32:35]\n"
    "s_waitcnt lgkmcnt(7)\n"
    "ds_read_b128 v[172:175], v190 offset:31744\n"
    "v_mfma_f32_16x16x32_bf16 v[4:7], v[128:131], a[160:163], v[4:7]\n"
    "v_mfma_f32_16x16x32_bf16 v[4:7], v[132:135], a[164:167], v[4:7]\n"
    "ds_read_b128 v[112:115], v176 offset:16384\n"
    "v_mfma_f32_16x16x32_bf16 v[4:7], v[136:139], a[168:171], v[4:7]\n"
    "buffer_load_dwordx4 a[68:71], v190, s[76:79], s69 offen offset:1024 sc1\n"
    "v_mfma_f32_16x16x32_bf16 v[4:7], v[140:143], a[172:175], v[4:7]\n"
    "ds_read_b128 v[116:119], v176 offset:20480\n"
    "v_mfma_f32_16x16x32_bf16 v[36:39], v[128:131], a[176:179], v[36:39]\n"
    "v_mfma_f32_16x16x32_bf16 v[36:39], v[132:135], a[180:183], v[36:39]\n"
    "ds_read_b128 v[120:123], v176 offset:24576\n"
    "v_mfma_f32_16x16x32_bf16 v[36:39], v[136:139], a[184:187], v[36:39]\n"
    "v_mfma_f32_16x16x32_bf16 v[36:39], v[140:143], a[188:191], v[36:39]\n"
    "s_waitcnt lgkmcnt(7)\n"
    "ds_read_b128 v[124:127], v176 offset:28672\n"
    "v_mfma_f32_16x16x32_bf16 v[8:11], v[144:147], a[160:163], v[8:11]\n"
    "v_mfma_f32_16x16x32_bf16 v[8:11], v[148:151], a[164:167], v[8:11]\n"
    "ds_read_b128 v[128:131], v176 offset:17408\n"
    "v_mfma_f32_16x16x32_bf16 v[8:11], v[152:155], a[168:171], v[8:11]\n"
    "v_mfma_f32_16x16x32_bf16 v[8:11], v[156:159], a[172:175], v[8:11]\n"
    "ds_read_b128 v[132:135], v176 offset:21504\n"
    "v_cvt_pk_bf16_f32 v0, v0, v1\n"
    "v_mfma_f32_16x16x32_bf16 v[40:43], v[144:147], a[176:179], v[40:43]\n"
    "v_mfma_f32_16x16x32_bf16 v[40:43], v[148:151], a[180:183], v[40:43]\n"
    "ds_read_b128 v[136:139], v176 offset:25600\n"
    "v_cvt_pk_bf16_f32 v1, v2, v3\n"
    "v_mfma_f32_16x16x32_bf16 v[40:43], v[152:155], a[184:187], v[40:43]\n"
    "v_mfma_f32_16x16x32_bf16 v[40:43], v[156:159], a[188:191], v[40:43]\n"
    "s_waitcnt lgkmcnt(7)\n"
    "ds_read_b128 v[140:143], v176 offset:29696\n"
    "v_cvt_pk_bf16_f32 v2, v4, v5\n"
    "v_mfma_f32_16x16x32_bf16 v[12:15], v[160:163], a[160:163], v[12:15]\n"
    "v_mfma_f32_16x16x32_bf16 v[12:15], v[164:167], a[164:167], v[12:15]\n"
    "ds_read_b128 v[144:147], v176 offset:18432\n"
    "v_cvt_pk_bf16_f32 v3, v6, v7\n"
    "v_mfma_f32_16x16x32_bf16 v[12:15], v[168:171], a[168:171], v[12:15]\n"
    "s_add_u32 s70, s88, 0x0\n"
    "buffer_store_dwordx4 v[0:3], v190, s[76:79], s70 offen sc1\n"
    "v_mfma_f32_16x16x32_bf16 v[12:15], v[172:175], a[172:175], v[12:15]\n"
    "ds_read_b128 v[148:151], v176 offset:22528\n"
    "v_cvt_pk_bf16_f32 v32, v32, v33\n"
    "v_mfma_f32_16x16x32_bf16 v[44:47], v[160:163], a[176:179], v[44:47]\n"
    "v_mfma_f32_16x16x32_bf16 v[44:47], v[164:167], a[180:183], v[44:47]\n"
    "ds_read_b128 v[152:155], v176 offset:26624\n"
    "v_cvt_pk_bf16_f32 v33, v34, v35\n"
    "v_mfma_f32_16x16x32_bf16 v[44:47], v[168:171], a[184:187], v[44:47]\n"
    "v_mfma_f32_16x16x32_bf16 v[44:47], v[172:175], a[188:191], v[44:47]\n"
    "s_waitcnt lgkmcnt(7)\n"
    "ds_read_b128 v[156:159], v176 offset:30720\n"
    "v_cvt_pk_bf16_f32 v34, v36, v37\n"
    "v_mfma_f32_16x16x32_bf16 v[16:19], v[112:115], a[160:163], v[16:19]\n"
    "v_mfma_f32_16x16x32_bf16 v[16:19], v[116:119], a[164:167], v[16:19]\n"
    "ds_read_b128 v[160:163], v176 offset:19456\n"
    "v_cvt_pk_bf16_f32 v35, v38, v39\n"
    "v_mfma_f32_16x16x32_bf16 v[16:19], v[120:123], a[168:171], v[16:19]\n"
    "v_mfma_f32_16x16x32_bf16 v[16:19], v[124:127], a[172:175], v[16:19]\n"
    "ds_read_b128 v[164:167], v176 offset:23552\n"
    "v_cvt_pk_bf16_f32 v8, v8, v9\n"
    "v_mfma_f32_16x16x32_bf16 v[48:51], v[112:115], a[176:179], v[48:51]\n"
    "v_mfma_f32_16x16x32_bf16 v[48:51], v[116:119], a[180:183], v[48:51]\n"
    "ds_read_b128 v[168:171], v176 offset:27648\n"
    "v_cvt_pk_bf16_f32 v9, v10, v11\n"
    "v_mfma_f32_16x16x32_bf16 v[48:51], v[120:123], a[184:187], v[48:51]\n"
    "buffer_load_dwordx4 a[72:75], v190, s[76:79], s69 offen offset:2048 sc1\n"
    "v_mfma_f32_16x16x32_bf16 v[48:51], v[124:127], a[188:191], v[48:51]\n"
    "s_waitcnt lgkmcnt(7)\n"
    "ds_read_b128 v[172:175], v176 offset:31744\n"
    "v_cvt_pk_bf16_f32 v10, v12, v13\n"
    "v_mfma_f32_16x16x32_bf16 v[20:23], v[128:131], a[160:163], v[20:23]\n"
    "v_mfma_f32_16x16x32_bf16 v[20:23], v[132:135], a[164:167], v[20:23]\n"
    "ds_read_b128 v[112:115], v176 offset:0\n"
    "v_cvt_pk_bf16_f32 v11, v14, v15\n"
    "v_mfma_f32_16x16x32_bf16 v[20:23], v[136:139], a[168:171], v[20:23]\n"
    "buffer_load_dwordx4 a[76:79], v190, s[76:79], s69 offen offset:3072 sc1\n"
    "v_mfma_f32_16x16x32_bf16 v[20:23], v[140:143], a[172:175], v[20:23]\n"
    "ds_read_b128 v[116:119], v176 offset:4096\n"
    "v_cvt_pk_bf16_f32 v40, v40, v41\n"
    "v_mfma_f32_16x16x32_bf16 v[52:55], v[128:131], a[176:179], v[52:55]\n"
    "v_mfma_f32_16x16x32_bf16 v[52:55], v[132:135], a[180:183], v[52:55]\n"
    "ds_read_b128 v[120:123], v176 offset:8192\n"
    "v_cvt_pk_bf16_f32 v41, v42, v43\n"
    "v_mfma_f32_16x16x32_bf16 v[52:55], v[136:139], a[184:187], v[52:55]\n"
    "s_add_u32 s70, s88, 0x80000\n"
    "buffer_store_dwordx4 v[32:35], v190, s[76:79], s70 offen sc1\n"
    "v_mfma_f32_16x16x32_bf16 v[52:55], v[140:143], a[188:191], v[52:55]\n"
    "s_waitcnt lgkmcnt(7)\n"
    "ds_read_b128 v[124:127], v176 offset:12288\n"
    "v_cvt_pk_bf16_f32 v42, v44, v45\n"
    "v_mfma_f32_16x16x32_bf16 v[24:27], v[144:147], a[160:163], v[24:27]\n"
    "v_mfma_f32_16x16x32_bf16 v[24:27], v[148:151], a[164:167], v[24:27]\n"
    "ds_read_b128 v[128:131], v176 offset:1024\n"
    "v_cvt_pk_bf16_f32 v43, v46, v47\n"
    "v_mfma_f32_16x16x32_bf16 v[24:27], v[152:155], a[168:171], v[24:27]\n"
    "v_mfma_f32_16x16x32_bf16 v[24:27], v[156:159], a[172:175], v[24:27]\n"
    "ds_read_b128 v[132:135], v176 offset:5120\n"
    "v_cvt_pk_bf16_f32 v16, v16, v17\n"
    "v_mfma_f32_16x16x32_bf16 v[56:59], v[144:147], a[176:179], v[56:59]\n"
    "v_mfma_f32_16x16x32_bf16 v[56:59], v[148:151], a[180:183], v[56:59]\n"
    "ds_read_b128 v[136:139], v176 offset:9216\n"
    "v_cvt_pk_bf16_f32 v17, v18, v19\n"
    "v_mfma_f32_16x16x32_bf16 v[56:59], v[152:155], a[184:187], v[56:59]\n"
    "v_mfma_f32_16x16x32_bf16 v[56:59], v[156:159], a[188:191], v[56:59]\n"
    "s_waitcnt lgkmcnt(7)\n"
    "ds_read_b128 v[140:143], v176 offset:13312\n"
    "v_cvt_pk_bf16_f32 v18, v20, v21\n"
    "v_mfma_f32_16x16x32_bf16 v[28:31], v[160:163], a[160:163], v[28:31]\n"
    "v_mfma_f32_16x16x32_bf16 v[28:31], v[164:167], a[164:167], v[28:31]\n"
    "ds_read_b128 v[144:147], v176 offset:2048\n"
    "v_cvt_pk_bf16_f32 v19, v22, v23\n"
    "v_mfma_f32_16x16x32_bf16 v[28:31], v[168:171], a[168:171], v[28:31]\n"
    "s_add_u32 s69, s69, 0x80000\n"
    "buffer_load_dwordx4 a[80:83], v190, s[76:79], s69 offen offset:0 sc1\n"
    "v_mfma_f32_16x16x32_bf16 v[28:31], v[172:175], a[172:175], v[28:31]\n"
    "ds_read_b128 v[148:151], v176 offset:6144\n"
    "v_cvt_pk_bf16_f32 v48, v48, v49\n"
    "v_mfma_f32_16x16x32_bf16 v[60:63], v[160:163], a[176:179], v[60:63]\n"
    "v_mfma_f32_16x16x32_bf16 v[60:63], v[164:167], a[180:183], v[60:63]\n"
    "ds_read_b128 v[152:155], v176 offset:10240\n"
    "v_cvt_pk_bf16_f32 v49, v50, v51\n"
    "v_mfma_f32_16x16x32_bf16 v[60:63], v[168:171], a[184:187], v[60:63]\n"
    "buffer_load_dwordx4 a[84:87], v190, s[76:79], s69 offen offset:1024 sc1\n"
    "v_mfma_f32_16x16x32_bf16 v[60:63], v[172:175], a[188:191], v[60:63]\n"
    "s_waitcnt vmcnt(16) lgkmcnt(7)\n"
    "ds_read_b128 v[156:159], v176 offset:14336\n"
    "v_cvt_pk_bf16_f32 v50, v52, v53\n"
    "v_mfma_f32_16x16x32_bf16 v[192:195], v[112:115], a[192:195], v[192:195]\n"
    "v_mfma_f32_16x16x32_bf16 v[192:195], v[116:119], a[196:199], v[192:195]\n"
    "ds_read_b128 v[160:163], v176 offset:3072\n"
    "v_cvt_pk_bf16_f32 v51, v54, v55\n"
    "v_mfma_f32_16x16x32_bf16 v[192:195], v[120:123], a[200:203], v[192:195]\n"
    "s_add_u32 s70, s88, 0x2000\n"
    "buffer_store_dwordx4 v[8:11], v190, s[76:79], s70 offen sc1\n"
    "v_mfma_f32_16x16x32_bf16 v[192:195], v[124:127], a[204:207], v[192:195]\n"
    "ds_read_b128 v[164:167], v176 offset:7168\n"
    "v_cvt_pk_bf16_f32 v24, v24, v25\n"
    "v_mfma_f32_16x16x32_bf16 v[224:227], v[112:115], a[208:211], v[224:227]\n"
    "v_mfma_f32_16x16x32_bf16 v[224:227], v[116:119], a[212:215], v[224:227]\n"
    "ds_read_b128 v[168:171], v176 offset:11264\n"
    "v_cvt_pk_bf16_f32 v25, v26, v27\n"
    "v_mfma_f32_16x16x32_bf16 v[224:227], v[120:123], a[216:219], v[224:227]\n"
    "v_mfma_f32_16x16x32_bf16 v[224:227], v[124:127], a[220:223], v[224:227]\n"
    "s_waitcnt lgkmcnt(7)\n"
    "ds_read_b128 v[172:175], v176 offset:15360\n"
    "v_cvt_pk_bf16_f32 v26, v28, v29\n"
    "v_mfma_f32_16x16x32_bf16 v[196:199], v[128:131], a[192:195], v[196:199]\n"
    "v_mfma_f32_16x16x32_bf16 v[196:199], v[132:135], a[196:199], v[196:199]\n"
    "ds_read_b128 v[112:115], v190 offset:0\n"
    "v_cvt_pk_bf16_f32 v27, v30, v31\n"
    "v_mfma_f32_16x16x32_bf16 v[196:199], v[136:139], a[200:203], v[196:199]\n"
    "v_mfma_f32_16x16x32_bf16 v[196:199], v[140:143], a[204:207], v[196:199]\n"
    "ds_read_b128 v[116:119], v190 offset:4096\n"
    "v_cvt_pk_bf16_f32 v56, v56, v57\n"
    "v_mfma_f32_16x16x32_bf16 v[228:231], v[128:131], a[208:211], v[228:231]\n"
    "v_mfma_f32_16x16x32_bf16 v[228:231], v[132:135], a[212:215], v[228:231]\n"
    "ds_read_b128 v[120:123], v190 offset:8192\n"
    "v_cvt_pk_bf16_f32 v57, v58, v59\n"
    "v_mfma_f32_16x16x32_bf16 v[228:231], v[136:139], a[216:219], v[228:231]\n"
    "buffer_load_dwordx4 a[88:91], v190, s[76:79], s69 offen offset:2048 sc1\n"
    "v_mfma_f32_16x16x32_bf16 v[228:231], v[140:143], a[220:223], v[228:231]\n"
    "s_waitcnt lgkmcnt(7)\n"
    "ds_read_b128 v[124:127], v190 offset:12288\n"
    "v_cvt_pk_bf16_f32 v58, v60, v61\n"
    "v_mfma_f32_16x16x32_bf16 v[200:203], v[144:147], a[192:195], v[200:203]\n"
    "v_mfma_f32_16x16x32_bf16 v[200:203], v[148:151], a[196:199], v[200:203]\n"
    "ds_read_b128 v[128:131], v190 offset:1024\n"
    "v_cvt_pk_bf16_f32 v59, v62, v63\n"
    "v_mfma_f32_16x16x32_bf16 v[200:203], v[152:155], a[200:203], v[200:203]\n"
    "buffer_load_dwordx4 a[92:95], v190, s[76:79], s69 offen offset:3072 sc1\n"
    "v_mfma_f32_16x16x32_bf16 v[200:203], v[156:159], a[204:207], v[200:203]\n"
    "ds_read_b128 v[132:135], v190 offset:5120\n"
    "v_mfma_f32_16x16x32_bf16 v[232:235], v[144:147], a[208:211], v[232:235]\n"
    "v_mfma_f32_16x16x32_bf16 v[232:235], v[148:151], a[212:215], v[232:235]\n"
    "ds_read_b128 v[136:139], v190 offset:9216\n"
    "v_mfma_f32_16x16x32_bf16 v[232:235], v[152:155], a[216:219], v[232:235]\n"
    "s_add_u32 s70, s88, 0x82000\n"
    "buffer_store_dwordx4 v[40:43], v190, s[76:79], s70 offen sc1\n"
    "v_mfma_f32_16x16x32_bf16 v[232:235], v[156:159], a[220:223], v[232:235]\n"
    "s_waitcnt lgkmcnt(7)\n"
    "ds_read_b128 v[140:143], v190 offset:13312\n"
    "v_mfma_f32_16x16x32_bf16 v[204:207], v[160:163], a[192:195], v[204:207]\n"
    "v_mfma_f32_16x16x32_bf16 v[204:207], v[164:167], a[196:199], v[204:207]\n"
    "ds_read_b128 v[144:147], v190 offset:2048\n"
    "v_mfma_f32_16x16x32_bf16 v[204:207], v[168:171], a[200:203], v[204:207]\n"
    "v_mfma_f32_16x16x32_bf16 v[204:207], v[172:175], a[204:207], v[204:207]\n"
    "ds_read_b128 v[148:151], v190 offset:6144\n"
    "v_mfma_f32_16x16x32_bf16 v[236:239], v[160:163], a[208:211], v[236:239]\n"
    "v_mfma_f32_16x16x32_bf16 v[236:239], v[164:167], a[212:215], v[236:239]\n"
    "ds_read_b128 v[152:155], v190 offset:10240\n"
    "v_mfma_f32_16x16x32_bf16 v[236:239], v[168:171], a[216:219], v[236:239]\n"
    "v_mfma_f32_16x16x32_bf16 v[236:239], v[172:175], a[220:223], v[236:239]\n"
    "s_waitcnt lgkmcnt(7)\n"
    "ds_read_b128 v[156:159], v190 offset:14336\n"
    "v_mfma_f32_16x16x32_bf16 v[208:211], v[112:115], a[192:195], v[208:211]\n"
    "v_mfma_f32_16x16x32_bf16 v[208:211], v[116:119], a[196:199], v[208:211]\n"
    "ds_read_b128 v[160:163], v190 offset:3072\n"
    "v_mfma_f32_16x16x32_bf16 v[208:211], v[120:123], a[200:203], v[208:211]\n"
    "s_add_u32 s70, s88, 0x4000\n"
    "buffer_store_dwordx4 v[16:19], v190, s[76:79], s70 offen sc1\n"
    "v_mfma_f32_16x16x32_bf16 v[208:211], v[124:127], a[204:207], v[208:211]\n"
    "ds_read_b128 v[164:167], v190 offset:7168\n"
    "v_mfma_f32_16x16x32_bf16 v[240:243], v[112:115], a[208:211], v[240:243]\n"
    "v_mfma_f32_16x16x32_bf16 v[240:243], v[116:119], a[212:215], v[240:243]\n"
    "ds_read_b128 v[168:171], v190 offset:11264\n"
    "v_mfma_f32_16x16x32_bf16 v[240:243], v[120:123], a[216:219], v[240:243]\n"
    "v_mfma_f32_16x16x32_bf16 v[240:243], v[124:127], a[220:223], v[240:243]\n"
    "s_waitcnt lgkmcnt(7)\n"
    "ds_read_b128 v[172:175], v190 offset:15360\n"
    "v_mfma_f32_16x16x32_bf16 v[212:215], v[128:131], a[192:195], v[212:215]\n"
    "v_mfma_f32_16x16x32_bf16 v[212:215], v[132:135], a[196:199], v[212:215]\n"
    "ds_read_b128 v[112:115], v176 offset:32768\n"
    "v_mfma_f32_16x16x32_bf16 v[212:215], v[136:139], a[200:203], v[212:215]\n"
    "v_mfma_f32_16x16x32_bf16 v[212:215], v[140:143], a[204:207], v[212:215]\n"
    "ds_read_b128 v[116:119], v176 offset:36864\n"
    "v_mfma_f32_16x16x32_bf16 v[244:247], v[128:131], a[208:211], v[244:247]\n"
    "v_mfma_f32_16x16x32_bf16 v[244:247], v[132:135], a[212:215], v[244:247]\n"
    "ds_read_b128 v[120:123], v176 offset:40960\n"
    "v_mfma_f32_16x16x32_bf16 v[244:247], v[136:139], a[216:219], v[244:247]\n"
    "s_add_u32 s70, s88, 0x84000\n"
    "buffer_store_dwordx4 v[48:51], v190, s[76:79], s70 offen sc1\n"
    "v_mfma_f32_16x16x32_bf16 v[244:247], v[140:143], a[220:223], v[244:247]\n"
    "s_waitcnt lgkmcnt(7)\n"
    "ds_read_b128 v[124:127], v176 offset:45056\n"
    "v_mfma_f32_16x16x32_bf16 v[216:219], v[144:147], a[192:195], v[216:219]\n"
    "v_mfma_f32_16x16x32_bf16 v[216:219], v[148:151], a[196:199], v[216:219]\n"
    "ds_read_b128 v[128:131], v190 offset:16384\n"
    "v_mfma_f32_16x16x32_bf16 v[216:219], v[152:155], a[200:203], v[216:219]\n"
    "v_mfma_f32_16x16x32_bf16 v[216:219], v[156:159], a[204:207], v[216:219]\n"
    "ds_read_b128 v[132:135], v190 offset:20480\n"
    "v_mfma_f32_16x16x32_bf16 v[248:251], v[144:147], a[208:211], v[248:251]\n"
    "v_mfma_f32_16x16x32_bf16 v[248:251], v[148:151], a[212:215], v[248:251]\n"
    "ds_read_b128 v[136:139], v190 offset:24576\n"
    "v_mfma_f32_16x16x32_bf16 v[248:251], v[152:155], a[216:219], v[248:251]\n"
    "v_mfma_f32_16x16x32_bf16 v[248:251], v[156:159], a[220:223], v[248:251]\n"
    "s_waitcnt lgkmcnt(7)\n"
    "ds_read_b128 v[140:143], v190 offset:28672\n"
    "v_mfma_f32_16x16x32_bf16 v[220:223], v[160:163], a[192:195], v[220:223]\n"
    "v_mfma_f32_16x16x32_bf16 v[220:223], v[164:167], a[196:199], v[220:223]\n"
    "ds_read_b128 v[144:147], v176 offset:33792\n"
    "v_mfma_f32_16x16x32_bf16 v[220:223], v[168:171], a[200:203], v[220:223]\n"
    "s_add_u32 s70, s88, 0x6000\n"
    "buffer_store_dwordx4 v[24:27], v190, s[76:79], s70 offen sc1\n"
    "v_mfma_f32_16x16x32_bf16 v[220:223], v[172:175], a[204:207], v[220:223]\n"
    "ds_read_b128 v[148:151], v176 offset:37888\n"
    "v_mfma_f32_16x16x32_bf16 v[252:255], v[160:163], a[208:211], v[252:255]\n"
    "v_mfma_f32_16x16x32_bf16 v[252:255], v[164:167], a[212:215], v[252:255]\n"
    "ds_read_b128 v[152:155], v176 offset:41984\n"
    "v_mfma_f32_16x16x32_bf16 v[252:255], v[168:171], a[216:219], v[252:255]\n"
    "v_mfma_f32_16x16x32_bf16 v[252:255], v[172:175], a[220:223], v[252:255]\n"
    "s_waitcnt vmcnt(15) lgkmcnt(7)\n"
    "ds_read_b128 v[156:159], v176 offset:46080\n"
    "v_mfma_f32_16x16x32_bf16 v[192:195], v[112:115], a[224:227], v[192:195]\n"
    "v_mfma_f32_16x16x32_bf16 v[192:195], v[116:119], a[228:231], v[192:195]\n"
    "ds_read_b128 v[160:163], v190 offset:17408\n"
    "v_mfma_f32_16x16x32_bf16 v[192:195], v[120:123], a[232:235], v[192:195]\n"
    "v_mfma_f32_16x16x32_bf16 v[192:195], v[124:127], a[236:239], v[192:195]\n"
    "ds_read_b128 v[164:167], v190 offset:21504\n"
    "v_mfma_f32_16x16x32_bf16 v[224:227], v[112:115], a[240:243], v[224:227]\n"
    "v_mfma_f32_16x16x32_bf16 v[224:227], v[116:119], a[244:247], v[224:227]\n"
    "ds_read_b128 v[168:171], v190 offset:25600\n"
    "v_mfma_f32_16x16x32_bf16 v[224:227], v[120:123], a[248:251], v[224:227]\n"
    "s_add_u32 s70, s88, 0x86000\n"
    "buffer_store_dwordx4 v[56:59], v190, s[76:79], s70 offen sc1\n"
    "v_mfma_f32_16x16x32_bf16 v[224:227], v[124:127], a[252:255], v[224:227]\n"
    "s_waitcnt vmcnt(5) lgkmcnt(7)\n"
    "ds_read_b128 v[172:175], v190 offset:29696\n"
    "v_mfma_f32_16x16x32_bf16 v[192:195], v[128:131], a[64:67], v[192:195]\n"
    "v_mfma_f32_16x16x32_bf16 v[192:195], v[132:135], a[68:71], v[192:195]\n"
    "ds_read_b128 v[112:115], v176 offset:34816\n"
    "v_mfma_f32_16x16x32_bf16 v[192:195], v[136:139], a[72:75], v[192:195]\n"
    "v_mfma_f32_16x16x32_bf16 v[192:195], v[140:143], a[76:79], v[192:195]\n"
    "ds_read_b128 v[116:119], v176 offset:38912\n"
    "v_mfma_f32_16x16x32_bf16 v[224:227], v[128:131], a[80:83], v[224:227]\n"
    "v_mfma_f32_16x16x32_bf16 v[224:227], v[132:135], a[84:87], v[224:227]\n"
    "ds_read_b128 v[120:123], v176 offset:43008\n"
    "v_mfma_f32_16x16x32_bf16 v[224:227], v[136:139], a[88:91], v[224:227]\n"
    "v_mfma_f32_16x16x32_bf16 v[224:227], v[140:143], a[92:95], v[224:227]\n"
    "s_waitcnt lgkmcnt(7)\n"
    "ds_read_b128 v[124:127], v176 offset:47104\n"
    "v_mfma_f32_16x16x32_bf16 v[196:199], v[144:147], a[224:227], v[196:199]\n"
    "v_mfma_f32_16x16x32_bf16 v[196:199], v[148:151], a[228:231], v[196:199]\n"
    "ds_read_b128 v[128:131], v190 offset:18432\n"
    "v_mfma_f32_16x16x32_bf16 v[196:199], v[152:155], a[232:235], v[196:199]\n"
    "buffer_load_dwordx4 v[72:75], v108, s[76:79], s17 offen offset:0 sc1\n"
    "v_mfma_f32_16x16x32_bf16 v[196:199], v[156:159], a[236:239], v[196:199]\n"
    "ds_read_b128 v[132:135], v190 offset:22528\n"
    "v_mfma_f32_16x16x32_bf16 v[228:231], v[144:147], a[240:243], v[228:231]\n"
    "buffer_load_dwordx4 v[76:79], v108, s[76:79], s17 offen offset:64 sc1\n"
    "v_mfma_f32_16x16x32_bf16 v[228:231], v[148:151], a[244:247], v[228:231]\n"
    "ds_read_b128 v[136:139], v190 offset:26624\n"
    "v_mfma_f32_16x16x32_bf16 v[228:231], v[152:155], a[248:251], v[228:231]\n"
    "s_add_u32 s70, s17, 4096\n"
    "buffer_load_dwordx4 v[80:83], v108, s[76:79], s70 offen offset:0 sc1\n"
    "v_mfma_f32_16x16x32_bf16 v[228:231], v[156:159], a[252:255], v[228:231]\n"
    "s_waitcnt lgkmcnt(7)\n"
    "ds_read_b128 v[140:143], v190 offset:30720\n"
    "v_mfma_f32_16x16x32_bf16 v[196:199], v[160:163], a[64:67], v[196:199]\n"
    "s_add_u32 s70, s17, 4096\n"
    "buffer_load_dwordx4 v[84:87], v108, s[76:79], s70 offen offset:64 sc1\n"
    "v_mfma_f32_16x16x32_bf16 v[196:199], v[164:167], a[68:71], v[196:199]\n"
    "ds_read_b128 v[144:147], v176 offset:35840\n"
    "v_mfma_f32_16x16x32_bf16 v[196:199], v[168:171], a[72:75], v[196:199]\n"
    "global_load_dwordx2 v[88:89], v109, s[8:9] offset:0\n"
    "v_mfma_f32_16x16x32_bf16 v[196:199], v[172:175], a[76:79], v[196:199]\n"
    "ds_read_b128 v[148:151], v176 offset:39936\n"
    "v_mfma_f32_16x16x32_bf16 v[228:231], v[160:163], a[80:83], v[228:231]\n"
    "global_load_dwordx2 v[90:91], v109, s[8:9] offset:32\n"
    "v_mfma_f32_16x16x32_bf16 v[228:231], v[164:167], a[84:87], v[228:231]\n"
    "ds_read_b128 v[152:155], v176 offset:44032\n"
    "v_mfma_f32_16x16x32_bf16 v[228:231], v[168:171], a[88:91], v[228:231]\n"
    "v_mfma_f32_16x16x32_bf16 v[228:231], v[172:175], a[92:95], v[228:231]\n"
    "s_waitcnt lgkmcnt(7)\n"
    "ds_read_b128 v[156:159], v176 offset:48128\n"
    "v_mfma_f32_16x16x32_bf16 v[200:203], v[112:115], a[224:227], v[200:203]\n"
    "v_mfma_f32_16x16x32_bf16 v[200:203], v[116:119], a[228:231], v[200:203]\n"
    "ds_read_b128 v[160:163], v190 offset:19456\n"
    "v_mfma_f32_16x16x32_bf16 v[200:203], v[120:123], a[232:235], v[200:203]\n"
    "v_mfma_f32_16x16x32_bf16 v[200:203], v[124:127], a[236:239], v[200:203]\n"
    "ds_read_b128 v[164:167], v190 offset:23552\n"
    "v_cvt_pk_bf16_f32 v192, v192, v193\n"
    "v_mfma_f32_16x16x32_bf16 v[232:235], v[112:115], a[240:243], v[232:235]\n"
    "v_mfma_f32_16x16x32_bf16 v[232:235], v[116:119], a[244:247], v[232:235]\n"
    "ds_read_b128 v[168:171], v190 offset:27648\n"
    "v_cvt_pk_bf16_f32 v193, v194, v195\n"
    "v_mfma_f32_16x16x32_bf16 v[232:235], v[120:123], a[248:251], v[232:235]\n"
    "v_mfma_f32_16x16x32_bf16 v[232:235], v[124:127], a[252:255], v[232:235]\n"
    "s_waitcnt lgkmcnt(7)\n"
    "ds_read_b128 v[172:175], v190 offset:31744\n"
    "v_cvt_pk_bf16_f32 v194, v196, v197\n"
    "v_mfma_f32_16x16x32_bf16 v[200:203], v[128:131], a[64:67], v[200:203]\n"
    "v_mfma_f32_16x16x32_bf16 v[200:203], v[132:135], a[68:71], v[200:203]\n"
    "ds_read_b128 v[112:115], v176 offset:49152\n"
    "v_cvt_pk_bf16_f32 v195, v198, v199\n"
    "v_mfma_f32_16x16x32_bf16 v[200:203], v[136:139], a[72:75], v[200:203]\n"
    "s_add_u32 s70, s89, 0x0\n"
    "buffer_store_dwordx4 v[192:195], v190, s[76:79], s70 offen sc1\n"
    "v_mfma_f32_16x16x32_bf16 v[200:203], v[140:143], a[76:79], v[200:203]\n"
    "ds_read_b128 v[116:119], v176 offset:53248\n"
    "v_cvt_pk_bf16_f32 v224, v224, v225\n"
    "v_mfma_f32_16x16x32_bf16 v[232:235], v[128:131], a[80:83], v[232:235]\n"
    "v_mfma_f32_16x16x32_bf16 v[232:235], v[132:135], a[84:87], v[232:235]\n"
    "ds_read_b128 v[120:123], v176 offset:57344\n"
    "v_cvt_pk_bf16_f32 v225, v226, v227\n"
    "v_mfma_f32_16x16x32_bf16 v[232:235], v[136:139], a[88:91], v[232:235]\n"
    "v_mfma_f32_16x16x32_bf16 v[232:235], v[140:143], a[92:95], v[232:235]\n"
    "s_waitcnt lgkmcnt(7)\n"
    "ds_read_b128 v[124:127], v176 offset:61440\n"
    "v_cvt_pk_bf16_f32 v226, v228, v229\n"
    "v_mfma_f32_16x16x32_bf16 v[204:207], v[144:147], a[224:227], v[204:207]\n"
    "v_mfma_f32_16x16x32_bf16 v[204:207], v[148:151], a[228:231], v[204:207]\n"
    "ds_read_b128 v[128:131], v176 offset:16384\n"
    "v_cvt_pk_bf16_f32 v227, v230, v231\n"
    "v_mfma_f32_16x16x32_bf16 v[204:207], v[152:155], a[232:235], v[204:207]\n"
    "v_mfma_f32_16x16x32_bf16 v[204:207], v[156:159], a[236:239], v[204:207]\n"
    "ds_read_b128 v[132:135], v176 offset:20480\n"
    "v_mfma_f32_16x16x32_bf16 v[236:239], v[144:147], a[240:243], v[236:239]\n"
    "v_mfma_f32_16x16x32_bf16 v[236:239], v[148:151], a[244:247], v[236:239]\n"
    "ds_read_b128 v[136:139], v176 offset:24576\n"
    "v_mfma_f32_16x16x32_bf16 v[236:239], v[152:155], a[248:251], v[236:239]\n"
    "s_lshr_b32 s70, s64, 5\n"
    "s_add_u32 s70, s70, s58\n"
    "s_waitcnt vmcnt(7)\n"
    "s_lshl_b32 s5, s70, 5\n"
    "s_add_u32 s5, s5, 0x5800000\n"
    "buffer_store_dword v182, v184, s[76:79], s5 offen sc1\n"
    "v_mfma_f32_16x16x32_bf16 v[236:239], v[156:159], a[252:255], v[236:239]\n"
    "s_waitcnt lgkmcnt(7)\n"
    "ds_read_b128 v[140:143], v176 offset:28672\n"
    "v_mfma_f32_16x16x32_bf16 v[204:207], v[160:163], a[64:67], v[204:207]\n"
    "s_add_u32 s70, s89, 0x80000\n"
    "buffer_store_dwordx4 v[224:227], v190, s[76:79], s70 offen sc1\n"
    "v_mfma_f32_16x16x32_bf16 v[204:207], v[164:167], a[68:71], v[204:207]\n"
    "ds_read_b128 v[144:147], v176 offset:50176\n"
    "v_mfma_f32_16x16x32_bf16 v[204:207], v[168:171], a[72:75], v[204:207]\n"
    "v_mfma_f32_16x16x32_bf16 v[204:207], v[172:175], a[76:79], v[204:207]\n"
    "ds_read_b128 v[148:151], v176 offset:54272\n"
    "v_mfma_f32_16x16x32_bf16 v[236:239], v[160:163], a[80:83], v[236:239]\n"
    "v_mfma_f32_16x16x32_bf16 v[236:239], v[164:167], a[84:87], v[236:239]\n"
    "ds_read_b128 v[152:155], v176 offset:58368\n"
    "v_mfma_f32_16x16x32_bf16 v[236:239], v[168:171], a[88:91], v[236:239]\n"
    "v_mfma_f32_16x16x32_bf16 v[236:239], v[172:175], a[92:95], v[236:239]\n"
    "s_waitcnt lgkmcnt(7)\n"
    "ds_read_b128 v[156:159], v176 offset:62464\n"
    "v_mfma_f32_16x16x32_bf16 v[208:211], v[112:115], a[224:227], v[208:211]\n"
    "v_mfma_f32_16x16x32_bf16 v[208:211], v[116:119], a[228:231], v[208:211]\n"
    "ds_read_b128 v[160:163], v176 offset:17408\n"
    "v_mfma_f32_16x16x32_bf16 v[208:211], v[120:123], a[232:235], v[208:211]\n"
    "v_mfma_f32_16x16x32_bf16 v[208:211], v[124:127], a[236:239], v[208:211]\n"
    "ds_read_b128 v[164:167], v176 offset:21504\n"
    "v_cvt_pk_bf16_f32 v200, v200, v201\n"
    "v_mfma_f32_16x16x32_bf16 v[240:243], v[112:115], a[240:243], v[240:243]\n"
    "s_add_u32 s68, s20, 0\n"
    "buffer_load_dwordx4 a[0:3], v190, s[76:79], s68 offen offset:0 sc1\n"
    "v_mfma_f32_16x16x32_bf16 v[240:243], v[116:119], a[244:247], v[240:243]\n"
    "ds_read_b128 v[168:171], v176 offset:25600\n"
    "v_cvt_pk_bf16_f32 v201, v202, v203\n"
    "v_mfma_f32_16x16x32_bf16 v[240:243], v[120:123], a[248:251], v[240:243]\n"
    "v_mfma_f32_16x16x32_bf16 v[240:243], v[124:127], a[252:255], v[240:243]\n"
    "s_waitcnt lgkmcnt(7)\n"
    "ds_read_b128 v[172:175], v176 offset:29696\n"
    "v_cvt_pk_bf16_f32 v202, v204, v205\n"
    "v_mfma_f32_16x16x32_bf16 v[208:211], v[128:131], a[64:67], v[208:211]\n"
    "buffer_load_dwordx4 a[4:7], v190, s[76:79], s68 offen offset:1024 sc1\n"
    "v_mfma_f32_16x16x32_bf16 v[208:211], v[132:135], a[68:71], v[208:211]\n"
    "ds_read_b128 v[112:115], v176 offset:51200\n"
    "v_cvt_pk_bf16_f32 v203, v206, v207\n"
    "v_mfma_f32_16x16x32_bf16 v[208:211], v[136:139], a[72:75], v[208:211]\n"
    "v_mfma_f32_16x16x32_bf16 v[208:211], v[140:143], a[76:79], v[208:211]\n"
    "ds_read_b128 v[116:119], v176 offset:55296\n"
    "v_cvt_pk_bf16_f32 v232, v232, v233\n"
    "v_mfma_f32_16x16x32_bf16 v[240:243], v[128:131], a[80:83], v[240:243]\n"
    "s_add_u32 s70, s89, 0x2000\n"
    "buffer_store_dwordx4 v[200:203], v190, s[76:79], s70 offen sc1\n"
    "v_mfma_f32_16x16x32_bf16 v[240:243], v[132:135], a[84:87], v[240:243]\n"
    "ds_read_b128 v[120:123], v176 offset:59392\n"
    "v_cvt_pk_bf16_f32 v233, v234, v235\n"
    "v_mfma_f32_16x16x32_bf16 v[240:243], v[136:139], a[88:91], v[240:243]\n"
    "v_mfma_f32_16x16x32_bf16 v[240:243], v[140:143], a[92:95], v[240:243]\n"
    "s_waitcnt lgkmcnt(7)\n"
    "ds_read_b128 v[124:127], v176 offset:63488\n"
    "v_cvt_pk_bf16_f32 v234, v236, v237\n"
    "v_mfma_f32_16x16x32_bf16 v[212:215], v[144:147], a[224:227], v[212:215]\n"
    "v_mfma_f32_16x16x32_bf16 v[212:215], v[148:151], a[228:231], v[212:215]\n"
    "ds_read_b128 v[128:131], v176 offset:18432\n"
    "v_cvt_pk_bf16_f32 v235, v238, v239\n"
    "v_mfma_f32_16x16x32_bf16 v[212:215], v[152:155], a[232:235], v[212:215]\n"
    "v_mfma_f32_16x16x32_bf16 v[212:215], v[156:159], a[236:239], v[212:215]\n"
    "ds_read_b128 v[132:135], v176 offset:22528\n"
    "v_mfma_f32_16x16x32_bf16 v[244:247], v[144:147], a[240:243], v[244:247]\n"
    "v_mfma_f32_16x16x32_bf16 v[244:247], v[148:151], a[244:247], v[244:247]\n"
    "ds_read_b128 v[136:139], v176 offset:26624\n"
    "v_mfma_f32_16x16x32_bf16 v[244:247], v[152:155], a[248:251], v[244:247]\n"
    "v_mfma_f32_16x16x32_bf16 v[244:247], v[156:159], a[252:255], v[244:247]\n"
    "s_waitcnt lgkmcnt(7)\n"
    "ds_read_b128 v[140:143], v176 offset:30720\n"
    "v_mfma_f32_16x16x32_bf16 v[212:215], v[160:163], a[64:67], v[212:215]\n"
    ".p2align 12\n"
    "L_pad11:\n"
    "s_cbranch_execz L_walked\n"
    "s_add_u32 s70, s89, 0x82000\n"
    "buffer_store_dwordx4 v[232:235], v190, s[76:79], s70 offen sc1\n"
    "v_mfma_f32_16x16x32_bf16 v[212:215], v[164:167], a[68:71], v[212:215]\n"
    "ds_read_b128 v[144:147], v176 offset:52224\n"
    "v_mfma_f32_16x16x32_bf16 v[212:215], v[168:171], a[72:75], v[212:215]\n"
    "v_mfma_f32_16x16x32_bf16 v[212:215], v[172:175], a[76:79], v[212:215]\n"
    "ds_read_b128 v[148:151], v176 offset:56320\n"
    "v_mfma_f32_16x16x32_bf16 v[244:247], v[160:163], a[80:83], v[244:247]\n"
    "v_mfma_f32_16x16x32_bf16 v[244:247], v[164:167], a[84:87], v[244:247]\n"
    "ds_read_b128 v[152:155], v176 offset:60416\n"
    "v_mfma_f32_16x16x32_bf16 v[244:247], v[168:171], a[88:91], v[244:247]\n"
    "v_mfma_f32_16x16x32_bf16 v[244:247], v[172:175], a[92:95], v[244:247]\n"
    "s_waitcnt lgkmcnt(7)\n"
    "ds_read_b128 v[156:159], v176 offset:64512\n"
    "v_mfma_f32_16x16x32_bf16 v[216:219], v[112:115], a[224:227], v[216:219]\n"
    "v_mfma_f32_16x16x32_bf16 v[216:219], v[116:119], a[228:231], v[216:219]\n"
    "ds_read_b128 v[160:163], v176 offset:19456\n"
    "v_mfma_f32_16x16x32_bf16 v[216:219], v[120:123], a[232:235], v[216:219]\n"
    "v_mfma_f32_16x16x32_bf16 v[216:219], v[124:127], a[236:239], v[216:219]\n"
    "ds_read_b128 v[164:167], v176 offset:23552\n"
    "v_cvt_pk_bf16_f32 v208, v208, v209\n"
    "v_mfma_f32_16x16x32_bf16 v[248:251], v[112:115], a[240:243], v[248:251]\n"
    "s_add_u32 s68, s20, 4096\n"
    "buffer_load_dwordx4 a[8:11], v190, s[76:79], s68 offen offset:0 sc1\n"
    "v_mfma_f32_16x16x32_bf16 v[248:251], v[116:119], a[244:247], v[248:251]\n"
    "ds_read_b128 v[168:171], v176 offset:27648\n"
    "v_cvt_pk_bf16_f32 v209, v210, v211\n"
    "v_mfma_f32_16x16x32_bf16 v[248:251], v[120:123], a[248:251], v[248:251]\n"
    "v_mfma_f32_16x16x32_bf16 v[248:251], v[124:127], a[252:255], v[248:251]\n"
    "s_waitcnt lgkmcnt(7)\n"
    "ds_read_b128 v[172:175], v176 offset:31744\n"
    "v_cvt_pk_bf16_f32 v210, v212, v213\n"
    "v_mfma_f32_16x16x32_bf16 v[216:219], v[128:131], a[64:67], v[216:219]\n"
    "buffer_load_dwordx4 a[12:15], v190, s[76:79], s68 offen offset:1024 sc1\n"
    "v_mfma_f32_16x16x32_bf16 v[216:219], v[132:135], a[68:71], v[216:219]\n"
    "v_cvt_pk_bf16_f32 v211, v214, v215\n"
    "v_cvt_pk_bf16_f32 v240, v240, v241\n"
    "v_mfma_f32_16x16x32_bf16 v[216:219], v[136:139], a[72:75], v[216:219]\n"
    "v_mfma_f32_16x16x32_bf16 v[216:219], v[140:143], a[76:79], v[216:219]\n"
    "v_cvt_pk_bf16_f32 v241, v242, v243\n"
    "v_cvt_pk_bf16_f32 v242, v244, v245\n"
    "v_mfma_f32_16x16x32_bf16 v[248:251], v[128:131], a[80:83], v[248:251]\n"
    "s_add_u32 s70, s89, 0x4000\n"
    "buffer_store_dwordx4 v[208:211], v190, s[76:79], s70 offen sc1\n"
    "v_mfma_f32_16x16x32_bf16 v[248:251], v[132:135], a[84:87], v[248:251]\n"
    "v_cvt_pk_bf16_f32 v243, v246, v247\n"
    "v_mfma_f32_16x16x32_bf16 v[248:251], v[136:139], a[88:91], v[248:251]\n"
    "v_mfma_f32_16x16x32_bf16 v[248:251], v[140:143], a[92:95], v[248:251]\n"
    "s_waitcnt lgkmcnt(4)\n"
    "v_mfma_f32_16x16x32_bf16 v[220:223], v[144:147], a[224:227], v[220:223]\n"
    "v_mfma_f32_16x16x32_bf16 v[220:223], v[148:151], a[228:231], v[220:223]\n"
    "v_mfma_f32_16x16x32_bf16 v[220:223], v[152:155], a[232:235], v[220:223]\n"
    "v_mfma_f32_16x16x32_bf16 v[220:223], v[156:159], a[236:239], v[220:223]\n"
    "v_mfma_f32_16x16x32_bf16 v[252:255], v[144:147], a[240:243], v[252:255]\n"
    "v_mfma_f32_16x16x32_bf16 v[252:255], v[148:151], a[244:247], v[252:255]\n"
    "v_mfma_f32_16x16x32_bf16 v[252:255], v[152:155], a[248:251], v[252:255]\n"
    "v_mfma_f32_16x16x32_bf16 v[252:255], v[156:159], a[252:255], v[252:255]\n"
    "s_waitcnt lgkmcnt(0)\n"
    "v_mfma_f32_16x16x32_bf16 v[220:223], v[160:163], a[64:67], v[220:223]\n"
    "s_add_u32 s70, s89, 0x84000\n"
    "buffer_store_dwordx4 v[240:243], v190, s[76:79], s70 offen sc1\n"
    "v_mfma_f32_16x16x32_bf16 v[220:223], v[164:167], a[68:71], v[220:223]\n"
    "v_mfma_f32_16x16x32_bf16 v[220:223], v[168:171], a[72:75], v[220:223]\n"
    "v_mfma_f32_16x16x32_bf16 v[220:223], v[172:175], a[76:79], v[220:223]\n"
    "v_mfma_f32_16x16x32_bf16 v[252:255], v[160:163], a[80:83], v[252:255]\n"
    "v_mfma_f32_16x16x32_bf16 v[252:255], v[164:167], a[84:87], v[252:255]\n"
    "v_mfma_f32_16x16x32_bf16 v[252:255], v[168:171], a[88:91], v[252:255]\n"
    "v_mfma_f32_16x16x32_bf16 v[252:255], v[172:175], a[92:95], v[252:255]\n"
    "v_lshrrev_b32 v108, 1, v190\n"
    "v_min_u32 v109, 1, v183\n"
    "v_lshl_or_b32 v108, v109, 30, v108\n"
    "s_barrier\n"
    "s_nop 1\n"
    "v_cvt_pk_bf16_f32 v216, v216, v217\n"
    "v_cvt_pk_bf16_f32 v217, v218, v219\n"
    "v_cvt_pk_bf16_f32 v218, v220, v221\n"
    "v_cvt_pk_bf16_f32 v219, v222, v223\n"
    "s_add_u32 s70, s89, 0x6000\n"
    "buffer_store_dwordx4 v[216:219], v190, s[76:79], s70 offen sc1\n"
    "v_cvt_pk_bf16_f32 v248, v248, v249\n"
    "v_cvt_pk_bf16_f32 v249, v250, v251\n"
    "v_cvt_pk_bf16_f32 v250, v252, v253\n"
    "v_cvt_pk_bf16_f32 v251, v254, v255\n"
    "s_add_u32 s70, s89, 0x86000\n"
    "buffer_store_dwordx4 v[248:251], v190, s[76:79], s70 offen sc1\n"
    "s_add_u32 m0, s57, 131072\n"
    "s_mov_b32 s69, 0x14c00\n"
    "buffer_load_dwordx4 v190, s[76:79], s69 offen offset:0 sc1 lds\n"
    "buffer_load_dwordx4 v190, s[76:79], s69 offen offset:1024 sc1 lds\n"
    "s_lshr_b32 s69, s91, 3\n"
    "v_add_u32 v97, s69, v190\n"
    "v_xor_b32 v98, 2048, v97\n"
    "s_add_u32 s68, s20, 229376\n"
    "buffer_load_dwordx4 a[16:19], v190, s[76:79], s68 offen offset:0 sc1\n"
    "buffer_load_dwordx4 a[20:23], v190, s[76:79], s68 offen offset:1024 sc1\n"
    "s_add_u32 s68, s20, 233472\n"
    "buffer_load_dwordx4 a[24:27], v190, s[76:79], s68 offen offset:0 sc1\n"
    "buffer_load_dwordx4 a[28:31], v190, s[76:79], s68 offen offset:1024 sc1\n"
    "s_add_u32 s68, s20, 458752\n"
    "buffer_load_dwordx4 a[32:35], v190, s[76:79], s68 offen offset:0 sc1\n"
    "buffer_load_dwordx4 a[36:39], v190, s[76:79], s68 offen offset:1024 sc1\n"
    "s_add_u32 s68, s20, 462848\n"
    "buffer_load_dwordx4 a[40:43], v190, s[76:79], s68 offen offset:0 sc1\n"
    "buffer_load_dwordx4 a[44:47], v190, s[76:79], s68 offen offset:1024 sc1\n"
    "s_lshl_b32 s4, s80, 5\n"
    "s_add_u32 s4, s4, 0x5800000\n"
    "buffer_load_dword v110, v185, s[76:79], s4 offen sc1\n"
    "s_add_u32 s68, s20, 688128\n"
    "buffer_load_dwordx4 a[48:51], v190, s[76:79], s68 offen offset:0 sc1\n"
    "buffer_load_dwordx4 a[52:55], v190, s[76:79], s68 offen offset:1024 sc1\n"
    "s_add_u32 s68, s20, 692224\n"
    "buffer_load_dwordx4 a[56:59], v190, s[76:79], s68 offen offset:0 sc1\n"
    "buffer_load_dwordx4 a[60:63], v190, s[76:79], s68 offen offset:1024 sc1\n"
    "s_lshr_b32 s70, s66, 5\n"
    "s_add_u32 s70, s70, s58\n"
    "s_waitcnt vmcnt(15)\n"
    "s_lshl_b32 s5, s70, 5\n"
    "s_add_u32 s5, s5, 0x5800000\n"
    "buffer_store_dword v182, v184, s[76:79], s5 offen sc1\n"
    "s_waitcnt vmcnt(5)\n"
    "v_cmp_ne_u32 vcc, 1, v110\n"
    "s_cbranch_vccz L_pa_ok\n"
    "s_mov_b32 s14, 0x40000\n"
    "L_pa_spin:\n"
    "buffer_load_dword v110, v185, s[76:79], s4 offen sc1\n"
    "s_sub_u32 s14, s14, 1\n"
    "s_waitcnt vmcnt(0)\n"
    "s_cbranch_scc1 L_pa_ok\n"
    "v_cmp_ne_u32 vcc, 1, v110\n"
    "s_cbranch_vccz L_pa_ok\n"
    "s_sleep 3\n"
    "s_branch L_pa_spin\n"
    "L_pa_ok:\n"
    "s_lshl_b32 s69, s81, 13\n"
    "s_add_u32 s69, s69, 0x4800000\n"
    "buffer_load_dwordx4 v[0:3], v190, s[76:79], s69 offen offset:0 sc1\n"
    "buffer_load_dwordx4 v[4:7], v190, s[76:79], s69 offen offset:1024 sc1\n"
    "buffer_load_dwordx4 v[8:11], v190, s[76:79], s69 offen offset:2048 sc1\n"
    "buffer_load_dwordx4 v[12:15], v190, s[76:79], s69 offen offset:3072 sc1\n"
    "s_lshl_b32 s69, s81, 13\n"
    "s_add_u32 s69, s69, 0x4801000\n"
    "buffer_load_dwordx4 v[16:19], v190, s[76:79], s69 offen offset:0 sc1\n"
    "buffer_load_dwordx4 v[20:23], v190, s[76:79], s69 offen offset:1024 sc1\n"
    "buffer_load_dwordx4 v[24:27], v190, s[76:79], s69 offen offset:2048 sc1\n"
    "buffer_load_dwordx4 v[28:31], v190, s[76:79], s69 offen offset:3072 sc1\n"
    "s_andn2_b32 s70, s73, 1\n"
    "s_mul_i32 s70, s70, 229376\n"
    "s_lshl_b32 s69, s29, 10\n"
    "s_add_u32 s28, s69, s70\n"
    "s_add_u32 s28, s28, 0x3600000\n"
    "s_lshr_b32 s69, s29, 3\n"
    "s_add_u32 s69, s69, 1\n"
    "s_and_b32 s69, s69, 8\n"
    "s_lshl_b32 s69, s69, 27\n"
    "s_or_b32 s28, s28, s69\n"
    "s_mul_i32 s70, s73, 229376\n"
    "s_add_u32 s70, s70, 0x3600000\n"
    "s_mul_i32 s69, s71, 14336\n"
    "s_add_u32 s70, s70, s69\n"
    "s_lshr_b32 s69, s71, 1\n"
    "s_mul_i32 s69, s69, 57344\n"
    "s_add_u32 s24, s70, s69\n"
    "s_sub_u32 s25, s70, s69\n"
    "s_add_u32 s25, s25, 57344\n"
    "s_lshr_b32 s70, s73, 1\n"
    "s_lshl_b32 s70, s70, 8\n"
    "s_add_u32 s70, s70, 0xa000\n"
    "s_lshl2_add_u32 s56, s29, s70\n"
    "s_mul_i32 s69, s71, 56\n"
    "s_add_u32 s26, s70, s69\n"
    "s_lshl_b32 s52, s82, 6\n"
    "s_add_u32 s52, s52, 0x8000\n"
    "v_bfe_u32 v181, v190, 2, 6\n"
    "v_lshlrev_b32 v181, 5, v181\n"
    "v_add_u32 v104, s57, v190\n"
    "v_xor_b32 v105, 4096, v104\n"
    "v_xor_b32 v106, 8192, v104\n"
    "v_xor_b32 v107, 12288, v104\n"
    "v_lshrrev_b32 v178, 2, v190\n"
    "v_min_u32 v178, 52, v178\n"
    "v_lshlrev_b32 v178, 5, v178\n"
    "v_lshrrev_b32 v177, 1, v190\n"
    "v_and_b32 v177, 0x78, v177\n"
    "v_lshl_or_b32 v179, v183, 9, v177\n"
    "v_add_u32 v177, 0x20800, v177\n"
    "v_bfe_u32 v109, v190, 4, 1\n"
    "v_xor_b32 v109, 1, v109\n"
    "v_lshlrev_b32 v109, 10, v109\n"
    "v_lshl_or_b32 v109, v183, 3, v109\n"
    "s_add_u32 s69, s57, 131072\n"
    "v_add_u32 v109, s69, v109\n"
    "ds_read2_b64 v[112:115], v109 offset0:0 offset1:4\n"
    "ds_read2_b64 v[116:119], v109 offset0:8 offset1:12\n"
    "ds_read2_b64 v[120:123], v109 offset0:16 offset1:20\n"
    "ds_read2_b64 v[124:127], v109 offset0:24 offset1:28\n"
    "ds_read2_b64 v[128:131], v109 offset0:32 offset1:36\n"
    "ds_read2_b64 v[132:135], v109 offset0:40 offset1:44\n"
    "ds_read2_b64 v[136:139], v109 offset0:48 offset1:52\n"
    "ds_read2_b64 v[140:143], v109 offset0:56 offset1:60\n"
    "ds_read2_b64 v[144:147], v109 offset0:64 offset1:68\n"
    "ds_read2_b64 v[148:151], v109 offset0:72 offset1:76\n"
    "ds_read2_b64 v[152:155], v109 offset0:80 offset1:84\n"
    "ds_read2_b64 v[156:159], v109 offset0:88 offset1:92\n"
    "ds_read2_b64 v[160:163], v109 offset0:96 offset1:100\n"
    "ds_read2_b64 v[164:167], v109 offset0:104 offset1:108\n"
    "ds_read2_b64 v[168:171], v109 offset0:112 offset1:116\n"
    "ds_read2_b64 v[172:175], v109 offset0:120 offset1:124\n"
    "s_add_u32 s80, s80, 0x2000\n"
    "v_permlane16_swap_b32 v64, v66\n"
    "v_permlane16_swap_b32 v65, v67\n"
    "v_lshlrev_b32 v224, 16, v64\n"
    "v_and_b32 v225, 0xffff0000, v64\n"
    "v_lshlrev_b32 v226, 16, v65\n"
    "v_and_b32 v227, 0xffff0000, v65\n"
    "v_lshlrev_b32 v228, 16, v66\n"
    "v_and_b32 v229, 0xffff0000, v66\n"
    "v_lshlrev_b32 v230, 16, v67\n"
    "v_and_b32 v231, 0xffff0000, v67\n"
    "s_waitcnt vmcnt(7)\n"
    "v_lshlrev_b32 v32, 16, v0\n"
    "v_and_b32 v33, 0xffff0000, v0\n"
    "v_lshlrev_b32 v34, 16, v1\n"
    "s_add_u32 s68, s20, 917504\n"
    "buffer_load_dwordx4 a[128:131], v190, s[76:79], s68 offen offset:0 sc1\n"
    "v_and_b32 v35, 0xffff0000, v1\n"
    "v_lshlrev_b32 v36, 16, v2\n"
    "v_and_b32 v37, 0xffff0000, v2\n"
    "v_lshlrev_b32 v38, 16, v3\n"
    "v_and_b32 v39, 0xffff0000, v3\n"
    "s_waitcnt vmcnt(7)\n"
    "v_lshlrev_b32 v40, 16, v4\n"
    "v_and_b32 v41, 0xffff0000, v4\n"
    "v_lshlrev_b32 v42, 16, v5\n"
    "v_and_b32 v43, 0xffff0000, v5\n"
    "v_lshlrev_b32 v44, 16, v6\n"
    "v_and_b32 v45, 0xffff0000, v6\n"
    "v_lshlrev_b32 v46, 16, v7\n"
    "v_and_b32 v47, 0xffff0000, v7\n"
    "v_pk_add_f32 v[32:33], v[32:33], v[40:41]\n"
    "v_pk_add_f32 v[34:35], v[34:35], v[42:43]\n"
    "buffer_load_dwordx4 a[132:135], v190, s[76:79], s68 offen offset:1024 sc1\n"
    "v_pk_add_f32 v[36:37], v[36:37], v[44:45]\n"
    "v_pk_add_f32 v[38:39], v[38:39], v[46:47]\n"
    "s_waitcnt vmcnt(7)\n"
    "v_lshlrev_b32 v40, 16, v8\n"
    "v_and_b32 v41, 0xffff0000, v8\n"
    "v_lshlrev_b32 v42, 16, v9\n"
    "v_and_b32 v43, 0xffff0000, v9\n"
    "v_lshlrev_b32 v44, 16, v10\n"
    "v_and_b32 v45, 0xffff0000, v10\n"
    "v_lshlrev_b32 v46, 16, v11\n"
    "v_and_b32 v47, 0xffff0000, v11\n"
    "v_pk_add_f32 v[32:33], v[32:33], v[40:41]\n"
    "v_pk_add_f32 v[34:35], v[34:35], v[42:43]\n"
    "v_pk_add_f32 v[36:37], v[36:37], v[44:45]\n"
    "v_pk_add_f32 v[38:39], v[38:39], v[46:47]\n"
    "s_waitcnt vmcnt(6)\n"
    "s_add_u32 s68, s20, 921600\n"
    "buffer_load_dwordx4 a[136:139], v190, s[76:79], s68 offen offset:0 sc1\n"
    "v_lshlrev_b32 v40, 16, v12\n"
    "v_and_b32 v41, 0xffff0000, v12\n"
    "v_lshlrev_b32 v42, 16, v13\n"
    "v_and_b32 v43, 0xffff0000, v13\n"
    "v_lshlrev_b32 v44, 16, v14\n"
    "v_and_b32 v45, 0xffff0000, v14\n"
    "v_lshlrev_b32 v46, 16, v15\n"
    "v_and_b32 v47, 0xffff0000, v15\n"
    "v_pk_add_f32 v[32:33], v[32:33], v[40:41]\n"
    "v_pk_add_f32 v[34:35], v[34:35], v[42:43]\n"
    "v_pk_add_f32 v[36:37], v[36:37], v[44:45]\n"
    "v_pk_add_f32 v[38:39], v[38:39], v[46:47]\n"
    "s_waitcnt vmcnt(6)\n"
    "v_lshlrev_b32 v40, 16, v16\n"
    "v_and_b32 v41, 0xffff0000, v16\n"
    "v_lshlrev_b32 v42, 16, v17\n"
    "buffer_load_dwordx4 a[140:143], v190, s[76:79], s68 offen offset:1024 sc1\n"
    "v_and_b32 v43, 0xffff0000, v17\n"
    "v_lshlrev_b32 v44, 16, v18\n"
    "v_and_b32 v45, 0xffff0000, v18\n"
    "v_lshlrev_b32 v46, 16, v19\n"
    "v_and_b32 v47, 0xffff0000, v19\n"
    "v_pk_add_f32 v[32:33], v[32:33], v[40:41]\n"
    "v_pk_add_f32 v[34:35], v[34:35], v[42:43]\n"
    "v_pk_add_f32 v[36:37], v[36:37], v[44:45]\n"
    "v_pk_add_f32 v[38:39], v[38:39], v[46:47]\n"
    "s_waitcnt vmcnt(6)\n"
    "v_lshlrev_b32 v40, 16, v20\n"
    "v_and_b32 v41, 0xffff0000, v20\n"
    "v_lshlrev_b32 v42, 16, v21\n"
    "v_and_b32 v43, 0xffff0000, v21\n"
    "v_lshlrev_b32 v44, 16, v22\n"
    "v_and_b32 v45, 0xffff0000, v22\n"
    "s_add_u32 s68, s20, 1146880\n"
    "buffer_load_dwordx4 a[144:147], v190, s[76:79], s68 offen offset:0 sc1\n"
    "v_lshlrev_b32 v46, 16, v23\n"
    "v_and_b32 v47, 0xffff0000, v23\n"
    "v_pk_add_f32 v[32:33], v[32:33], v[40:41]\n"
    "v_pk_add_f32 v[34:35], v[34:35], v[42:43]\n"
    "v_pk_add_f32 v[36:37], v[36:37], v[44:45]\n"
    "v_pk_add_f32 v[38:39], v[38:39], v[46:47]\n"
    "s_waitcnt vmcnt(6)\n"
    "v_lshlrev_b32 v40, 16, v24\n"
    "v_and_b32 v41, 0xffff0000, v24\n"
    "v_lshlrev_b32 v42, 16, v25\n"
    "v_and_b32 v43, 0xffff0000, v25\n"
    "v_lshlrev_b32 v44, 16, v26\n"
    "v_and_b32 v45, 0xffff0000, v26\n"
    "v_lshlrev_b32 v46, 16, v27\n"
    "v_and_b32 v47, 0xffff0000, v27\n"
    "v_pk_add_f32 v[32:33], v[32:33], v[40:41]\n"
    "buffer_load_dwordx4 a[148:151], v190, s[76:79], s68 offen offset:1024 sc1\n"
    "v_pk_add_f32 v[34:35], v[34:35], v[42:43]\n"
    "v_pk_add_f32 v[36:37], v[36:37], v[44:45]\n"
    "v_pk_add_f32 v[38:39], v[38:39], v[46:47]\n"
    "s_waitcnt vmcnt(6)\n"
    "v_lshlrev_b32 v40, 16, v28\n"
    "v_and_b32 v41, 0xffff0000, v28\n"
    "v_lshlrev_b32 v42, 16, v29\n"
    "v_and_b32 v43, 0xffff0000, v29\n"
    "v_lshlrev_b32 v44, 16, v30\n"
    "v_and_b32 v45, 0xffff0000, v30\n"
    "v_lshlrev_b32 v46, 16, v31\n"
    "v_and_b32 v47, 0xffff0000, v31\n"
    "v_pk_add_f32 v[32:33], v[32:33], v[40:41]\n"
    "v_pk_add_f32 v[34:35], v[34:35], v[42:43]\n"
    "v_pk_add_f32 v[36:37], v[36:37], v[44:45]\n"
    "v_pk_add_f32 v[38:39], v[38:39], v[46:47]\n"
    "s_add_u32 s68, s20, 1150976\n"
    "buffer_load_dwordx4 a[152:155], v190, s[76:79], s68 offen offset:0 sc1\n"
    "v_pk_fma_f32 v[32:33], v[32:33], v[72:73], v[224:225]\n"
    "v_pk_fma_f32 v[34:35], v[34:35], v[74:75], v[226:227]\n"
    "v_pk_fma_f32 v[36:37], v[36:37], v[76:77], v[228:229]\n"
    "v_pk_fma_f32 v[38:39], v[38:39], v[78:79], v[230:231]\n"
    "v_cvt_pk_bf16_f32 v40, v32, v33\n"
    "v_cvt_pk_bf16_f32 v41, v34, v35\n"
    "v_cvt_pk_bf16_f32 v42, v36, v37\n"
    "v_cvt_pk_bf16_f32 v43, v38, v39\n"
    "v_lshlrev_b32 v224, 16, v40\n"
    "v_and_b32 v225, 0xffff0000, v40\n"
    "v_lshlrev_b32 v226, 16, v41\n"
    "v_and_b32 v227, 0xffff0000, v41\n"
    "v_lshlrev_b32 v228, 16, v42\n"
    "v_and_b32 v229, 0xffff0000, v42\n"
    "v_lshlrev_b32 v230, 16, v43\n"
    "buffer_load_dwordx4 a[156:159], v190, s[76:79], s68 offen offset:1024 sc1\n"
    "v_and_b32 v231, 0xffff0000, v43\n"
    "v_pk_mul_f32 v[32:33], v[224:225], v[80:81]\n"
    "v_pk_mul_f32 v[34:35], v[226:227], v[82:83]\n"
    "v_pk_mul_f32 v[36:37], v[228:229], v[84:85]\n"
    "v_pk_mul_f32 v[38:39], v[230:231], v[86:87]\n"
    "v_cvt_pk_bf16_f32 v44, v32, v33\n"
    "v_cvt_pk_bf16_f32 v45, v34, v35\n"
    "v_cvt_pk_bf16_f32 v46, v36, v37\n"
    "v_cvt_pk_bf16_f32 v47, v38, v39\n"
    "s_lshl_b32 s69, s81, 10\n"
    "s_add_u32 s69, s69, 0x3400000\n"
    "buffer_store_dwordx4 v[44:47], v190, s[76:79], s69 offen sc1\n"
    "v_pk_add_f32 v[40:41], v[224:225], v[226:227]\n"
    "v_pk_add_f32 v[42:43], v[228:229], v[230:231]\n"
    "v_pk_add_f32 v[40:41], v[40:41], v[42:43]\n"
    "v_add_f32 v40, v40, v41\n"
    "s_add_u32 s68, s20, 1376256\n"
    "buffer_load_dwordx4 a[160:163], v190, s[76:79], s68 offen offset:0 sc1\n"
    "v_mov_b32 v42, v40\n"
    "s_nop 1\n"
    "v_permlane32_swap_b32 v40, v42\n"
    "v_add_f32 v40, v40, v42\n"
    "v_mov_b32 v42, v40\n"
    "s_nop 1\n"
    "v_permlane16_swap_b32 v40, v42\n"
    "v_add_f32 v40, v40, v42\n"
    "v_mul_f32 v40, 0x3d000000, v40\n"
    "v_pk_add_f32 v[32:33], v[224:225], v[40:41] op_sel_hi:[1,0] neg_lo:[0,1] neg_hi:[0,1]\n"
    "v_pk_add_f32 v[34:35], v[226:227], v[40:41] op_sel_hi:[1,0] neg_lo:[0,1] neg_hi:[0,1]\n"
    "v_pk_add_f32 v[36:37], v[228:229], v[40:41] op_sel_hi:[1,0] neg_lo:[0,1] neg_hi:[0,1]\n"
    "v_pk_add_f32 v[38:39], v[230:231], v[40:41] op_sel_hi:[1,0] neg_lo:[0,1] neg_hi:[0,1]\n"
    "v_pk_mul_f32 v[42:43], v[32:33], v[32:33]\n"
    "v_pk_fma_f32 v[42:43], v[34:35], v[34:35], v[42:43]\n"
    "v_pk_fma_f32 v[42:43], v[36:37], v[36:37], v[42:43]\n"
    "buffer_load_dwordx4 a[164:167], v190, s[76:79], s68 offen offset:1024 sc1\n"
    "v_pk_fma_f32 v[42:43], v[38:39], v[38:39], v[42:43]\n"
    "v_add_f32 v41, v42, v43\n"
    "v_mov_b32 v42, v41\n"
    "s_nop 1\n"
    "v_permlane32_swap_b32 v41, v42\n"
    "v_add_f32 v41, v41, v42\n"
    "v_mov_b32 v42, v41\n"
    "s_nop 1\n"
    "v_permlane16_swap_b32 v41, v42\n"
    "v_add_f32 v41, v41, v42\n"
    "s_lshl_b32 s69, s81, 7\n"
    "s_add_u32 s69, s69, 0x20000\n"
    "buffer_store_dwordx2 v[40:41], v108, s[76:79], s69 offen sc1\n"
    "s_lshl_b32 s4, s80, 5\n"
    "s_add_u32 s4, s4, 0x5800000\n"
    "buffer_load_dword v110, v185, s[76:79], s4 offen sc1\n"
    "s_add_u32 s68, s20, 1380352\n"
    "buffer_load_dwordx4 a[168:171], v190, s[76:79], s68 offen offset:0 sc1\n"
    "buffer_load_dwordx4 a[172:175], v190, s[76:79], s68 offen offset:1024 sc1\n"
    "s_add_u32 s68, s20, 1605632\n"
    "buffer_load_dwordx4 a[176:179], v190, s[76:79], s68 offen offset:0 sc1\n"
    "buffer_load_dwordx4 a[180:183], v190, s[76:79], s68 offen offset:1024 sc1\n"
    "s_add_u32 s68, s20, 1609728\n"
    "buffer_load_dwordx4 a[184:187], v190, s[76:79], s68 offen offset:0 sc1\n"
    "buffer_load_dwordx4 a[188:191], v190, s[76:79], s68 offen offset:1024 sc1\n"
    "s_add_u32 s68, s83, 0\n"
    "buffer_load_dwordx4 a[64:67], v97, s[76:79], s68 offen offset:0 sc1\n"
    "buffer_load_dwordx4 a[68:71], v97, s[76:79], s68 offen offset:1024 sc1\n"
    "s_lshl_b32 s69, s81, 2\n"
    "s_add_u32 s69, s69, 0x8000\n"
    "s_waitcnt vmcnt(9)\n"
    "s_lshl_b32 s5, s69, 5\n"
    "s_add_u32 s5, s5, 0x5800000\n"
    "buffer_store_dword v182, v180, s[76:79], s5 offen sc1\n"
    "s_waitcnt vmcnt(9)\n"
    "v_cmp_ne_u32 vcc, 1, v110\n"
    "s_cbranch_vccz L_pb_ok\n"
    "s_mov_b32 s14, 0x40000\n"
    "L_pb_spin:\n"
    "buffer_load_dword v110, v185, s[76:79], s4 offen sc1\n"
    "s_sub_u32 s14, s14, 1\n"
    "s_waitcnt vmcnt(0)\n"
    "s_cbranch_scc1 L_pb_ok\n"
    "v_cmp_ne_u32 vcc, 1, v110\n"
    "s_cbranch_vccz L_pb_ok\n"
    "s_sleep 3\n"
    "s_branch L_pb_spin\n"
    "L_pb_ok:\n"
    "s_lshl_b32 s69, s81, 13\n"
    "s_add_u32 s69, s69, 0x5000000\n"
    "buffer_load_dwordx4 v[0:3], v190, s[76:79], s69 offen offset:0 sc1\n"
    "buffer_load_dwordx4 v[4:7], v190, s[76:79], s69 offen offset:1024 sc1\n"
    "buffer_load_dwordx4 v[8:11], v190, s[76:79], s69 offen offset:2048 sc1\n"
    "buffer_load_dwordx4 v[12:15], v190, s[76:79], s69 offen offset:3072 sc1\n"
    "s_lshl_b32 s69, s81, 13\n"
    "s_add_u32 s69, s69, 0x5001000\n"
    "buffer_load_dwordx4 v[16:19], v190, s[76:79], s69 offen offset:0 sc1\n"
    "buffer_load_dwordx4 v[20:23], v190, s[76:79], s69 offen offset:1024 sc1\n"
    "buffer_load_dwordx4 v[24:27], v190, s[76:79], s69 offen offset:2048 sc1\n"
    "buffer_load_dwordx4 v[28:31], v190, s[76:79], s69 offen offset:3072 sc1\n"
    "s_waitcnt vmcnt(11) lgkmcnt(0)\n"
    "v_mfma_f32_16x16x32_bf16 v[48:51], a[0:3], v[112:115], 0\n"
    "v_mfma_f32_16x16x32_bf16 v[52:55], a[4:7], v[112:115], 0\n"
    "v_mfma_f32_16x16x32_bf16 v[48:51], a[8:11], v[116:119], v[48:51]\n"
    "v_mfma_f32_16x16x32_bf16 v[52:55], a[12:15], v[116:119], v[52:55]\n"
    "v_mfma_f32_16x16x32_bf16 v[48:51], a[16:19], v[120:123], v[48:51]\n"
    "v_mfma_f32_16x16x32_bf16 v[52:55], a[20:23], v[120:123], v[52:55]\n"
    "v_mfma_f32_16x16x32_bf16 v[48:51], a[24:27], v[124:127], v[48:51]\n"
    "v_mfma_f32_16x16x32_bf16 v[52:55], a[28:31], v[124:127], v[52:55]\n"
    "v_mfma_f32_16x16x32_bf16 v[48:51], a[32:35], v[128:131], v[48:51]\n"
    "v_mfma_f32_16x16x32_bf16 v[52:55], a[36:39], v[128:131], v[52:55]\n"
    "v_mfma_f32_16x16x32_bf16 v[48:51], a[40:43], v[132:135], v[48:51]\n"
    "v_mfma_f32_16x16x32_bf16 v[52:55], a[44:47], v[132:135], v[52:55]\n"
    "v_mfma_f32_16x16x32_bf16 v[48:51], a[48:51], v[136:139], v[48:51]\n"
    "v_mfma_f32_16x16x32_bf16 v[52:55], a[52:55], v[136:139], v[52:55]\n"
    "v_mfma_f32_16x16x32_bf16 v[48:51], a[56:59], v[140:143], v[48:51]\n"
    "v_mfma_f32_16x16x32_bf16 v[52:55], a[60:63], v[140:143], v[52:55]\n"
    "v_mfma_f32_16x16x32_bf16 v[48:51], a[128:131], v[144:147], v[48:51]\n"
    "v_mfma_f32_16x16x32_bf16 v[52:55], a[132:135], v[144:147], v[52:55]\n"
    "v_mfma_f32_16x16x32_bf16 v[48:51], a[136:139], v[148:151], v[48:51]\n"
    "v_mfma_f32_16x16x32_bf16 v[52:55], a[140:143], v[148:151], v[52:55]\n"
    "v_mfma_f32_16x16x32_bf16 v[48:51], a[144:147], v[152:155], v[48:51]\n"
    "v_mfma_f32_16x16x32_bf16 v[52:55], a[148:151], v[152:155], v[52:55]\n"
    "v_mfma_f32_16x16x32_bf16 v[48:51], a[152:155], v[156:159], v[48:51]\n"
    "v_mfma_f32_16x16x32_bf16 v[52:55], a[156:159], v[156:159], v[52:55]\n"
    "v_mfma_f32_16x16x32_bf16 v[48:51], a[160:163], v[160:163], v[48:51]\n"
    "v_mfma_f32_16x16x32_bf16 v[52:55], a[164:167], v[160:163], v[52:55]\n"
    "v_mfma_f32_16x16x32_bf16 v[48:51], a[168:171], v[164:167], v[48:51]\n"
    "v_mfma_f32_16x16x32_bf16 v[52:55], a[172:175], v[164:167], v[52:55]\n"
    "v_mfma_f32_16x16x32_bf16 v[48:51], a[176:179], v[168:171], v[48:51]\n"
    "v_mfma_f32_16x16x32_bf16 v[52:55], a[180:183], v[168:171], v[52:55]\n"
    "v_mfma_f32_16x16x32_bf16 v[48:51], a[184:187], v[172:175], v[48:51]\n"
    "v_mfma_f32_16x16x32_bf16 v[52:55], a[188:191], v[172:175], v[52:55]\n"
    "v_lshlrev_b32 v200, 16, v88\n"
    "v_and_b32 v201, 0xffff0000, v88\n"
    "v_lshlrev_b32 v202, 16, v89\n"
    "v_and_b32 v203, 0xffff0000, v89\n"
    "v_lshlrev_b32 v204, 16, v90\n"
    "v_and_b32 v205, 0xffff0000, v90\n"
    "v_lshlrev_b32 v206, 16, v91\n"
    "v_and_b32 v207, 0xffff0000, v91\n"
    "v_mov_b32_dpp v192, v48 quad_perm:[0,0,2,2] row_mask:0xf bank_mask:0xf\n"
    "v_add_f32_dpp v200, v48, v200 quad_perm:[1,1,3,3] row_mask:0xf bank_mask:0xf\n"
    "v_mov_b32_dpp v193, v49 quad_perm:[0,0,2,2] row_mask:0xf bank_mask:0xf\n"
    "v_add_f32_dpp v201, v49, v201 quad_perm:[1,1,3,3] row_mask:0xf bank_mask:0xf\n"
    "v_mov_b32_dpp v194, v50 quad_perm:[0,0,2,2] row_mask:0xf bank_mask:0xf\n"
    "v_add_f32_dpp v202, v50, v202 quad_perm:[1,1,3,3] row_mask:0xf bank_mask:0xf\n"
    "v_mov_b32_dpp v195, v51 quad_perm:[0,0,2,2] row_mask:0xf bank_mask:0xf\n"
    "v_add_f32_dpp v203, v51, v203 quad_perm:[1,1,3,3] row_mask:0xf bank_mask:0xf\n"
    "v_mov_b32_dpp v196, v52 quad_perm:[0,0,2,2] row_mask:0xf bank_mask:0xf\n"
    "v_add_f32_dpp v204, v52, v204 quad_perm:[1,1,3,3] row_mask:0xf bank_mask:0xf\n"
    "v_mov_b32_dpp v197, v53 quad_perm:[0,0,2,2] row_mask:0xf bank_mask:0xf\n"
    "v_add_f32_dpp v205, v53, v205 quad_perm:[1,1,3,3] row_mask:0xf bank_mask:0xf\n"
    "v_mov_b32_dpp v198, v54 quad_perm:[0,0,2,2] row_mask:0xf bank_mask:0xf\n"
    "v_add_f32_dpp v206, v54, v206 quad_perm:[1,1,3,3] row_mask:0xf bank_mask:0xf\n"
    "v_mov_b32_dpp v199, v55 quad_perm:[0,0,2,2] row_mask:0xf bank_mask:0xf\n"
    "v_add_f32_dpp v207, v55, v207 quad_perm:[1,1,3,3] row_mask:0xf bank_mask:0xf\n"
    "v_mul_f32 v200, 0x3fb8aa3b, v200\n"
    "v_mul_f32 v201, 0x3fb8aa3b, v201\n"
    "v_mul_f32 v202, 0x3fb8aa3b, v202\n"
    "v_mul_f32 v203, 0x3fb8aa3b, v203\n"
    "v_mul_f32 v204, 0x3fb8aa3b, v204\n"
    "v_mul_f32 v205, 0x3fb8aa3b, v205\n"
    "v_mul_f32 v206, 0x3fb8aa3b, v206\n"
    "v_mul_f32 v207, 0x3fb8aa3b, v207\n"
    "v_permlane16_swap_b32 v68, v70\n"
    "v_permlane16_swap_b32 v69, v71\n"
    "v_lshlrev_b32 v232, 16, v68\n"
    "v_and_b32 v233, 0xffff0000, v68\n"
    "v_lshlrev_b32 v234, 16, v69\n"
    "v_and_b32 v235, 0xffff0000, v69\n"
    "v_lshlrev_b32 v236, 16, v70\n"
    "v_and_b32 v237, 0xffff0000, v70\n"
    "v_lshlrev_b32 v238, 16, v71\n"
    "v_and_b32 v239, 0xffff0000, v71\n"
    "s_waitcnt vmcnt(7)\n"
    "v_lshlrev_b32 v32, 16, v0\n"
    "v_and_b32 v33, 0xffff0000, v0\n"
    "v_lshlrev_b32 v34, 16, v1\n"
    "v_and_b32 v35, 0xffff0000, v1\n"
    "v_lshlrev_b32 v36, 16, v2\n"
    "v_and_b32 v37, 0xffff0000, v2\n"
    "v_lshlrev_b32 v38, 16, v3\n"
    "v_and_b32 v39, 0xffff0000, v3\n"
    "s_waitcnt vmcnt(6)\n"
    "v_lshlrev_b32 v40, 16, v4\n"
    "v_and_b32 v41, 0xffff0000, v4\n"
    "v_lshlrev_b32 v42, 16, v5\n"
    "v_and_b32 v43, 0xffff0000, v5\n"
    "v_lshlrev_b32 v44, 16, v6\n"
    "v_and_b32 v45, 0xffff0000, v6\n"
    "v_lshlrev_b32 v46, 16, v7\n"
    "v_and_b32 v47, 0xffff0000, v7\n"
    "v_pk_add_f32 v[32:33], v[32:33], v[40:41]\n"
    "v_pk_add_f32 v[34:35], v[34:35], v[42:43]\n"
    "v_pk_add_f32 v[36:37], v[36:37], v[44:45]\n"
    "v_pk_add_f32 v[38:39], v[38:39], v[46:47]\n"
    "s_waitcnt vmcnt(5)\n"
    "v_lshlrev_b32 v40, 16, v8\n"
    "v_and_b32 v41, 0xffff0000, v8\n"
    "v_lshlrev_b32 v42, 16, v9\n"
    "v_and_b32 v43, 0xffff0000, v9\n"
    "v_lshlrev_b32 v44, 16, v10\n"
    "v_and_b32 v45, 0xffff0000, v10\n"
    "v_lshlrev_b32 v46, 16, v11\n"
    "v_and_b32 v47, 0xffff0000, v11\n"
    "v_pk_add_f32 v[32:33], v[32:33], v[40:41]\n"
    "v_pk_add_f32 v[34:35], v[34:35], v[42:43]\n"
    "v_pk_add_f32 v[36:37], v[36:37], v[44:45]\n"
    "v_pk_add_f32 v[38:39], v[38:39], v[46:47]\n"
    "s_waitcnt vmcnt(4)\n"
    "v_lshlrev_b32 v40, 16, v12\n"
    "v_and_b32 v41, 0xffff0000, v12\n"
    "v_lshlrev_b32 v42, 16, v13\n"
    "v_and_b32 v43, 0xffff0000, v13\n"
    "v_lshlrev_b32 v44, 16, v14\n"
    "v_and_b32 v45, 0xffff0000, v14\n"
    "v_lshlrev_b32 v46, 16, v15\n"
    "v_and_b32 v47, 0xffff0000, v15\n"
    "v_pk_add_f32 v[32:33], v[32:33], v[40:41]\n"
    "v_pk_add_f32 v[34:35], v[34:35], v[42:43]\n"
    "v_pk_add_f32 v[36:37], v[36:37], v[44:45]\n"
    "v_pk_add_f32 v[38:39], v[38:39], v[46:47]\n"
    "s_waitcnt vmcnt(3)\n"
    "v_lshlrev_b32 v40, 16, v16\n"
    "v_and_b32 v41, 0xffff0000, v16\n"
    "v_lshlrev_b32 v42, 16, v17\n"
    "v_and_b32 v43, 0xffff0000, v17\n"
    "v_lshlrev_b32 v44, 16, v18\n"
    "v_and_b32 v45, 0xffff0000, v18\n"
    "v_lshlrev_b32 v46, 16, v19\n"
    "v_and_b32 v47, 0xffff0000, v19\n"
    "v_pk_add_f32 v[32:33], v[32:33], v[40:41]\n"
    "v_pk_add_f32 v[34:35], v[34:35], v[42:43]\n"
    "v_pk_add_f32 v[36:37], v[36:37], v[44:45]\n"
    "v_pk_add_f32 v[38:39], v[38:39], v[46:47]\n"
    "s_waitcnt vmcnt(2)\n"
    "v_lshlrev_b32 v40, 16, v20\n"
    "v_and_b32 v41, 0xffff0000, v20\n"
    "v_lshlrev_b32 v42, 16, v21\n"
    "v_and_b32 v43, 0xffff0000, v21\n"
    "v_lshlrev_b32 v44, 16, v22\n"
    "v_and_b32 v45, 0xffff0000, v22\n"
    "v_lshlrev_b32 v46, 16, v23\n"
    "v_and_b32 v47, 0xffff0000, v23\n"
    "v_pk_add_f32 v[32:33], v[32:33], v[40:41]\n"
    "v_pk_add_f32 v[34:35], v[34:35], v[42:43]\n"
    "v_pk_add_f32 v[36:37], v[36:37], v[44:45]\n"
    "v_pk_add_f32 v[38:39], v[38:39], v[46:47]\n"
    "s_waitcnt vmcnt(1)\n"
    "v_lshlrev_b32 v40, 16, v24\n"
    "v_and_b32 v41, 0xffff0000, v24\n"
    "v_lshlrev_b32 v42, 16, v25\n"
    "v_and_b32 v43, 0xffff0000, v25\n"
    "v_lshlrev_b32 v44, 16, v26\n"
    "v_and_b32 v45, 0xffff0000, v26\n"
    "v_lshlrev_b32 v46, 16, v27\n"
    "v_and_b32 v47, 0xffff0000, v27\n"
    "v_pk_add_f32 v[32:33], v[32:33], v[40:41]\n"
    "v_pk_add_f32 v[34:35], v[34:35], v[42:43]\n"
    "v_pk_add_f32 v[36:37], v[36:37], v[44:45]\n"
    "v_pk_add_f32 v[38:39], v[38:39], v[46:47]\n"
    "s_waitcnt vmcnt(0)\n"
    "v_lshlrev_b32 v40, 16, v28\n"
    "v_and_b32 v41, 0xffff0000, v28\n"
    "v_lshlrev_b32 v42, 16, v29\n"
    "v_and_b32 v43, 0xffff0000, v29\n"
    "v_lshlrev_b32 v44, 16, v30\n"
    "v_and_b32 v45, 0xffff0000, v30\n"
    "v_lshlrev_b32 v46, 16, v31\n"
    "v_and_b32 v47, 0xffff0000, v31\n"
    ".p2align 12\n"
    "L_pad12:\n"
    "s_cbranch_execz L_walked\n"
    "v_pk_add_f32 v[32:33], v[32:33], v[40:41]\n"
    "v_pk_add_f32 v[34:35], v[34:35], v[42:43]\n"
    "v_pk_add_f32 v[36:37], v[36:37], v[44:45]\n"
    "v_pk_add_f32 v[38:39], v[38:39], v[46:47]\n"
    "v_pk_fma_f32 v[32:33], v[32:33], v[72:73], v[232:233]\n"
    "v_pk_fma_f32 v[34:35], v[34:35], v[74:75], v[234:235]\n"
    "v_pk_fma_f32 v[36:37], v[36:37], v[76:77], v[236:237]\n"
    "v_pk_fma_f32 v[38:39], v[38:39], v[78:79], v[238:239]\n"
    "v_cvt_pk_bf16_f32 v40, v32, v33\n"
    "v_cvt_pk_bf16_f32 v41, v34, v35\n"
    "v_cvt_pk_bf16_f32 v42, v36, v37\n"
    "v_cvt_pk_bf16_f32 v43, v38, v39\n"
    "v_lshlrev_b32 v232, 16, v40\n"
    "v_and_b32 v233, 0xffff0000, v40\n"
    "v_lshlrev_b32 v234, 16, v41\n"
    "v_and_b32 v235, 0xffff0000, v41\n"
    "v_lshlrev_b32 v236, 16, v42\n"
    "v_and_b32 v237, 0xffff0000, v42\n"
    "v_lshlrev_b32 v238, 16, v43\n"
    "v_and_b32 v239, 0xffff0000, v43\n"
    "v_pk_mul_f32 v[32:33], v[232:233], v[80:81]\n"
    "v_pk_mul_f32 v[34:35], v[234:235], v[82:83]\n"
    "v_pk_mul_f32 v[36:37], v[236:237], v[84:85]\n"
    "v_pk_mul_f32 v[38:39], v[238:239], v[86:87]\n"
    "v_cvt_pk_bf16_f32 v44, v32, v33\n"
    "v_cvt_pk_bf16_f32 v45, v34, v35\n"
    "v_cvt_pk_bf16_f32 v46, v36, v37\n"
    "v_cvt_pk_bf16_f32 v47, v38, v39\n"
    "s_lshl_b32 s69, s81, 10\n"
    "s_add_u32 s69, s69, 0x3500000\n"
    "buffer_store_dwordx4 v[44:47], v190, s[76:79], s69 offen sc1\n"
    "v_pk_add_f32 v[40:41], v[232:233], v[234:235]\n"
    "v_pk_add_f32 v[42:43], v[236:237], v[238:239]\n"
    "v_pk_add_f32 v[40:41], v[40:41], v[42:43]\n"
    "v_add_f32 v40, v40, v41\n"
    "v_mov_b32 v42, v40\n"
    "s_nop 1\n"
    "v_permlane32_swap_b32 v40, v42\n"
    "v_add_f32 v40, v40, v42\n"
    "v_mov_b32 v42, v40\n"
    "s_nop 1\n"
    "v_permlane16_swap_b32 v40, v42\n"
    "v_add_f32 v40, v40, v42\n"
    "v_mul_f32 v40, 0x3d000000, v40\n"
    "v_pk_add_f32 v[32:33], v[232:233], v[40:41] op_sel_hi:[1,0] neg_lo:[0,1] neg_hi:[0,1]\n"
    "v_pk_add_f32 v[34:35], v[234:235], v[40:41] op_sel_hi:[1,0] neg_lo:[0,1] neg_hi:[0,1]\n"
    "v_pk_add_f32 v[36:37], v[236:237], v[40:41] op_sel_hi:[1,0] neg_lo:[0,1] neg_hi:[0,1]\n"
    "v_pk_add_f32 v[38:39], v[238:239], v[40:41] op_sel_hi:[1,0] neg_lo:[0,1] neg_hi:[0,1]\n"
    "v_pk_mul_f32 v[42:43], v[32:33], v[32:33]\n"
    "v_pk_fma_f32 v[42:43], v[34:35], v[34:35], v[42:43]\n"
    "v_pk_fma_f32 v[42:43], v[36:37], v[36:37], v[42:43]\n"
    "v_pk_fma_f32 v[42:43], v[38:39], v[38:39], v[42:43]\n"
    "v_add_f32 v41, v42, v43\n"
    "v_mov_b32 v42, v41\n"
    "s_nop 1\n"
    "v_permlane32_swap_b32 v41, v42\n"
    "v_add_f32 v41, v41, v42\n"
    "v_mov_b32 v42, v41\n"
    "s_nop 1\n"
    "v_permlane16_swap_b32 v41, v42\n"
    "v_add_f32 v41, v41, v42\n"
    "s_lshl_b32 s69, s81, 7\n"
    "s_add_u32 s69, s69, 0x40000\n"
    "buffer_store_dwordx2 v[40:41], v108, s[76:79], s69 offen sc1\n"
    "buffer_load_dwordx4 a[72:75], v98, s[76:79], s68 offen offset:0 sc1\n"
    "buffer_load_dwordx4 a[76:79], v98, s[76:79], s68 offen offset:1024 sc1\n"
    "s_add_u32 s68, s83, 4096\n"
    "buffer_load_dwordx4 a[80:83], v97, s[76:79], s68 offen offset:0 sc1\n"
    "buffer_load_dwordx4 a[84:87], v97, s[76:79], s68 offen offset:1024 sc1\n"
    "s_lshl_b32 s4, s52, 5\n"
    "s_add_u32 s4, s4, 0x5800000\n"
    "buffer_load_dword v110, v181, s[76:79], s4 offen sc1\n"
    "buffer_load_dwordx4 a[88:91], v98, s[76:79], s68 offen offset:0 sc1\n"
    "buffer_load_dwordx4 a[92:95], v98, s[76:79], s68 offen offset:1024 sc1\n"
    "s_add_u32 s68, s83, 8192\n"
    "buffer_load_dwordx4 a[96:99], v97, s[76:79], s68 offen offset:0 sc1\n"
    "buffer_load_dwordx4 a[100:103], v97, s[76:79], s68 offen offset:1024 sc1\n"
    "s_lshl_b32 s69, s81, 2\n"
    "s_add_u32 s69, s69, 0x9000\n"
    "s_waitcnt vmcnt(9)\n"
    "s_lshl_b32 s5, s69, 5\n"
    "s_add_u32 s5, s5, 0x5800000\n"
    "buffer_store_dword v182, v180, s[76:79], s5 offen sc1\n"
    "s_waitcnt vmcnt(5)\n"
    "v_cmp_ne_u32 vcc, 1, v110\n"
    "s_cbranch_vccz L_ya_ok\n"
    "s_mov_b32 s14, 0x40000\n"
    "L_ya_spin:\n"
    "buffer_load_dword v110, v181, s[76:79], s4 offen sc1\n"
    "s_sub_u32 s14, s14, 1\n"
    "s_waitcnt vmcnt(0)\n"
    "s_cbranch_scc1 L_ya_ok\n"
    "v_cmp_ne_u32 vcc, 1, v110\n"
    "s_cbranch_vccz L_ya_ok\n"
    "s_sleep 3\n"
    "s_branch L_ya_spin\n"
    "L_ya_ok:\n"
    "s_lshl_b32 s69, s82, 11\n"
    "s_add_u32 s69, s69, 0x20000\n"
    "buffer_load_dwordx2 v[24:25], v179, s[76:79], s69 offen offset:0 sc1\n"
    "buffer_load_dwordx2 v[26:27], v179, s[76:79], s69 offen offset:128 sc1\n"
    "buffer_load_dwordx2 v[28:29], v179, s[76:79], s69 offen offset:256 sc1\n"
    "buffer_load_dwordx2 v[30:31], v179, s[76:79], s69 offen offset:384 sc1\n"
    "s_lshl_b32 s89, s82, 14\n"
    "s_add_u32 m0, s57, 0\n"
    "s_add_u32 s70, s89, 0x3400000\n"
    "buffer_load_dwordx4 v190, s[76:79], s70 offen offset:0 sc1 lds\n"
    "buffer_load_dwordx4 v190, s[76:79], s70 offen offset:1024 sc1 lds\n"
    "buffer_load_dwordx4 v190, s[76:79], s70 offen offset:2048 sc1 lds\n"
    "buffer_load_dwordx4 v190, s[76:79], s70 offen offset:3072 sc1 lds\n"
    "s_add_u32 m0, s57, 16384\n"
    "s_add_u32 s70, s89, 0x3401000\n"
    "buffer_load_dwordx4 v190, s[76:79], s70 offen offset:0 sc1 lds\n"
    "buffer_load_dwordx4 v190, s[76:79], s70 offen offset:1024 sc1 lds\n"
    "buffer_load_dwordx4 v190, s[76:79], s70 offen offset:2048 sc1 lds\n"
    "buffer_load_dwordx4 v190, s[76:79], s70 offen offset:3072 sc1 lds\n"
    "s_add_u32 m0, s57, 32768\n"
    "s_add_u32 s70, s89, 0x3402000\n"
    "buffer_load_dwordx4 v190, s[76:79], s70 offen offset:0 sc1 lds\n"
    "buffer_load_dwordx4 v190, s[76:79], s70 offen offset:1024 sc1 lds\n"
    "buffer_load_dwordx4 v190, s[76:79], s70 offen offset:2048 sc1 lds\n"
    "buffer_load_dwordx4 v190, s[76:79], s70 offen offset:3072 sc1 lds\n"
    "s_add_u32 m0, s57, 49152\n"
    "s_add_u32 s70, s89, 0x3403000\n"
    "buffer_load_dwordx4 v190, s[76:79], s70 offen offset:0 sc1 lds\n"
    "buffer_load_dwordx4 v190, s[76:79], s70 offen offset:1024 sc1 lds\n"
    "buffer_load_dwordx4 v190, s[76:79], s70 offen offset:2048 sc1 lds\n"
    "buffer_load_dwordx4 v190, s[76:79], s70 offen offset:3072 sc1 lds\n"
    "s_add_u32 s52, s52, 0x1000\n"
    "s_waitcnt vmcnt(16)\n"
    "v_add_f32 v16, v24, v26\n"
    "v_add_f32 v17, v25, v27\n"
    "v_add_f32 v16, v16, v28\n"
    "v_add_f32 v17, v17, v29\n"
    "v_add_f32 v16, v16, v30\n"
    "v_add_f32 v17, v17, v31\n"
    "v_mov_b32 v18, v16\n"
    "v_mov_b32 v19, v17\n"
    "s_nop 1\n"
    "v_permlane32_swap_b32 v16, v18\n"
    "v_permlane32_swap_b32 v17, v19\n"
    "v_add_f32 v16, v16, v18\n"
    "v_add_f32 v17, v17, v19\n"
    "v_mov_b32 v18, v16\n"
    "v_mov_b32 v19, v17\n"
    "s_nop 1\n"
    "v_permlane16_swap_b32 v16, v18\n"
    "v_permlane16_swap_b32 v17, v19\n"
    "v_add_f32 v16, v16, v18\n"
    "v_add_f32 v17, v17, v19\n"
    "v_mul_f32 v16, 0x3d800000, v16\n"
    "v_sub_f32 v24, v24, v16\n"
    "v_sub_f32 v26, v26, v16\n"
    "v_sub_f32 v28, v28, v16\n"
    "v_sub_f32 v30, v30, v16\n"
    "v_mul_f32 v25, v24, v24\n"
    "v_fmac_f32 v25, v26, v26\n"
    "v_fmac_f32 v25, v28, v28\n"
    "v_fmac_f32 v25, v30, v30\n"
    "v_mov_b32 v18, v25\n"
    "s_nop 1\n"
    "v_permlane32_swap_b32 v25, v18\n"
    "v_add_f32 v25, v25, v18\n"
    "v_mov_b32 v18, v25\n"
    "s_nop 1\n"
    "v_permlane16_swap_b32 v25, v18\n"
    "v_add_f32 v25, v25, v18\n"
    "v_fmamk_f32 v17, v25, 0x42000000, v17\n"
    "v_mov_b32 v18, 0x3727c5ac\n"
    "v_fmamk_f32 v17, v17, 0x3b000000, v18\n"
    "v_rsq_f32 v17, v17\n"
    "v_add_u32 v18, s57, v177\n"
    "v_mul_f32 v17, 0x3fb8aa3b, v17\n"
    "ds_write_b64 v18, v[16:17] offset:0\n"
    "s_waitcnt vmcnt(0) lgkmcnt(0)\n"
    "s_barrier\n"
    "ds_read_b128 v[32:35], v190 offset:0\n"
    "ds_read_b128 v[36:39], v190 offset:1024\n"
    "ds_read_b128 v[40:43], v190 offset:2048\n"
    "ds_read_b128 v[44:47], v190 offset:3072\n"
    "ds_read_b128 v[48:51], v190 offset:16384\n"
    "ds_read_b128 v[52:55], v190 offset:17408\n"
    "ds_read_b128 v[56:59], v190 offset:18432\n"
    "ds_read_b128 v[60:63], v190 offset:19456\n"
    "ds_read_b128 v[64:67], v190 offset:32768\n"
    "ds_read_b128 v[68:71], v190 offset:33792\n"
    "ds_read_b128 v[72:75], v190 offset:34816\n"
    "ds_read_b128 v[76:79], v190 offset:35840\n"
    "s_waitcnt lgkmcnt(8)\n"
    "v_mfma_f32_16x16x32_bf16 v[0:3], a[0:3], v[32:35], 0\n"
    "s_lshl_b32 s4, s52, 5\n"
    "s_add_u32 s4, s4, 0x5800000\n"
    "buffer_load_dword v110, v181, s[76:79], s4 offen sc1\n"
    "v_mfma_f32_16x16x32_bf16 v[0:3], a[8:11], v[36:39], v[0:3]\n"
    "s_mov_b32 s60, 0x3f3504f3\n"
    "ds_read_b128 v[80:83], v190 offset:49152\n"
    "ds_read_b128 v[84:87], v190 offset:50176\n"
    "v_mfma_f32_16x16x32_bf16 v[0:3], a[16:19], v[40:43], v[0:3]\n"
    "v_mfma_f32_16x16x32_bf16 v[0:3], a[24:27], v[44:47], v[0:3]\n"
    "s_mov_b32 s61, 0x3f3504f3\n"
    "ds_read_b64 v[240:241], v177 offset:0\n"
    "ds_read_b64 v[242:243], v177 offset:4096\n"
    "v_mfma_f32_16x16x32_bf16 v[4:7], a[4:7], v[32:35], 0\n"
    "buffer_load_dwordx4 a[104:107], v98, s[76:79], s68 offen offset:0 sc1\n"
    "v_mfma_f32_16x16x32_bf16 v[4:7], a[12:15], v[36:39], v[4:7]\n"
    "ds_read_b128 v[88:91], v190 offset:51200\n"
    "ds_read_b128 v[92:95], v190 offset:52224\n"
    "v_mfma_f32_16x16x32_bf16 v[4:7], a[20:23], v[40:43], v[4:7]\n"
    "v_mfma_f32_16x16x32_bf16 v[4:7], a[28:31], v[44:47], v[4:7]\n"
    "s_waitcnt lgkmcnt(10)\n"
    "ds_read_b64 v[244:245], v177 offset:8192\n"
    "ds_read_b64 v[246:247], v177 offset:12288\n"
    "v_mfma_f32_16x16x32_bf16 v[0:3], a[32:35], v[48:51], v[0:3]\n"
    "buffer_load_dwordx4 a[108:111], v98, s[76:79], s68 offen offset:1024 sc1\n"
    "v_mfma_f32_16x16x32_bf16 v[0:3], a[40:43], v[52:55], v[0:3]\n"
    "ds_read_b128 v[32:35], v190 offset:4096\n"
    "ds_read_b128 v[36:39], v190 offset:5120\n"
    "v_mfma_f32_16x16x32_bf16 v[0:3], a[48:51], v[56:59], v[0:3]\n"
    "v_mfma_f32_16x16x32_bf16 v[0:3], a[56:59], v[60:63], v[0:3]\n"
    "v_mfma_f32_16x16x32_bf16 v[4:7], a[36:39], v[48:51], v[4:7]\n"
    "s_add_u32 s68, s83, 12288\n"
    "buffer_load_dwordx4 a[112:115], v97, s[76:79], s68 offen offset:0 sc1\n"
    "v_mfma_f32_16x16x32_bf16 v[4:7], a[44:47], v[52:55], v[4:7]\n"
    "ds_read_b128 v[40:43], v190 offset:6144\n"
    "ds_read_b128 v[44:47], v190 offset:7168\n"
    "v_mfma_f32_16x16x32_bf16 v[4:7], a[52:55], v[56:59], v[4:7]\n"
    "v_mfma_f32_16x16x32_bf16 v[4:7], a[60:63], v[60:63], v[4:7]\n"
    "s_waitcnt lgkmcnt(12)\n"
    "v_mfma_f32_16x16x32_bf16 v[0:3], a[128:131], v[64:67], v[0:3]\n"
    "buffer_load_dwordx4 a[116:119], v97, s[76:79], s68 offen offset:1024 sc1\n"
    "v_mfma_f32_16x16x32_bf16 v[0:3], a[136:139], v[68:71], v[0:3]\n"
    "ds_read_b128 v[48:51], v190 offset:20480\n"
    "ds_read_b128 v[52:55], v190 offset:21504\n"
    "v_mfma_f32_16x16x32_bf16 v[0:3], a[144:147], v[72:75], v[0:3]\n"
    "v_mfma_f32_16x16x32_bf16 v[0:3], a[152:155], v[76:79], v[0:3]\n"
    "v_mfma_f32_16x16x32_bf16 v[4:7], a[132:135], v[64:67], v[4:7]\n"
    "buffer_load_dwordx4 a[120:123], v98, s[76:79], s68 offen offset:0 sc1\n"
    "v_mfma_f32_16x16x32_bf16 v[4:7], a[140:143], v[68:71], v[4:7]\n"
    "ds_read_b128 v[56:59], v190 offset:22528\n"
    "ds_read_b128 v[60:63], v190 offset:23552\n"
    "v_mfma_f32_16x16x32_bf16 v[4:7], a[148:151], v[72:75], v[4:7]\n"
    "s_waitcnt vmcnt(5)\n"
    "v_cmp_ne_u32 vcc, 1, v110\n"
    "s_cbranch_vccz L_yb_ok\n"
    "s_mov_b32 s14, 0x40000\n"
    "L_yb_spin:\n"
    "buffer_load_dword v110, v181, s[76:79], s4 offen sc1\n"
    "s_sub_u32 s14, s14, 1\n"
    "s_waitcnt vmcnt(0)\n"
    "s_cbranch_scc1 L_yb_ok\n"
    "v_cmp_ne_u32 vcc, 1, v110\n"
    "s_cbranch_vccz L_yb_ok\n"
    "s_sleep 3\n"
    "s_branch L_yb_spin\n"
    "L_yb_ok:\n"
    "v_mfma_f32_16x16x32_bf16 v[4:7], a[156:159], v[76:79], v[4:7]\n"
    "s_waitcnt lgkmcnt(10)\n"
    "s_lshl_b32 s69, s82, 11\n"
    "s_add_u32 s69, s69, 0x40000\n"
    "buffer_load_dwordx2 v[208:209], v179, s[76:79], s69 offen offset:0 sc1\n"
    "v_mfma_f32_16x16x32_bf16 v[0:3], a[160:163], v[80:83], v[0:3]\n"
    "v_mfma_f32_16x16x32_bf16 v[0:3], a[168:171], v[84:87], v[0:3]\n"
    "ds_read_b128 v[64:67], v190 offset:36864\n"
    "ds_read_b128 v[68:71], v190 offset:37888\n"
    "v_mfma_f32_16x16x32_bf16 v[0:3], a[176:179], v[88:91], v[0:3]\n"
    "buffer_load_dwordx2 v[210:211], v179, s[76:79], s69 offen offset:128 sc1\n"
    "v_mfma_f32_16x16x32_bf16 v[0:3], a[184:187], v[92:95], v[0:3]\n"
    "v_mfma_f32_16x16x32_bf16 v[4:7], a[164:167], v[80:83], v[4:7]\n"
    "buffer_load_dwordx2 v[212:213], v179, s[76:79], s69 offen offset:256 sc1\n"
    "v_mfma_f32_16x16x32_bf16 v[4:7], a[172:175], v[84:87], v[4:7]\n"
    "ds_read_b128 v[72:75], v190 offset:38912\n"
    "ds_read_b128 v[76:79], v190 offset:39936\n"
    "v_mfma_f32_16x16x32_bf16 v[4:7], a[180:183], v[88:91], v[4:7]\n"
    "buffer_load_dwordx2 v[214:215], v179, s[76:79], s69 offen offset:384 sc1\n"
    "v_mfma_f32_16x16x32_bf16 v[4:7], a[188:191], v[92:95], v[4:7]\n"
    "s_waitcnt lgkmcnt(8)\n"
    "v_mfma_f32_16x16x32_bf16 v[8:11], a[0:3], v[32:35], 0\n"
    "s_lshl_b32 s89, s82, 14\n"
    "s_add_u32 m0, s57, 65536\n"
    "s_add_u32 s70, s89, 0x3500000\n"
    "buffer_load_dwordx4 v190, s[76:79], s70 offen offset:0 sc1 lds\n"
    "v_mfma_f32_16x16x32_bf16 v[8:11], a[8:11], v[36:39], v[8:11]\n"
    "ds_read_b128 v[80:83], v190 offset:53248\n"
    "ds_read_b128 v[84:87], v190 offset:54272\n"
    "v_mfma_f32_16x16x32_bf16 v[8:11], a[16:19], v[40:43], v[8:11]\n"
    "buffer_load_dwordx4 v190, s[76:79], s70 offen offset:1024 sc1 lds\n"
    "v_mfma_f32_16x16x32_bf16 v[8:11], a[24:27], v[44:47], v[8:11]\n"
    "v_mfma_f32_16x16x32_bf16 v[12:15], a[4:7], v[32:35], 0\n"
    "buffer_load_dwordx4 v190, s[76:79], s70 offen offset:2048 sc1 lds\n"
    "v_mfma_f32_16x16x32_bf16 v[12:15], a[12:15], v[36:39], v[12:15]\n"
    "ds_read_b128 v[88:91], v190 offset:55296\n"
    "ds_read_b128 v[92:95], v190 offset:56320\n"
    "v_pk_fma_f32 v[0:1], v[192:193], v[240:241], v[0:1] op_sel_hi:[1,0,1] neg_lo:[0,1,0] neg_hi:[0,1,0]\n"
    "v_pk_fma_f32 v[2:3], v[194:195], v[240:241], v[2:3] op_sel_hi:[1,0,1] neg_lo:[0,1,0] neg_hi:[0,1,0]\n"
    "v_pk_fma_f32 v[4:5], v[196:197], v[240:241], v[4:5] op_sel_hi:[1,0,1] neg_lo:[0,1,0] neg_hi:[0,1,0]\n"
    "v_pk_fma_f32 v[6:7], v[198:199], v[240:241], v[6:7] op_sel_hi:[1,0,1] neg_lo:[0,1,0] neg_hi:[0,1,0]\n"
    "v_pk_fma_f32 v[0:1], v[0:1], v[240:241], v[200:201] op_sel:[0,1,0]\n"
    "v_pk_fma_f32 v[2:3], v[2:3], v[240:241], v[202:203] op_sel:[0,1,0]\n"
    "v_pk_fma_f32 v[4:5], v[4:5], v[240:241], v[204:205] op_sel:[0,1,0]\n"
    "v_pk_fma_f32 v[6:7], v[6:7], v[240:241], v[206:207] op_sel:[0,1,0]\n"
    "v_mfma_f32_16x16x32_bf16 v[12:15], a[20:23], v[40:43], v[12:15]\n"
    "buffer_load_dwordx4 v190, s[76:79], s70 offen offset:3072 sc1 lds\n"
    "v_exp_f32 v16, v0\n"
    "v_mfma_f32_16x16x32_bf16 v[12:15], a[28:31], v[44:47], v[12:15]\n"
    "s_waitcnt lgkmcnt(8)\n"
    "v_exp_f32 v17, v1\n"
    "v_mfma_f32_16x16x32_bf16 v[8:11], a[32:35], v[48:51], v[8:11]\n"
    "s_add_u32 m0, s57, 81920\n"
    "s_add_u32 s70, s89, 0x3501000\n"
    "buffer_load_dwordx4 v190, s[76:79], s70 offen offset:0 sc1 lds\n"
    "v_exp_f32 v18, v2\n"
    "v_mfma_f32_16x16x32_bf16 v[8:11], a[40:43], v[52:55], v[8:11]\n"
    "ds_read_b128 v[32:35], v190 offset:8192\n"
    "ds_read_b128 v[36:39], v190 offset:9216\n"
    "v_mfma_f32_16x16x32_bf16 v[8:11], a[48:51], v[56:59], v[8:11]\n"
    "buffer_load_dwordx4 v190, s[76:79], s70 offen offset:1024 sc1 lds\n"
    "v_exp_f32 v19, v3\n"
    "v_mfma_f32_16x16x32_bf16 v[8:11], a[56:59], v[60:63], v[8:11]\n"
    "v_exp_f32 v20, v4\n"
    "v_mfma_f32_16x16x32_bf16 v[12:15], a[36:39], v[48:51], v[12:15]\n"
    "buffer_load_dwordx4 v190, s[76:79], s70 offen offset:2048 sc1 lds\n"
    "v_exp_f32 v21, v5\n"
    "v_mfma_f32_16x16x32_bf16 v[12:15], a[44:47], v[52:55], v[12:15]\n"
    "ds_read_b128 v[40:43], v190 offset:10240\n"
    "ds_read_b128 v[44:47], v190 offset:11264\n"
    "v_mfma_f32_16x16x32_bf16 v[12:15], a[52:55], v[56:59], v[12:15]\n"
    "buffer_load_dwordx4 v190, s[76:79], s70 offen offset:3072 sc1 lds\n"
    "v_exp_f32 v22, v6\n"
    "v_exp_f32 v23, v7\n"
    "v_pk_fma_f32 v[16:17], v[16:17], s[60:61], s[60:61]\n"
    "v_pk_fma_f32 v[18:19], v[18:19], s[60:61], s[60:61]\n"
    "v_pk_fma_f32 v[20:21], v[20:21], s[60:61], s[60:61]\n"
    "v_pk_fma_f32 v[22:23], v[22:23], s[60:61], s[60:61]\n"
    "v_pk_fma_f32 v[16:17], v[16:17], v[16:17], 0.5 op_sel_hi:[1,1,0]\n"
    "v_pk_fma_f32 v[18:19], v[18:19], v[18:19], 0.5 op_sel_hi:[1,1,0]\n"
    "v_pk_fma_f32 v[20:21], v[20:21], v[20:21], 0.5 op_sel_hi:[1,1,0]\n"
    "v_pk_fma_f32 v[22:23], v[22:23], v[22:23], 0.5 op_sel_hi:[1,1,0]\n"
    "v_mfma_f32_16x16x32_bf16 v[12:15], a[60:63], v[60:63], v[12:15]\n"
    "s_waitcnt lgkmcnt(8)\n"
    "v_rcp_f32 v16, v16\n"
    "v_mfma_f32_16x16x32_bf16 v[8:11], a[128:131], v[64:67], v[8:11]\n"
    "s_add_u32 m0, s57, 98304\n"
    "s_add_u32 s70, s89, 0x3502000\n"
    "buffer_load_dwordx4 v190, s[76:79], s70 offen offset:0 sc1 lds\n"
    "v_rcp_f32 v17, v17\n"
    "v_mfma_f32_16x16x32_bf16 v[8:11], a[136:139], v[68:71], v[8:11]\n"
    "ds_read_b128 v[48:51], v190 offset:24576\n"
    "ds_read_b128 v[52:55], v190 offset:25600\n"
    "v_mfma_f32_16x16x32_bf16 v[8:11], a[144:147], v[72:75], v[8:11]\n"
    "buffer_load_dwordx4 v190, s[76:79], s70 offen offset:1024 sc1 lds\n"
    "v_rcp_f32 v18, v18\n"
    "v_mfma_f32_16x16x32_bf16 v[8:11], a[152:155], v[76:79], v[8:11]\n"
    "v_rcp_f32 v19, v19\n"
    "s_waitcnt vmcnt(10)\n"
    "v_mfma_f32_16x16x32_bf16 v[12:15], a[132:135], v[64:67], v[12:15]\n"
    "buffer_load_dwordx4 v190, s[76:79], s70 offen offset:2048 sc1 lds\n"
    "v_rcp_f32 v20, v20\n"
    "v_mfma_f32_16x16x32_bf16 v[12:15], a[140:143], v[68:71], v[12:15]\n"
    "ds_read_b128 v[56:59], v190 offset:26624\n"
    "ds_read_b128 v[60:63], v190 offset:27648\n"
    "v_add_f32 v216, v208, v210\n"
    "v_mfma_f32_16x16x32_bf16 v[12:15], a[148:151], v[72:75], v[12:15]\n"
    "v_rcp_f32 v21, v21\n"
    "v_add_f32 v217, v209, v211\n"
    "v_mfma_f32_16x16x32_bf16 v[12:15], a[156:159], v[76:79], v[12:15]\n"
    "s_waitcnt lgkmcnt(8)\n"
    "buffer_load_dwordx4 v190, s[76:79], s70 offen offset:3072 sc1 lds\n"
    "v_mfma_f32_16x16x32_bf16 v[8:11], a[160:163], v[80:83], v[8:11]\n"
    "v_rcp_f32 v22, v22\n"
    "v_rcp_f32 v23, v23\n"
    "v_pk_fma_f32 v[0:1], v[0:1], v[16:17], v[0:1] neg_lo:[1,0,0] neg_hi:[1,0,0]\n"
    "v_pk_fma_f32 v[2:3], v[2:3], v[18:19], v[2:3] neg_lo:[1,0,0] neg_hi:[1,0,0]\n"
    "v_pk_fma_f32 v[4:5], v[4:5], v[20:21], v[4:5] neg_lo:[1,0,0] neg_hi:[1,0,0]\n"
    "v_pk_fma_f32 v[6:7], v[6:7], v[22:23], v[6:7] neg_lo:[1,0,0] neg_hi:[1,0,0]\n"
    "v_mfma_f32_16x16x32_bf16 v[8:11], a[168:171], v[84:87], v[8:11]\n"
    "s_add_u32 m0, s57, 114688\n"
    "s_add_u32 s70, s89, 0x3503000\n"
    "ds_read_b128 v[64:67], v190 offset:40960\n"
    "ds_read_b128 v[68:71], v190 offset:41984\n"
    "buffer_load_dwordx4 v190, s[76:79], s70 offen offset:0 sc1 lds\n"
    "v_mfma_f32_16x16x32_bf16 v[8:11], a[176:179], v[88:91], v[8:11]\n"
    "v_cvt_pk_bf16_f32 v0, v0, v1\n"
    "v_cvt_pk_bf16_f32 v1, v2, v3\n"
    "v_add_f32 v216, v216, v212\n"
    "v_mfma_f32_16x16x32_bf16 v[8:11], a[184:187], v[92:95], v[8:11]\n"
    "buffer_load_dwordx4 v190, s[76:79], s70 offen offset:1024 sc1 lds\n"
    "v_cvt_pk_bf16_f32 v2, v4, v5\n"
    "v_cvt_pk_bf16_f32 v3, v6, v7\n"
    "v_mfma_f32_16x16x32_bf16 v[12:15], a[164:167], v[80:83], v[12:15]\n"
    "s_add_u32 s88, s28, 0\n"
    "buffer_store_dwordx4 v[0:3], v190, s[76:79], s88 offen sc1\n"
    "v_mfma_f32_16x16x32_bf16 v[12:15], a[172:175], v[84:87], v[12:15]\n"
    "ds_read_b128 v[72:75], v190 offset:43008\n"
    "ds_read_b128 v[76:79], v190 offset:44032\n"
    "v_add_f32 v217, v217, v213\n"
    "v_mfma_f32_16x16x32_bf16 v[12:15], a[180:183], v[88:91], v[12:15]\n"
    "buffer_load_dwordx4 v190, s[76:79], s70 offen offset:2048 sc1 lds\n"
    "v_mfma_f32_16x16x32_bf16 v[12:15], a[188:191], v[92:95], v[12:15]\n"
    "s_waitcnt lgkmcnt(8)\n"
    "v_add_f32 v216, v216, v214\n"
    "v_add_f32 v217, v217, v215\n"
    "v_mov_b32 v218, v216\n"
    "v_mfma_f32_16x16x32_bf16 v[0:3], a[0:3], v[32:35], 0\n"
    "buffer_load_dwordx4 v190, s[76:79], s70 offen offset:3072 sc1 lds\n"
    "v_mfma_f32_16x16x32_bf16 v[0:3], a[8:11], v[36:39], v[0:3]\n"
    "ds_read_b128 v[80:83], v190 offset:57344\n"
    "ds_read_b128 v[84:87], v190 offset:58368\n"
    "v_mov_b32 v219, v217\n"
    "v_mfma_f32_16x16x32_bf16 v[0:3], a[16:19], v[40:43], v[0:3]\n"
    "s_nop 1\n"
    "v_permlane32_swap_b32 v216, v218\n"
    "v_mfma_f32_16x16x32_bf16 v[0:3], a[24:27], v[44:47], v[0:3]\n"
    "v_permlane32_swap_b32 v217, v219\n"
    "v_mfma_f32_16x16x32_bf16 v[4:7], a[4:7], v[32:35], 0\n"
    "buffer_load_dwordx4 a[124:127], v98, s[76:79], s68 offen offset:1024 sc1\n"
    "v_mfma_f32_16x16x32_bf16 v[4:7], a[12:15], v[36:39], v[4:7]\n"
    "ds_read_b128 v[88:91], v190 offset:59392\n"
    "ds_read_b128 v[92:95], v190 offset:60416\n"
    "v_pk_fma_f32 v[8:9], v[192:193], v[242:243], v[8:9] op_sel_hi:[1,0,1] neg_lo:[0,1,0] neg_hi:[0,1,0]\n"
    "v_pk_fma_f32 v[10:11], v[194:195], v[242:243], v[10:11] op_sel_hi:[1,0,1] neg_lo:[0,1,0] neg_hi:[0,1,0]\n"
    "v_pk_fma_f32 v[12:13], v[196:197], v[242:243], v[12:13] op_sel_hi:[1,0,1] neg_lo:[0,1,0] neg_hi:[0,1,0]\n"
    "v_pk_fma_f32 v[14:15], v[198:199], v[242:243], v[14:15] op_sel_hi:[1,0,1] neg_lo:[0,1,0] neg_hi:[0,1,0]\n"
    "v_pk_fma_f32 v[8:9], v[8:9], v[242:243], v[200:201] op_sel:[0,1,0]\n"
    "v_pk_fma_f32 v[10:11], v[10:11], v[242:243], v[202:203] op_sel:[0,1,0]\n"
    "v_pk_fma_f32 v[12:13], v[12:13], v[242:243], v[204:205] op_sel:[0,1,0]\n"
    "v_pk_fma_f32 v[14:15], v[14:15], v[242:243], v[206:207] op_sel:[0,1,0]\n"
    "v_mfma_f32_16x16x32_bf16 v[4:7], a[20:23], v[40:43], v[4:7]\n"
    "v_exp_f32 v16, v8\n"
    "v_add_f32 v216, v216, v218\n"
    "v_mfma_f32_16x16x32_bf16 v[4:7], a[28:31], v[44:47], v[4:7]\n"
    "s_waitcnt lgkmcnt(8)\n"
    "v_exp_f32 v17, v9\n"
    "v_add_f32 v217, v217, v219\n"
    "v_mfma_f32_16x16x32_bf16 v[0:3], a[32:35], v[48:51], v[0:3]\n"
    "s_add_u32 s68, s83, 16384\n"
    "buffer_load_dwordx4 a[192:195], v97, s[76:79], s68 offen offset:0 sc1\n"
    "v_exp_f32 v18, v10\n"
    "v_mfma_f32_16x16x32_bf16 v[0:3], a[40:43], v[52:55], v[0:3]\n"
    "ds_read_b128 v[32:35], v190 offset:12288\n"
    "ds_read_b128 v[36:39], v190 offset:13312\n"
    "v_mov_b32 v218, v216\n"
    "v_mfma_f32_16x16x32_bf16 v[0:3], a[48:51], v[56:59], v[0:3]\n"
    "v_exp_f32 v19, v11\n"
    "v_mov_b32 v219, v217\n"
    "v_mfma_f32_16x16x32_bf16 v[0:3], a[56:59], v[60:63], v[0:3]\n"
    "v_exp_f32 v20, v12\n"
    "s_nop 1\n"
    "v_mfma_f32_16x16x32_bf16 v[4:7], a[36:39], v[48:51], v[4:7]\n"
    "v_exp_f32 v21, v13\n"
    "v_mfma_f32_16x16x32_bf16 v[4:7], a[44:47], v[52:55], v[4:7]\n"
    "ds_read_b128 v[40:43], v190 offset:14336\n"
    "ds_read_b128 v[44:47], v190 offset:15360\n"
    "buffer_load_dwordx4 a[196:199], v97, s[76:79], s68 offen offset:1024 sc1\n"
    "v_mfma_f32_16x16x32_bf16 v[4:7], a[52:55], v[56:59], v[4:7]\n"
    "v_exp_f32 v22, v14\n"
    "v_exp_f32 v23, v15\n"
    "v_pk_fma_f32 v[16:17], v[16:17], s[60:61], s[60:61]\n"
    "v_pk_fma_f32 v[18:19], v[18:19], s[60:61], s[60:61]\n"
    "v_pk_fma_f32 v[20:21], v[20:21], s[60:61], s[60:61]\n"
    "v_pk_fma_f32 v[22:23], v[22:23], s[60:61], s[60:61]\n"
    "v_pk_fma_f32 v[16:17], v[16:17], v[16:17], 0.5 op_sel_hi:[1,1,0]\n"
    "v_pk_fma_f32 v[18:19], v[18:19], v[18:19], 0.5 op_sel_hi:[1,1,0]\n"
    "v_pk_fma_f32 v[20:21], v[20:21], v[20:21], 0.5 op_sel_hi:[1,1,0]\n"
    "v_pk_fma_f32 v[22:23], v[22:23], v[22:23], 0.5 op_sel_hi:[1,1,0]\n"
    "v_mfma_f32_16x16x32_bf16 v[4:7], a[60:63], v[60:63], v[4:7]\n"
    "s_waitcnt lgkmcnt(8)\n"
    "v_rcp_f32 v16, v16\n"
    "v_permlane16_swap_b32 v216, v218\n"
    "v_mfma_f32_16x16x32_bf16 v[0:3], a[128:131], v[64:67], v[0:3]\n"
    "v_rcp_f32 v17, v17\n"
    "v_mfma_f32_16x16x32_bf16 v[0:3], a[136:139], v[68:71], v[0:3]\n"
    "ds_read_b128 v[48:51], v190 offset:28672\n"
    "ds_read_b128 v[52:55], v190 offset:29696\n"
    "v_mfma_f32_16x16x32_bf16 v[0:3], a[144:147], v[72:75], v[0:3]\n"
    "buffer_load_dwordx4 a[200:203], v98, s[76:79], s68 offen offset:0 sc1\n"
    "v_rcp_f32 v18, v18\n"
    "v_mfma_f32_16x16x32_bf16 v[0:3], a[152:155], v[76:79], v[0:3]\n"
    "v_rcp_f32 v19, v19\n"
    "v_permlane16_swap_b32 v217, v219\n"
    "v_mfma_f32_16x16x32_bf16 v[4:7], a[132:135], v[64:67], v[4:7]\n"
    "v_rcp_f32 v20, v20\n"
    "v_add_f32 v216, v216, v218\n"
    "v_mfma_f32_16x16x32_bf16 v[4:7], a[140:143], v[68:71], v[4:7]\n"
    "ds_read_b128 v[56:59], v190 offset:30720\n"
    "ds_read_b128 v[60:63], v190 offset:31744\n"
    "v_add_f32 v217, v217, v219\n"
    "v_mfma_f32_16x16x32_bf16 v[4:7], a[148:151], v[72:75], v[4:7]\n"
    "v_rcp_f32 v21, v21\n"
    "v_mul_f32 v216, 0x3d800000, v216\n"
    "v_mfma_f32_16x16x32_bf16 v[4:7], a[156:159], v[76:79], v[4:7]\n"
    "s_waitcnt lgkmcnt(8)\n"
    "buffer_load_dwordx4 a[204:207], v98, s[76:79], s68 offen offset:1024 sc1\n"
    "v_mfma_f32_16x16x32_bf16 v[0:3], a[160:163], v[80:83], v[0:3]\n"
    "v_rcp_f32 v22, v22\n"
    "v_rcp_f32 v23, v23\n"
    "v_pk_fma_f32 v[8:9], v[8:9], v[16:17], v[8:9] neg_lo:[1,0,0] neg_hi:[1,0,0]\n"
    "v_pk_fma_f32 v[10:11], v[10:11], v[18:19], v[10:11] neg_lo:[1,0,0] neg_hi:[1,0,0]\n"
    "v_pk_fma_f32 v[12:13], v[12:13], v[20:21], v[12:13] neg_lo:[1,0,0] neg_hi:[1,0,0]\n"
    "v_pk_fma_f32 v[14:15], v[14:15], v[22:23], v[14:15] neg_lo:[1,0,0] neg_hi:[1,0,0]\n"
    "v_mfma_f32_16x16x32_bf16 v[0:3], a[168:171], v[84:87], v[0:3]\n"
    "ds_read_b128 v[64:67], v190 offset:45056\n"
    "ds_read_b128 v[68:71], v190 offset:46080\n"
    "v_sub_f32 v208, v208, v216\n"
    "v_mfma_f32_16x16x32_bf16 v[0:3], a[176:179], v[88:91], v[0:3]\n"
    "v_cvt_pk_bf16_f32 v8, v8, v9\n"
    "v_cvt_pk_bf16_f32 v9, v10, v11\n"
    "v_sub_f32 v210, v210, v216\n"
    "v_mfma_f32_16x16x32_bf16 v[0:3], a[184:187], v[92:95], v[0:3]\n"
    "v_cvt_pk_bf16_f32 v10, v12, v13\n"
    "v_cvt_pk_bf16_f32 v11, v14, v15\n"
    "v_mfma_f32_16x16x32_bf16 v[4:7], a[164:167], v[80:83], v[4:7]\n"
    "s_add_u32 s88, s28, 57344\n"
    "buffer_store_dwordx4 v[8:11], v190, s[76:79], s88 offen sc1\n"
    "v_mfma_f32_16x16x32_bf16 v[4:7], a[172:175], v[84:87], v[4:7]\n"
    "ds_read_b128 v[72:75], v190 offset:47104\n"
    "ds_read_b128 v[76:79], v190 offset:48128\n"
    "v_sub_f32 v212, v212, v216\n"
    "v_mfma_f32_16x16x32_bf16 v[4:7], a[180:183], v[88:91], v[4:7]\n"
    "s_add_u32 s68, s83, 20480\n"
    "buffer_load_dwordx4 a[208:211], v97, s[76:79], s68 offen offset:0 sc1\n"
    "v_mfma_f32_16x16x32_bf16 v[4:7], a[188:191], v[92:95], v[4:7]\n"
    "s_waitcnt lgkmcnt(8)\n"
    "v_sub_f32 v214, v214, v216\n"
    "v_mul_f32 v209, v208, v208\n"
    "v_fmac_f32 v209, v210, v210\n"
    "v_mfma_f32_16x16x32_bf16 v[8:11], a[0:3], v[32:35], 0\n"
    "v_fmac_f32 v209, v212, v212\n"
    "v_fmac_f32 v209, v214, v214\n"
    "v_mov_b32 v218, v209\n"
    "v_mfma_f32_16x16x32_bf16 v[8:11], a[8:11], v[36:39], v[8:11]\n"
    "ds_read_b128 v[80:83], v190 offset:61440\n"
    "ds_read_b128 v[84:87], v190 offset:62464\n"
    "s_nop 1\n"
    "v_mfma_f32_16x16x32_bf16 v[8:11], a[16:19], v[40:43], v[8:11]\n"
    "buffer_load_dwordx4 a[212:215], v97, s[76:79], s68 offen offset:1024 sc1\n"
    "v_mfma_f32_16x16x32_bf16 v[8:11], a[24:27], v[44:47], v[8:11]\n"
    "v_permlane32_swap_b32 v209, v218\n"
    "v_add_f32 v209, v209, v218\n"
    "v_mfma_f32_16x16x32_bf16 v[12:15], a[4:7], v[32:35], 0\n"
    "v_mov_b32 v218, v209\n"
    "s_nop 1\n"
    "v_mfma_f32_16x16x32_bf16 v[12:15], a[12:15], v[36:39], v[12:15]\n"
    "ds_read_b128 v[88:91], v190 offset:63488\n"
    "ds_read_b128 v[92:95], v190 offset:64512\n"
    "v_pk_fma_f32 v[0:1], v[192:193], v[244:245], v[0:1] op_sel_hi:[1,0,1] neg_lo:[0,1,0] neg_hi:[0,1,0]\n"
    "v_pk_fma_f32 v[2:3], v[194:195], v[244:245], v[2:3] op_sel_hi:[1,0,1] neg_lo:[0,1,0] neg_hi:[0,1,0]\n"
    "v_pk_fma_f32 v[4:5], v[196:197], v[244:245], v[4:5] op_sel_hi:[1,0,1] neg_lo:[0,1,0] neg_hi:[0,1,0]\n"
    "v_pk_fma_f32 v[6:7], v[198:199], v[244:245], v[6:7] op_sel_hi:[1,0,1] neg_lo:[0,1,0] neg_hi:[0,1,0]\n"
    "v_pk_fma_f32 v[0:1], v[0:1], v[244:245], v[200:201] op_sel:[0,1,0]\n"
    "v_pk_fma_f32 v[2:3], v[2:3], v[244:245], v[202:203] op_sel:[0,1,0]\n"
    "v_pk_fma_f32 v[4:5], v[4:5], v[244:245], v[204:205] op_sel:[0,1,0]\n"
    "v_pk_fma_f32 v[6:7], v[6:7], v[244:245], v[206:207] op_sel:[0,1,0]\n"
    "v_mfma_f32_16x16x32_bf16 v[12:15], a[20:23], v[40:43], v[12:15]\n"
    "buffer_load_dwordx4 a[216:219], v98, s[76:79], s68 offen offset:0 sc1\n"
    "v_exp_f32 v16, v0\n"
    "v_mfma_f32_16x16x32_bf16 v[12:15], a[28:31], v[44:47], v[12:15]\n"
    "s_waitcnt lgkmcnt(8)\n"
    "v_exp_f32 v17, v1\n"
    "v_permlane16_swap_b32 v209, v218\n"
    "v_mfma_f32_16x16x32_bf16 v[8:11], a[32:35], v[48:51], v[8:11]\n"
    "v_exp_f32 v18, v2\n"
    "v_mfma_f32_16x16x32_bf16 v[8:11], a[40:43], v[52:55], v[8:11]\n"
    "v_add_f32 v209, v209, v218\n"
    "v_fmamk_f32 v217, v209, 0x42000000, v217\n"
    "v_mov_b32 v218, 0x3727c5ac\n"
    "v_mfma_f32_16x16x32_bf16 v[8:11], a[48:51], v[56:59], v[8:11]\n"
    "buffer_load_dwordx4 a[220:223], v98, s[76:79], s68 offen offset:1024 sc1\n"
    "v_exp_f32 v19, v3\n"
    "v_mfma_f32_16x16x32_bf16 v[8:11], a[56:59], v[60:63], v[8:11]\n"
    "v_exp_f32 v20, v4\n"
    "v_fmamk_f32 v217, v217, 0x3b000000, v218\n"
    "v_mfma_f32_16x16x32_bf16 v[12:15], a[36:39], v[48:51], v[12:15]\n"
    "v_exp_f32 v21, v5\n"
    "v_mfma_f32_16x16x32_bf16 v[12:15], a[44:47], v[52:55], v[12:15]\n"
    "v_rsq_f32 v217, v217\n"
    "v_add_u32 v218, s57, v177\n"
    "v_mul_f32 v217, 0x3fb8aa3b, v217\n"
    "v_mfma_f32_16x16x32_bf16 v[12:15], a[52:55], v[56:59], v[12:15]\n"
    "v_exp_f32 v22, v6\n"
    "v_exp_f32 v23, v7\n"
    "v_pk_fma_f32 v[16:17], v[16:17], s[60:61], s[60:61]\n"
    "v_pk_fma_f32 v[18:19], v[18:19], s[60:61], s[60:61]\n"
    "v_pk_fma_f32 v[20:21], v[20:21], s[60:61], s[60:61]\n"
    "v_pk_fma_f32 v[22:23], v[22:23], s[60:61], s[60:61]\n"
    "v_pk_fma_f32 v[16:17], v[16:17], v[16:17], 0.5 op_sel_hi:[1,1,0]\n"
    "v_pk_fma_f32 v[18:19], v[18:19], v[18:19], 0.5 op_sel_hi:[1,1,0]\n"
    "v_pk_fma_f32 v[20:21], v[20:21], v[20:21], 0.5 op_sel_hi:[1,1,0]\n"
    "v_pk_fma_f32 v[22:23], v[22:23], v[22:23], 0.5 op_sel_hi:[1,1,0]\n"
    "v_mfma_f32_16x16x32_bf16 v[12:15], a[60:63], v[60:63], v[12:15]\n"
    "s_waitcnt lgkmcnt(4)\n"
    "s_add_u32 s68, s83, 24576\n"
    "buffer_load_dwordx4 a[224:227], v97, s[76:79], s68 offen offset:0 sc1\n"
    "v_rcp_f32 v16, v16\n"
    "ds_write_b64 v218, v[216:217] offset:128\n"
    "s_waitcnt vmcnt(11) lgkmcnt(0)\n"
    "s_barrier\n"
    "v_mfma_f32_16x16x32_bf16 v[8:11], a[128:131], v[64:67], v[8:11]\n"
    "ds_read_b128 v[32:35], v176 offset:0\n"
    "ds_read_b128 v[36:39], v176 offset:1024\n"
    "v_rcp_f32 v17, v17\n"
    "v_mfma_f32_16x16x32_bf16 v[8:11], a[136:139], v[68:71], v[8:11]\n"
    "ds_read_b128 v[48:51], v176 offset:16384\n"
    "ds_read_b128 v[52:55], v176 offset:17408\n"
    "v_mfma_f32_16x16x32_bf16 v[8:11], a[144:147], v[72:75], v[8:11]\n"
    "ds_read_b128 v[40:43], v176 offset:2048\n"
    "ds_read_b128 v[44:47], v176 offset:3072\n"
    "v_rcp_f32 v18, v18\n"
    "v_mfma_f32_16x16x32_bf16 v[8:11], a[152:155], v[76:79], v[8:11]\n"
    "v_rcp_f32 v19, v19\n"
    "v_mfma_f32_16x16x32_bf16 v[12:15], a[132:135], v[64:67], v[12:15]\n"
    "buffer_load_dwordx4 a[228:231], v97, s[76:79], s68 offen offset:1024 sc1\n"
    "v_rcp_f32 v20, v20\n"
    "v_mfma_f32_16x16x32_bf16 v[12:15], a[140:143], v[68:71], v[12:15]\n"
    "ds_read_b128 v[56:59], v176 offset:18432\n"
    ".p2align 12\n"
    "L_pad13:\n"
    "s_cbranch_execz L_walked\n"
    "ds_read_b128 v[60:63], v176 offset:19456\n"
    "v_mfma_f32_16x16x32_bf16 v[12:15], a[148:151], v[72:75], v[12:15]\n"
    "v_rcp_f32 v21, v21\n"
    "v_mfma_f32_16x16x32_bf16 v[12:15], a[156:159], v[76:79], v[12:15]\n"
    "v_mfma_f32_16x16x32_bf16 v[8:11], a[160:163], v[80:83], v[8:11]\n"
    "v_rcp_f32 v22, v22\n"
    "v_rcp_f32 v23, v23\n"
    "v_pk_fma_f32 v[0:1], v[0:1], v[16:17], v[0:1] neg_lo:[1,0,0] neg_hi:[1,0,0]\n"
    "v_pk_fma_f32 v[2:3], v[2:3], v[18:19], v[2:3] neg_lo:[1,0,0] neg_hi:[1,0,0]\n"
    "v_pk_fma_f32 v[4:5], v[4:5], v[20:21], v[4:5] neg_lo:[1,0,0] neg_hi:[1,0,0]\n"
    "v_pk_fma_f32 v[6:7], v[6:7], v[22:23], v[6:7] neg_lo:[1,0,0] neg_hi:[1,0,0]\n"
    "v_mfma_f32_16x16x32_bf16 v[8:11], a[168:171], v[84:87], v[8:11]\n"
    "ds_read_b128 v[64:67], v176 offset:32768\n"
    "ds_read_b128 v[68:71], v176 offset:33792\n"
    "buffer_load_dwordx4 a[232:235], v98, s[76:79], s68 offen offset:0 sc1\n"
    "v_mfma_f32_16x16x32_bf16 v[8:11], a[176:179], v[88:91], v[8:11]\n"
    "v_cvt_pk_bf16_f32 v0, v0, v1\n"
    "v_cvt_pk_bf16_f32 v1, v2, v3\n"
    "v_mfma_f32_16x16x32_bf16 v[8:11], a[184:187], v[92:95], v[8:11]\n"
    "v_cvt_pk_bf16_f32 v2, v4, v5\n"
    "v_cvt_pk_bf16_f32 v3, v6, v7\n"
    "v_mfma_f32_16x16x32_bf16 v[12:15], a[164:167], v[80:83], v[12:15]\n"
    "s_add_u32 s88, s28, 229376\n"
    "buffer_store_dwordx4 v[0:3], v190, s[76:79], s88 offen sc1\n"
    "v_mfma_f32_16x16x32_bf16 v[12:15], a[172:175], v[84:87], v[12:15]\n"
    "ds_read_b128 v[72:75], v176 offset:34816\n"
    "ds_read_b128 v[76:79], v176 offset:35840\n"
    "v_mfma_f32_16x16x32_bf16 v[12:15], a[180:183], v[88:91], v[12:15]\n"
    "buffer_load_dwordx4 a[236:239], v98, s[76:79], s68 offen offset:1024 sc1\n"
    "v_mfma_f32_16x16x32_bf16 v[12:15], a[188:191], v[92:95], v[12:15]\n"
    "s_waitcnt lgkmcnt(6)\n"
    "v_mfma_f32_16x16x32_bf16 v[0:3], a[0:3], v[32:35], 0\n"
    "v_mfma_f32_16x16x32_bf16 v[0:3], a[8:11], v[36:39], v[0:3]\n"
    "ds_read_b128 v[80:83], v176 offset:49152\n"
    "ds_read_b128 v[84:87], v176 offset:50176\n"
    "v_mfma_f32_16x16x32_bf16 v[0:3], a[16:19], v[40:43], v[0:3]\n"
    "v_mfma_f32_16x16x32_bf16 v[0:3], a[24:27], v[44:47], v[0:3]\n"
    "s_add_u32 s68, s83, 28672\n"
    "buffer_load_dwordx4 a[240:243], v97, s[76:79], s68 offen offset:0 sc1\n"
    "v_mfma_f32_16x16x32_bf16 v[4:7], a[4:7], v[32:35], 0\n"
    "v_mfma_f32_16x16x32_bf16 v[4:7], a[12:15], v[36:39], v[4:7]\n"
    "ds_read_b128 v[88:91], v176 offset:51200\n"
    "ds_read_b128 v[92:95], v176 offset:52224\n"
    "ds_read_b64 v[248:249], v177 offset:128\n"
    "ds_read_b64 v[250:251], v177 offset:4224\n"
    "ds_read_b64 v[252:253], v177 offset:8320\n"
    "ds_read_b64 v[254:255], v177 offset:12416\n"
    "v_pk_fma_f32 v[8:9], v[192:193], v[246:247], v[8:9] op_sel_hi:[1,0,1] neg_lo:[0,1,0] neg_hi:[0,1,0]\n"
    "v_pk_fma_f32 v[10:11], v[194:195], v[246:247], v[10:11] op_sel_hi:[1,0,1] neg_lo:[0,1,0] neg_hi:[0,1,0]\n"
    "v_pk_fma_f32 v[12:13], v[196:197], v[246:247], v[12:13] op_sel_hi:[1,0,1] neg_lo:[0,1,0] neg_hi:[0,1,0]\n"
    "v_pk_fma_f32 v[14:15], v[198:199], v[246:247], v[14:15] op_sel_hi:[1,0,1] neg_lo:[0,1,0] neg_hi:[0,1,0]\n"
    "v_pk_fma_f32 v[8:9], v[8:9], v[246:247], v[200:201] op_sel:[0,1,0]\n"
    "v_pk_fma_f32 v[10:11], v[10:11], v[246:247], v[202:203] op_sel:[0,1,0]\n"
    "v_pk_fma_f32 v[12:13], v[12:13], v[246:247], v[204:205] op_sel:[0,1,0]\n"
    "v_pk_fma_f32 v[14:15], v[14:15], v[246:247], v[206:207] op_sel:[0,1,0]\n"
    "v_mfma_f32_16x16x32_bf16 v[4:7], a[20:23], v[40:43], v[4:7]\n"
    "v_exp_f32 v16, v8\n"
    "v_mfma_f32_16x16x32_bf16 v[4:7], a[28:31], v[44:47], v[4:7]\n"
    "s_waitcnt lgkmcnt(12)\n"
    "v_exp_f32 v17, v9\n"
    "v_mfma_f32_16x16x32_bf16 v[0:3], a[32:35], v[48:51], v[0:3]\n"
    "buffer_load_dwordx4 a[244:247], v97, s[76:79], s68 offen offset:1024 sc1\n"
    "v_exp_f32 v18, v10\n"
    "v_mfma_f32_16x16x32_bf16 v[0:3], a[40:43], v[52:55], v[0:3]\n"
    "ds_read_b128 v[32:35], v176 offset:4096\n"
    "ds_read_b128 v[36:39], v176 offset:5120\n"
    "v_mfma_f32_16x16x32_bf16 v[0:3], a[48:51], v[56:59], v[0:3]\n"
    "v_exp_f32 v19, v11\n"
    "v_mfma_f32_16x16x32_bf16 v[0:3], a[56:59], v[60:63], v[0:3]\n"
    "v_exp_f32 v20, v12\n"
    "v_mfma_f32_16x16x32_bf16 v[4:7], a[36:39], v[48:51], v[4:7]\n"
    "buffer_load_dwordx4 a[248:251], v98, s[76:79], s68 offen offset:0 sc1\n"
    "v_exp_f32 v21, v13\n"
    "v_mfma_f32_16x16x32_bf16 v[4:7], a[44:47], v[52:55], v[4:7]\n"
    "ds_read_b128 v[40:43], v176 offset:6144\n"
    "ds_read_b128 v[44:47], v176 offset:7168\n"
    "v_mfma_f32_16x16x32_bf16 v[4:7], a[52:55], v[56:59], v[4:7]\n"
    "v_exp_f32 v22, v14\n"
    "v_exp_f32 v23, v15\n"
    "v_pk_fma_f32 v[16:17], v[16:17], s[60:61], s[60:61]\n"
    "v_pk_fma_f32 v[18:19], v[18:19], s[60:61], s[60:61]\n"
    "v_pk_fma_f32 v[20:21], v[20:21], s[60:61], s[60:61]\n"
    "v_pk_fma_f32 v[22:23], v[22:23], s[60:61], s[60:61]\n"
    "v_pk_fma_f32 v[16:17], v[16:17], v[16:17], 0.5 op_sel_hi:[1,1,0]\n"
    "v_pk_fma_f32 v[18:19], v[18:19], v[18:19], 0.5 op_sel_hi:[1,1,0]\n"
    "v_pk_fma_f32 v[20:21], v[20:21], v[20:21], 0.5 op_sel_hi:[1,1,0]\n"
    "v_pk_fma_f32 v[22:23], v[22:23], v[22:23], 0.5 op_sel_hi:[1,1,0]\n"
    "v_mfma_f32_16x16x32_bf16 v[4:7], a[60:63], v[60:63], v[4:7]\n"
    "s_waitcnt lgkmcnt(12)\n"
    "v_rcp_f32 v16, v16\n"
    "v_mfma_f32_16x16x32_bf16 v[0:3], a[128:131], v[64:67], v[0:3]\n"
    "v_rcp_f32 v17, v17\n"
    "v_mfma_f32_16x16x32_bf16 v[0:3], a[136:139], v[68:71], v[0:3]\n"
    "ds_read_b128 v[48:51], v176 offset:20480\n"
    "ds_read_b128 v[52:55], v176 offset:21504\n"
    "buffer_load_dwordx4 a[252:255], v98, s[76:79], s68 offen offset:1024 sc1\n"
    "v_mfma_f32_16x16x32_bf16 v[0:3], a[144:147], v[72:75], v[0:3]\n"
    "v_rcp_f32 v18, v18\n"
    "v_mfma_f32_16x16x32_bf16 v[0:3], a[152:155], v[76:79], v[0:3]\n"
    "v_rcp_f32 v19, v19\n"
    "v_mfma_f32_16x16x32_bf16 v[4:7], a[132:135], v[64:67], v[4:7]\n"
    "v_rcp_f32 v20, v20\n"
    "v_mfma_f32_16x16x32_bf16 v[4:7], a[140:143], v[68:71], v[4:7]\n"
    "ds_read_b128 v[56:59], v176 offset:22528\n"
    "ds_read_b128 v[60:63], v176 offset:23552\n"
    "v_mfma_f32_16x16x32_bf16 v[4:7], a[148:151], v[72:75], v[4:7]\n"
    "v_rcp_f32 v21, v21\n"
    "v_mfma_f32_16x16x32_bf16 v[4:7], a[156:159], v[76:79], v[4:7]\n"
    "s_waitcnt lgkmcnt(12)\n"
    "s_add_u32 s68, s83, 32768\n"
    "buffer_load_dwordx4 v[112:115], v97, s[76:79], s68 offen offset:0 sc1\n"
    "v_mfma_f32_16x16x32_bf16 v[0:3], a[160:163], v[80:83], v[0:3]\n"
    "v_rcp_f32 v22, v22\n"
    "v_rcp_f32 v23, v23\n"
    "v_pk_fma_f32 v[8:9], v[8:9], v[16:17], v[8:9] neg_lo:[1,0,0] neg_hi:[1,0,0]\n"
    "v_pk_fma_f32 v[10:11], v[10:11], v[18:19], v[10:11] neg_lo:[1,0,0] neg_hi:[1,0,0]\n"
    "v_pk_fma_f32 v[12:13], v[12:13], v[20:21], v[12:13] neg_lo:[1,0,0] neg_hi:[1,0,0]\n"
    "v_pk_fma_f32 v[14:15], v[14:15], v[22:23], v[14:15] neg_lo:[1,0,0] neg_hi:[1,0,0]\n"
    "v_mfma_f32_16x16x32_bf16 v[0:3], a[168:171], v[84:87], v[0:3]\n"
    "ds_read_b128 v[64:67], v176 offset:36864\n"
    "ds_read_b128 v[68:71], v176 offset:37888\n"
    "v_mfma_f32_16x16x32_bf16 v[0:3], a[176:179], v[88:91], v[0:3]\n"
    "v_cvt_pk_bf16_f32 v8, v8, v9\n"
    "v_cvt_pk_bf16_f32 v9, v10, v11\n"
    "v_mfma_f32_16x16x32_bf16 v[0:3], a[184:187], v[92:95], v[0:3]\n"
    "buffer_load_dwordx4 v[116:119], v97, s[76:79], s68 offen offset:1024 sc1\n"
    "v_cvt_pk_bf16_f32 v10, v12, v13\n"
    "v_cvt_pk_bf16_f32 v11, v14, v15\n"
    "v_mfma_f32_16x16x32_bf16 v[4:7], a[164:167], v[80:83], v[4:7]\n"
    "s_add_u32 s88, s28, 286720\n"
    "buffer_store_dwordx4 v[8:11], v190, s[76:79], s88 offen sc1\n"
    "v_mfma_f32_16x16x32_bf16 v[4:7], a[172:175], v[84:87], v[4:7]\n"
    "ds_read_b128 v[72:75], v176 offset:38912\n"
    "ds_read_b128 v[76:79], v176 offset:39936\n"
    "v_mfma_f32_16x16x32_bf16 v[4:7], a[180:183], v[88:91], v[4:7]\n"
    "v_mfma_f32_16x16x32_bf16 v[4:7], a[188:191], v[92:95], v[4:7]\n"
    "s_waitcnt lgkmcnt(8)\n"
    "v_mfma_f32_16x16x32_bf16 v[8:11], a[0:3], v[32:35], 0\n"
    "buffer_load_dwordx4 v[120:123], v98, s[76:79], s68 offen offset:0 sc1\n"
    "v_mfma_f32_16x16x32_bf16 v[8:11], a[8:11], v[36:39], v[8:11]\n"
    "ds_read_b128 v[80:83], v176 offset:53248\n"
    "ds_read_b128 v[84:87], v176 offset:54272\n"
    "v_mfma_f32_16x16x32_bf16 v[8:11], a[16:19], v[40:43], v[8:11]\n"
    "v_mfma_f32_16x16x32_bf16 v[8:11], a[24:27], v[44:47], v[8:11]\n"
    "v_mfma_f32_16x16x32_bf16 v[12:15], a[4:7], v[32:35], 0\n"
    "v_mfma_f32_16x16x32_bf16 v[12:15], a[12:15], v[36:39], v[12:15]\n"
    "ds_read_b128 v[88:91], v176 offset:55296\n"
    "ds_read_b128 v[92:95], v176 offset:56320\n"
    "buffer_load_dwordx4 v[124:127], v98, s[76:79], s68 offen offset:1024 sc1\n"
    "v_pk_fma_f32 v[0:1], v[192:193], v[248:249], v[0:1] op_sel_hi:[1,0,1] neg_lo:[0,1,0] neg_hi:[0,1,0]\n"
    "v_pk_fma_f32 v[2:3], v[194:195], v[248:249], v[2:3] op_sel_hi:[1,0,1] neg_lo:[0,1,0] neg_hi:[0,1,0]\n"
    "v_pk_fma_f32 v[4:5], v[196:197], v[248:249], v[4:5] op_sel_hi:[1,0,1] neg_lo:[0,1,0] neg_hi:[0,1,0]\n"
    "v_pk_fma_f32 v[6:7], v[198:199], v[248:249], v[6:7] op_sel_hi:[1,0,1] neg_lo:[0,1,0] neg_hi:[0,1,0]\n"
    "v_pk_fma_f32 v[0:1], v[0:1], v[248:249], v[200:201] op_sel:[0,1,0]\n"
    "v_pk_fma_f32 v[2:3], v[2:3], v[248:249], v[202:203] op_sel:[0,1,0]\n"
    "v_pk_fma_f32 v[4:5], v[4:5], v[248:249], v[204:205] op_sel:[0,1,0]\n"
    "v_pk_fma_f32 v[6:7], v[6:7], v[248:249], v[206:207] op_sel:[0,1,0]\n"
    "v_mfma_f32_16x16x32_bf16 v[12:15], a[20:23], v[40:43], v[12:15]\n"
    "v_exp_f32 v16, v0\n"
    "v_mfma_f32_16x16x32_bf16 v[12:15], a[28:31], v[44:47], v[12:15]\n"
    "s_waitcnt lgkmcnt(8)\n"
    "v_exp_f32 v17, v1\n"
    "v_mfma_f32_16x16x32_bf16 v[8:11], a[32:35], v[48:51], v[8:11]\n"
    "v_exp_f32 v18, v2\n"
    "v_mfma_f32_16x16x32_bf16 v[8:11], a[40:43], v[52:55], v[8:11]\n"
    "ds_read_b128 v[32:35], v176 offset:8192\n"
    "ds_read_b128 v[36:39], v176 offset:9216\n"
    "v_mfma_f32_16x16x32_bf16 v[8:11], a[48:51], v[56:59], v[8:11]\n"
    "s_add_u32 s68, s83, 36864\n"
    "buffer_load_dwordx4 v[128:131], v97, s[76:79], s68 offen offset:0 sc1\n"
    "v_exp_f32 v19, v3\n"
    "v_mfma_f32_16x16x32_bf16 v[8:11], a[56:59], v[60:63], v[8:11]\n"
    "v_exp_f32 v20, v4\n"
    "v_mfma_f32_16x16x32_bf16 v[12:15], a[36:39], v[48:51], v[12:15]\n"
    "v_exp_f32 v21, v5\n"
    "v_mfma_f32_16x16x32_bf16 v[12:15], a[44:47], v[52:55], v[12:15]\n"
    "ds_read_b128 v[40:43], v176 offset:10240\n"
    "ds_read_b128 v[44:47], v176 offset:11264\n"
    "v_mfma_f32_16x16x32_bf16 v[12:15], a[52:55], v[56:59], v[12:15]\n"
    "v_exp_f32 v22, v6\n"
    "v_exp_f32 v23, v7\n"
    "v_pk_fma_f32 v[16:17], v[16:17], s[60:61], s[60:61]\n"
    "v_pk_fma_f32 v[18:19], v[18:19], s[60:61], s[60:61]\n"
    "v_pk_fma_f32 v[20:21], v[20:21], s[60:61], s[60:61]\n"
    "v_pk_fma_f32 v[22:23], v[22:23], s[60:61], s[60:61]\n"
    "v_pk_fma_f32 v[16:17], v[16:17], v[16:17], 0.5 op_sel_hi:[1,1,0]\n"
    "v_pk_fma_f32 v[18:19], v[18:19], v[18:19], 0.5 op_sel_hi:[1,1,0]\n"
    "v_pk_fma_f32 v[20:21], v[20:21], v[20:21], 0.5 op_sel_hi:[1,1,0]\n"
    "v_pk_fma_f32 v[22:23], v[22:23], v[22:23], 0.5 op_sel_hi:[1,1,0]\n"
    "v_mfma_f32_16x16x32_bf16 v[12:15], a[60:63], v[60:63], v[12:15]\n"
    "s_waitcnt lgkmcnt(8)\n"
    "buffer_load_dwordx4 v[132:135], v97, s[76:79], s68 offen offset:1024 sc1\n"
    "v_rcp_f32 v16, v16\n"
    "v_mfma_f32_16x16x32_bf16 v[8:11], a[128:131], v[64:67], v[8:11]\n"
    "v_rcp_f32 v17, v17\n"
    "v_mfma_f32_16x16x32_bf16 v[8:11], a[136:139], v[68:71], v[8:11]\n"
    "ds_read_b128 v[48:51], v176 offset:24576\n"
    "ds_read_b128 v[52:55], v176 offset:25600\n"
    "v_mfma_f32_16x16x32_bf16 v[8:11], a[144:147], v[72:75], v[8:11]\n"
    "v_rcp_f32 v18, v18\n"
    "v_mfma_f32_16x16x32_bf16 v[8:11], a[152:155], v[76:79], v[8:11]\n"
    "buffer_load_dwordx4 v[136:139], v98, s[76:79], s68 offen offset:0 sc1\n"
    "v_rcp_f32 v19, v19\n"
    "v_mfma_f32_16x16x32_bf16 v[12:15], a[132:135], v[64:67], v[12:15]\n"
    "v_rcp_f32 v20, v20\n"
    "v_mfma_f32_16x16x32_bf16 v[12:15], a[140:143], v[68:71], v[12:15]\n"
    "ds_read_b128 v[56:59], v176 offset:26624\n"
    "ds_read_b128 v[60:63], v176 offset:27648\n"
    "v_mfma_f32_16x16x32_bf16 v[12:15], a[148:151], v[72:75], v[12:15]\n"
    "v_rcp_f32 v21, v21\n"
    "v_mfma_f32_16x16x32_bf16 v[12:15], a[156:159], v[76:79], v[12:15]\n"
    "s_waitcnt lgkmcnt(8)\n"
    "v_mfma_f32_16x16x32_bf16 v[8:11], a[160:163], v[80:83], v[8:11]\n"
    "buffer_load_dwordx4 v[140:143], v98, s[76:79], s68 offen offset:1024 sc1\n"
    "v_rcp_f32 v22, v22\n"
    "v_rcp_f32 v23, v23\n"
    "v_pk_fma_f32 v[0:1], v[0:1], v[16:17], v[0:1] neg_lo:[1,0,0] neg_hi:[1,0,0]\n"
    "v_pk_fma_f32 v[2:3], v[2:3], v[18:19], v[2:3] neg_lo:[1,0,0] neg_hi:[1,0,0]\n"
    "v_pk_fma_f32 v[4:5], v[4:5], v[20:21], v[4:5] neg_lo:[1,0,0] neg_hi:[1,0,0]\n"
    "v_pk_fma_f32 v[6:7], v[6:7], v[22:23], v[6:7] neg_lo:[1,0,0] neg_hi:[1,0,0]\n"
    "v_mfma_f32_16x16x32_bf16 v[8:11], a[168:171], v[84:87], v[8:11]\n"
    "ds_read_b128 v[64:67], v176 offset:40960\n"
    "ds_read_b128 v[68:71], v176 offset:41984\n"
    "v_mfma_f32_16x16x32_bf16 v[8:11], a[176:179], v[88:91], v[8:11]\n"
    "v_cvt_pk_bf16_f32 v0, v0, v1\n"
    "v_cvt_pk_bf16_f32 v1, v2, v3\n"
    "v_mfma_f32_16x16x32_bf16 v[8:11], a[184:187], v[92:95], v[8:11]\n"
    "v_cvt_pk_bf16_f32 v2, v4, v5\n"
    "v_cvt_pk_bf16_f32 v3, v6, v7\n"
    "v_mfma_f32_16x16x32_bf16 v[12:15], a[164:167], v[80:83], v[12:15]\n"
    "s_add_u32 s88, s28, 114688\n"
    "buffer_store_dwordx4 v[0:3], v190, s[76:79], s88 offen sc1\n"
    "v_mfma_f32_16x16x32_bf16 v[12:15], a[172:175], v[84:87], v[12:15]\n"
    "ds_read_b128 v[72:75], v176 offset:43008\n"
    "ds_read_b128 v[76:79], v176 offset:44032\n"
    "v_mfma_f32_16x16x32_bf16 v[12:15], a[180:183], v[88:91], v[12:15]\n"
    "s_waitcnt vmcnt(7)\n"
    "s_lshl_b32 s5, s56, 5\n"
    "s_add_u32 s5, s5, 0x5800000\n"
    "buffer_store_dword v182, v180, s[76:79], s5 offen sc1\n"
    "s_add_u32 s56, s56, 0x1000\n"
    "v_mfma_f32_16x16x32_bf16 v[12:15], a[188:191], v[92:95], v[12:15]\n"
    "s_waitcnt lgkmcnt(8)\n"
    "s_add_u32 s68, s83, 40960\n"
    "buffer_load_dwordx4 v[144:147], v97, s[76:79], s68 offen offset:0 sc1\n"
    "v_mfma_f32_16x16x32_bf16 v[0:3], a[0:3], v[32:35], 0\n"
    "v_mfma_f32_16x16x32_bf16 v[0:3], a[8:11], v[36:39], v[0:3]\n"
    "ds_read_b128 v[80:83], v176 offset:57344\n"
    "ds_read_b128 v[84:87], v176 offset:58368\n"
    "v_mfma_f32_16x16x32_bf16 v[0:3], a[16:19], v[40:43], v[0:3]\n"
    "buffer_load_dwordx4 v[148:151], v97, s[76:79], s68 offen offset:1024 sc1\n"
    "v_mfma_f32_16x16x32_bf16 v[0:3], a[24:27], v[44:47], v[0:3]\n"
    "v_mfma_f32_16x16x32_bf16 v[4:7], a[4:7], v[32:35], 0\n"
    "v_mfma_f32_16x16x32_bf16 v[4:7], a[12:15], v[36:39], v[4:7]\n"
    "ds_read_b128 v[88:91], v176 offset:59392\n"
    "ds_read_b128 v[92:95], v176 offset:60416\n"
    "v_pk_fma_f32 v[8:9], v[192:193], v[250:251], v[8:9] op_sel_hi:[1,0,1] neg_lo:[0,1,0] neg_hi:[0,1,0]\n"
    "v_pk_fma_f32 v[10:11], v[194:195], v[250:251], v[10:11] op_sel_hi:[1,0,1] neg_lo:[0,1,0] neg_hi:[0,1,0]\n"
    "v_pk_fma_f32 v[12:13], v[196:197], v[250:251], v[12:13] op_sel_hi:[1,0,1] neg_lo:[0,1,0] neg_hi:[0,1,0]\n"
    "v_pk_fma_f32 v[14:15], v[198:199], v[250:251], v[14:15] op_sel_hi:[1,0,1] neg_lo:[0,1,0] neg_hi:[0,1,0]\n"
    "v_pk_fma_f32 v[8:9], v[8:9], v[250:251], v[200:201] op_sel:[0,1,0]\n"
    "v_pk_fma_f32 v[10:11], v[10:11], v[250:251], v[202:203] op_sel:[0,1,0]\n"
    "v_pk_fma_f32 v[12:13], v[12:13], v[250:251], v[204:205] op_sel:[0,1,0]\n"
    "v_pk_fma_f32 v[14:15], v[14:15], v[250:251], v[206:207] op_sel:[0,1,0]\n"
    "v_mfma_f32_16x16x32_bf16 v[4:7], a[20:23], v[40:43], v[4:7]\n"
    "v_exp_f32 v16, v8\n"
    "v_mfma_f32_16x16x32_bf16 v[4:7], a[28:31], v[44:47], v[4:7]\n"
    "s_waitcnt lgkmcnt(8)\n"
    "buffer_load_dwordx4 v[152:155], v98, s[76:79], s68 offen offset:0 sc1\n"
    "v_exp_f32 v17, v9\n"
    "v_mfma_f32_16x16x32_bf16 v[0:3], a[32:35], v[48:51], v[0:3]\n"
    "v_exp_f32 v18, v10\n"
    "v_mfma_f32_16x16x32_bf16 v[0:3], a[40:43], v[52:55], v[0:3]\n"
    "ds_read_b128 v[32:35], v176 offset:12288\n"
    "ds_read_b128 v[36:39], v176 offset:13312\n"
    "v_mfma_f32_16x16x32_bf16 v[0:3], a[48:51], v[56:59], v[0:3]\n"
    "v_exp_f32 v19, v11\n"
    "v_mfma_f32_16x16x32_bf16 v[0:3], a[56:59], v[60:63], v[0:3]\n"
    "v_exp_f32 v20, v12\n"
    "v_mfma_f32_16x16x32_bf16 v[4:7], a[36:39], v[48:51], v[4:7]\n"
    "buffer_load_dwordx4 v[156:159], v98, s[76:79], s68 offen offset:1024 sc1\n"
    "v_exp_f32 v21, v13\n"
    "v_mfma_f32_16x16x32_bf16 v[4:7], a[44:47], v[52:55], v[4:7]\n"
    "ds_read_b128 v[40:43], v176 offset:14336\n"
    "ds_read_b128 v[44:47], v176 offset:15360\n"
    "v_mfma_f32_16x16x32_bf16 v[4:7], a[52:55], v[56:59], v[4:7]\n"
    "v_exp_f32 v22, v14\n"
    "v_exp_f32 v23, v15\n"
    "v_pk_fma_f32 v[16:17], v[16:17], s[60:61], s[60:61]\n"
    "v_pk_fma_f32 v[18:19], v[18:19], s[60:61], s[60:61]\n"
    "v_pk_fma_f32 v[20:21], v[20:21], s[60:61], s[60:61]\n"
    "v_pk_fma_f32 v[22:23], v[22:23], s[60:61], s[60:61]\n"
    "v_pk_fma_f32 v[16:17], v[16:17], v[16:17], 0.5 op_sel_hi:[1,1,0]\n"
    "v_pk_fma_f32 v[18:19], v[18:19], v[18:19], 0.5 op_sel_hi:[1,1,0]\n"
    "v_pk_fma_f32 v[20:21], v[20:21], v[20:21], 0.5 op_sel_hi:[1,1,0]\n"
    "v_pk_fma_f32 v[22:23], v[22:23], v[22:23], 0.5 op_sel_hi:[1,1,0]\n"
    "v_mfma_f32_16x16x32_bf16 v[4:7], a[60:63], v[60:63], v[4:7]\n"
    "s_waitcnt lgkmcnt(8)\n"
    "v_rcp_f32 v16, v16\n"
    "v_mfma_f32_16x16x32_bf16 v[0:3], a[128:131], v[64:67], v[0:3]\n"
    "v_rcp_f32 v17, v17\n"
    "v_mfma_f32_16x16x32_bf16 v[0:3], a[136:139], v[68:71], v[0:3]\n"
    "s_add_u32 s68, s83, 45056\n"
    "ds_read_b128 v[48:51], v176 offset:28672\n"
    "ds_read_b128 v[52:55], v176 offset:29696\n"
    "buffer_load_dwordx4 v[160:163], v97, s[76:79], s68 offen offset:0 sc1\n"
    "v_mfma_f32_16x16x32_bf16 v[0:3], a[144:147], v[72:75], v[0:3]\n"
    "v_rcp_f32 v18, v18\n"
    "v_mfma_f32_16x16x32_bf16 v[0:3], a[152:155], v[76:79], v[0:3]\n"
    "v_rcp_f32 v19, v19\n"
    "v_mfma_f32_16x16x32_bf16 v[4:7], a[132:135], v[64:67], v[4:7]\n"
    "v_rcp_f32 v20, v20\n"
    "v_mfma_f32_16x16x32_bf16 v[4:7], a[140:143], v[68:71], v[4:7]\n"
    "ds_read_b128 v[56:59], v176 offset:30720\n"
    "ds_read_b128 v[60:63], v176 offset:31744\n"
    "v_mfma_f32_16x16x32_bf16 v[4:7], a[148:151], v[72:75], v[4:7]\n"
    "v_rcp_f32 v21, v21\n"
    "v_mfma_f32_16x16x32_bf16 v[4:7], a[156:159], v[76:79], v[4:7]\n"
    "s_waitcnt lgkmcnt(8)\n"
    "buffer_load_dwordx4 v[164:167], v97, s[76:79], s68 offen offset:1024 sc1\n"
    "v_mfma_f32_16x16x32_bf16 v[0:3], a[160:163], v[80:83], v[0:3]\n"
    "v_rcp_f32 v22, v22\n"
    "v_rcp_f32 v23, v23\n"
    "v_pk_fma_f32 v[8:9], v[8:9], v[16:17], v[8:9] neg_lo:[1,0,0] neg_hi:[1,0,0]\n"
    "v_pk_fma_f32 v[10:11], v[10:11], v[18:19], v[10:11] neg_lo:[1,0,0] neg_hi:[1,0,0]\n"
    "v_pk_fma_f32 v[12:13], v[12:13], v[20:21], v[12:13] neg_lo:[1,0,0] neg_hi:[1,0,0]\n"
    "v_pk_fma_f32 v[14:15], v[14:15], v[22:23], v[14:15] neg_lo:[1,0,0] neg_hi:[1,0,0]\n"
    "v_mfma_f32_16x16x32_bf16 v[0:3], a[168:171], v[84:87], v[0:3]\n"
    "ds_read_b128 v[64:67], v176 offset:45056\n"
    "ds_read_b128 v[68:71], v176 offset:46080\n"
    "v_mfma_f32_16x16x32_bf16 v[0:3], a[176:179], v[88:91], v[0:3]\n"
    "buffer_load_dwordx4 v[168:171], v98, s[76:79], s68 offen offset:0 sc1\n"
    "v_cvt_pk_bf16_f32 v8, v8, v9\n"
    "v_cvt_pk_bf16_f32 v9, v10, v11\n"
    "v_mfma_f32_16x16x32_bf16 v[0:3], a[184:187], v[92:95], v[0:3]\n"
    "v_cvt_pk_bf16_f32 v10, v12, v13\n"
    "v_cvt_pk_bf16_f32 v11, v14, v15\n"
    "v_mfma_f32_16x16x32_bf16 v[4:7], a[164:167], v[80:83], v[4:7]\n"
    "s_add_u32 s88, s28, 172032\n"
    "buffer_store_dwordx4 v[8:11], v190, s[76:79], s88 offen sc1\n"
    "v_mfma_f32_16x16x32_bf16 v[4:7], a[172:175], v[84:87], v[4:7]\n"
    "ds_read_b128 v[72:75], v176 offset:47104\n"
    "ds_read_b128 v[76:79], v176 offset:48128\n"
    "v_mfma_f32_16x16x32_bf16 v[4:7], a[180:183], v[88:91], v[4:7]\n"
    "v_mfma_f32_16x16x32_bf16 v[4:7], a[188:191], v[92:95], v[4:7]\n"
    "s_waitcnt lgkmcnt(8)\n"
    "buffer_load_dwordx4 v[172:175], v98, s[76:79], s68 offen offset:1024 sc1\n"
    "v_mfma_f32_16x16x32_bf16 v[8:11], a[0:3], v[32:35], 0\n"
    "v_mfma_f32_16x16x32_bf16 v[8:11], a[8:11], v[36:39], v[8:11]\n"
    "ds_read_b128 v[80:83], v176 offset:61440\n"
    "ds_read_b128 v[84:87], v176 offset:62464\n"
    "v_mfma_f32_16x16x32_bf16 v[8:11], a[16:19], v[40:43], v[8:11]\n"
    "s_lshl_b32 s4, s26, 5\n"
    "s_add_u32 s4, s4, 0x5800000\n"
    "buffer_load_dword v110, v178, s[76:79], s4 offen sc1\n"
    "v_mfma_f32_16x16x32_bf16 v[8:11], a[24:27], v[44:47], v[8:11]\n"
    "v_mfma_f32_16x16x32_bf16 v[12:15], a[4:7], v[32:35], 0\n"
    "s_add_u32 s68, s83, 49152\n"
    "buffer_load_dwordx4 v[208:211], v97, s[76:79], s68 offen offset:0 sc1\n"
    "v_mfma_f32_16x16x32_bf16 v[12:15], a[12:15], v[36:39], v[12:15]\n"
    "ds_read_b128 v[88:91], v176 offset:63488\n"
    "ds_read_b128 v[92:95], v176 offset:64512\n"
    "v_pk_fma_f32 v[0:1], v[192:193], v[252:253], v[0:1] op_sel_hi:[1,0,1] neg_lo:[0,1,0] neg_hi:[0,1,0]\n"
    "v_pk_fma_f32 v[2:3], v[194:195], v[252:253], v[2:3] op_sel_hi:[1,0,1] neg_lo:[0,1,0] neg_hi:[0,1,0]\n"
    "v_pk_fma_f32 v[4:5], v[196:197], v[252:253], v[4:5] op_sel_hi:[1,0,1] neg_lo:[0,1,0] neg_hi:[0,1,0]\n"
    "v_pk_fma_f32 v[6:7], v[198:199], v[252:253], v[6:7] op_sel_hi:[1,0,1] neg_lo:[0,1,0] neg_hi:[0,1,0]\n"
    "v_pk_fma_f32 v[0:1], v[0:1], v[252:253], v[200:201] op_sel:[0,1,0]\n"
    "v_pk_fma_f32 v[2:3], v[2:3], v[252:253], v[202:203] op_sel:[0,1,0]\n"
    "v_pk_fma_f32 v[4:5], v[4:5], v[252:253], v[204:205] op_sel:[0,1,0]\n"
    "v_pk_fma_f32 v[6:7], v[6:7], v[252:253], v[206:207] op_sel:[0,1,0]\n"
    "v_mfma_f32_16x16x32_bf16 v[12:15], a[20:23], v[40:43], v[12:15]\n"
    "v_exp_f32 v16, v0\n"
    "v_mfma_f32_16x16x32_bf16 v[12:15], a[28:31], v[44:47], v[12:15]\n"
    "s_waitcnt lgkmcnt(8)\n"
    "v_exp_f32 v17, v1\n"
    "v_mfma_f32_16x16x32_bf16 v[8:11], a[32:35], v[48:51], v[8:11]\n"
    "v_exp_f32 v18, v2\n"
    "v_mfma_f32_16x16x32_bf16 v[8:11], a[40:43], v[52:55], v[8:11]\n"
    "buffer_load_dwordx4 v[212:215], v97, s[76:79], s68 offen offset:1024 sc1\n"
    "v_mfma_f32_16x16x32_bf16 v[8:11], a[48:51], v[56:59], v[8:11]\n"
    "v_exp_f32 v19, v3\n"
    "v_mfma_f32_16x16x32_bf16 v[8:11], a[56:59], v[60:63], v[8:11]\n"
    "v_exp_f32 v20, v4\n"
    "v_mfma_f32_16x16x32_bf16 v[12:15], a[36:39], v[48:51], v[12:15]\n"
    "v_exp_f32 v21, v5\n"
    "v_mfma_f32_16x16x32_bf16 v[12:15], a[44:47], v[52:55], v[12:15]\n"
    "v_mfma_f32_16x16x32_bf16 v[12:15], a[52:55], v[56:59], v[12:15]\n"
    "buffer_load_dwordx4 v[216:219], v98, s[76:79], s68 offen offset:0 sc1\n"
    "v_exp_f32 v22, v6\n"
    "v_exp_f32 v23, v7\n"
    "v_pk_fma_f32 v[16:17], v[16:17], s[60:61], s[60:61]\n"
    "v_pk_fma_f32 v[18:19], v[18:19], s[60:61], s[60:61]\n"
    "v_pk_fma_f32 v[20:21], v[20:21], s[60:61], s[60:61]\n"
    "v_pk_fma_f32 v[22:23], v[22:23], s[60:61], s[60:61]\n"
    "v_pk_fma_f32 v[16:17], v[16:17], v[16:17], 0.5 op_sel_hi:[1,1,0]\n"
    "v_pk_fma_f32 v[18:19], v[18:19], v[18:19], 0.5 op_sel_hi:[1,1,0]\n"
    "v_pk_fma_f32 v[20:21], v[20:21], v[20:21], 0.5 op_sel_hi:[1,1,0]\n"
    "v_pk_fma_f32 v[22:23], v[22:23], v[22:23], 0.5 op_sel_hi:[1,1,0]\n"
    "v_mfma_f32_16x16x32_bf16 v[12:15], a[60:63], v[60:63], v[12:15]\n"
    "s_waitcnt lgkmcnt(4)\n"
    "v_rcp_f32 v16, v16\n"
    "v_mfma_f32_16x16x32_bf16 v[8:11], a[128:131], v[64:67], v[8:11]\n"
    "v_rcp_f32 v17, v17\n"
    "v_mfma_f32_16x16x32_bf16 v[8:11], a[136:139], v[68:71], v[8:11]\n"
    "v_mfma_f32_16x16x32_bf16 v[8:11], a[144:147], v[72:75], v[8:11]\n"
    "v_rcp_f32 v18, v18\n"
    "v_mfma_f32_16x16x32_bf16 v[8:11], a[152:155], v[76:79], v[8:11]\n"
    "v_rcp_f32 v19, v19\n"
    "v_mfma_f32_16x16x32_bf16 v[12:15], a[132:135], v[64:67], v[12:15]\n"
    "v_rcp_f32 v20, v20\n"
    "v_mfma_f32_16x16x32_bf16 v[12:15], a[140:143], v[68:71], v[12:15]\n"
    "buffer_load_dwordx4 v[220:223], v98, s[76:79], s68 offen offset:1024 sc1\n"
    "v_mfma_f32_16x16x32_bf16 v[12:15], a[148:151], v[72:75], v[12:15]\n"
    "v_rcp_f32 v21, v21\n"
    "v_mfma_f32_16x16x32_bf16 v[12:15], a[156:159], v[76:79], v[12:15]\n"
    "s_waitcnt lgkmcnt(0)\n"
    "v_mfma_f32_16x16x32_bf16 v[8:11], a[160:163], v[80:83], v[8:11]\n"
    "v_rcp_f32 v22, v22\n"
    "v_rcp_f32 v23, v23\n"
    "v_pk_fma_f32 v[0:1], v[0:1], v[16:17], v[0:1] neg_lo:[1,0,0] neg_hi:[1,0,0]\n"
    "v_pk_fma_f32 v[2:3], v[2:3], v[18:19], v[2:3] neg_lo:[1,0,0] neg_hi:[1,0,0]\n"
    "v_pk_fma_f32 v[4:5], v[4:5], v[20:21], v[4:5] neg_lo:[1,0,0] neg_hi:[1,0,0]\n"
    "v_pk_fma_f32 v[6:7], v[6:7], v[22:23], v[6:7] neg_lo:[1,0,0] neg_hi:[1,0,0]\n"
    "v_mfma_f32_16x16x32_bf16 v[8:11], a[168:171], v[84:87], v[8:11]\n"
    "v_mfma_f32_16x16x32_bf16 v[8:11], a[176:179], v[88:91], v[8:11]\n"
    "v_cvt_pk_bf16_f32 v0, v0, v1\n"
    "v_cvt_pk_bf16_f32 v1, v2, v3\n"
    "v_mfma_f32_16x16x32_bf16 v[8:11], a[184:187], v[92:95], v[8:11]\n"
    "v_cvt_pk_bf16_f32 v2, v4, v5\n"
    "v_cvt_pk_bf16_f32 v3, v6, v7\n"
    "v_mfma_f32_16x16x32_bf16 v[12:15], a[164:167], v[80:83], v[12:15]\n"
    "s_add_u32 s88, s28, 344064\n"
    "buffer_store_dwordx4 v[0:3], v190, s[76:79], s88 offen sc1\n"
    "v_mfma_f32_16x16x32_bf16 v[12:15], a[172:175], v[84:87], v[12:15]\n"
    "v_mfma_f32_16x16x32_bf16 v[12:15], a[180:183], v[88:91], v[12:15]\n"
    "v_mfma_f32_16x16x32_bf16 v[12:15], a[188:191], v[92:95], v[12:15]\n"
    "s_waitcnt vmcnt(5)\n"
    "v_cmp_ne_u32 vcc, 1, v110\n"
    "s_cbranch_vccz L_aa_ok\n"
    "s_mov_b32 s14, 0x40000\n"
    "L_aa_spin:\n"
    "buffer_load_dword v110, v178, s[76:79], s4 offen sc1\n"
    "s_sub_u32 s14, s14, 1\n"
    "s_waitcnt vmcnt(0)\n"
    "s_cbranch_scc1 L_aa_ok\n"
    "v_cmp_ne_u32 vcc, 1, v110\n"
    "s_cbranch_vccz L_aa_ok\n"
    "s_sleep 3\n"
    "s_branch L_aa_spin\n"
    "L_aa_ok:\n"
    "s_add_u32 s70, s24, 0\n"
    "buffer_load_dwordx4 a[0:3], v190, s[76:79], s70 offen offset:0 sc1\n"
    "s_add_u32 s70, s25, 0\n"
    "buffer_load_dwordx4 a[4:7], v190, s[76:79], s70 offen offset:0 sc1\n"
    "s_nop 1\n"
    "v_pk_fma_f32 v[8:9], v[192:193], v[254:255], v[8:9] op_sel_hi:[1,0,1] neg_lo:[0,1,0] neg_hi:[0,1,0]\n"
    "v_pk_fma_f32 v[10:11], v[194:195], v[254:255], v[10:11] op_sel_hi:[1,0,1] neg_lo:[0,1,0] neg_hi:[0,1,0]\n"
    "v_pk_fma_f32 v[12:13], v[196:197], v[254:255], v[12:13] op_sel_hi:[1,0,1] neg_lo:[0,1,0] neg_hi:[0,1,0]\n"
    "v_pk_fma_f32 v[14:15], v[198:199], v[254:255], v[14:15] op_sel_hi:[1,0,1] neg_lo:[0,1,0] neg_hi:[0,1,0]\n"
    "v_pk_fma_f32 v[8:9], v[8:9], v[254:255], v[200:201] op_sel:[0,1,0]\n"
    "v_pk_fma_f32 v[10:11], v[10:11], v[254:255], v[202:203] op_sel:[0,1,0]\n"
    "v_pk_fma_f32 v[12:13], v[12:13], v[254:255], v[204:205] op_sel:[0,1,0]\n"
    "v_pk_fma_f32 v[14:15], v[14:15], v[254:255], v[206:207] op_sel:[0,1,0]\n"
    "v_exp_f32 v16, v8\n"
    "v_exp_f32 v17, v9\n"
    "v_exp_f32 v18, v10\n"
    "v_exp_f32 v19, v11\n"
    "s_add_u32 s70, s24, 0\n"
    "buffer_load_dwordx4 a[8:11], v190, s[76:79], s70 offen offset:1024 sc1\n"
    "v_exp_f32 v20, v12\n"
    "v_exp_f32 v21, v13\n"
    "v_exp_f32 v22, v14\n"
    "v_exp_f32 v23, v15\n"
    "v_pk_fma_f32 v[16:17], v[16:17], s[60:61], s[60:61]\n"
    "v_pk_fma_f32 v[18:19], v[18:19], s[60:61], s[60:61]\n"
    "v_pk_fma_f32 v[20:21], v[20:21], s[60:61], s[60:61]\n"
    "v_pk_fma_f32 v[22:23], v[22:23], s[60:61], s[60:61]\n"
    "v_pk_fma_f32 v[16:17], v[16:17], v[16:17], 0.5 op_sel_hi:[1,1,0]\n"
    "v_pk_fma_f32 v[18:19], v[18:19], v[18:19], 0.5 op_sel_hi:[1,1,0]\n"
    "v_pk_fma_f32 v[20:21], v[20:21], v[20:21], 0.5 op_sel_hi:[1,1,0]\n"
    "v_pk_fma_f32 v[22:23], v[22:23], v[22:23], 0.5 op_sel_hi:[1,1,0]\n"
    "s_add_u32 s70, s25, 0\n"
    "buffer_load_dwordx4 a[12:15], v190, s[76:79], s70 offen offset:1024 sc1\n"
    "v_rcp_f32 v16, v16\n"
    "v_rcp_f32 v17, v17\n"
    "v_rcp_f32 v18, v18\n"
    "v_rcp_f32 v19, v19\n"
    "v_rcp_f32 v20, v20\n"
    "v_rcp_f32 v21, v21\n"
    "v_rcp_f32 v22, v22\n"
    "v_rcp_f32 v23, v23\n"
    "v_pk_fma_f32 v[8:9], v[8:9], v[16:17], v[8:9] neg_lo:[1,0,0] neg_hi:[1,0,0]\n"
    "v_pk_fma_f32 v[10:11], v[10:11], v[18:19], v[10:11] neg_lo:[1,0,0] neg_hi:[1,0,0]\n"
    "v_pk_fma_f32 v[12:13], v[12:13], v[20:21], v[12:13] neg_lo:[1,0,0] neg_hi:[1,0,0]\n"
    "v_pk_fma_f32 v[14:15], v[14:15], v[22:23], v[14:15] neg_lo:[1,0,0] neg_hi:[1,0,0]\n"
    "s_add_u32 s70, s24, 0\n"
    "buffer_load_dwordx4 a[16:19], v190, s[76:79], s70 offen offset:2048 sc1\n"
    "v_cvt_pk_bf16_f32 v8, v8, v9\n"
    "v_cvt_pk_bf16_f32 v9, v10, v11\n"
    "v_cvt_pk_bf16_f32 v10, v12, v13\n"
    "v_cvt_pk_bf16_f32 v11, v14, v15\n"
    "s_add_u32 s88, s28, 401408\n"
    "buffer_store_dwordx4 v[8:11], v190, s[76:79], s88 offen sc1\n"
    "s_add_u32 s70, s25, 0\n"
    "buffer_load_dwordx4 a[20:23], v190, s[76:79], s70 offen offset:2048 sc1\n"
    "s_add_u32 s70, s24, 0\n"
    "buffer_load_dwordx4 a[24:27], v190, s[76:79], s70 offen offset:3072 sc1\n"
    "s_add_u32 s70, s25, 0\n"
    "buffer_load_dwordx4 a[28:31], v190, s[76:79], s70 offen offset:3072 sc1\n"
    "v_lshlrev_b32 v108, 4, v183\n"
    "s_add_u32 s69, s17, 6144\n"
    "s_add_u32 s26, s26, 0x1000\n"
    "s_waitcnt vmcnt(5)\n"
    "v_mfma_f32_16x16x32_bf16 v[0:3], a[64:67], a[0:3], 0\n"
    "s_add_u32 s70, s24, 4096\n"
    "buffer_load_dwordx4 a[32:35], v190, s[76:79], s70 offen offset:0 sc1\n"
    "v_mfma_f32_16x16x32_bf16 v[0:3], a[80:83], a[8:11], v[0:3]\n"
    "v_mfma_f32_16x16x32_bf16 v[4:7], a[68:71], a[0:3], 0\n"
    "v_mfma_f32_16x16x32_bf16 v[4:7], a[84:87], a[8:11], v[4:7]\n"
    "v_mfma_f32_16x16x32_bf16 v[8:11], a[72:75], a[0:3], 0\n"
    "s_add_u32 s70, s25, 4096\n"
    "buffer_load_dwordx4 a[36:39], v190, s[76:79], s70 offen offset:0 sc1\n"
    "v_mfma_f32_16x16x32_bf16 v[8:11], a[88:91], a[8:11], v[8:11]\n"
    "v_mfma_f32_16x16x32_bf16 v[12:15], a[76:79], a[0:3], 0\n"
    "v_mfma_f32_16x16x32_bf16 v[12:15], a[92:95], a[8:11], v[12:15]\n"
    "v_mfma_f32_16x16x32_bf16 v[16:19], a[64:67], a[4:7], 0\n"
    "s_add_u32 s70, s24, 4096\n"
    "buffer_load_dwordx4 a[40:43], v190, s[76:79], s70 offen offset:1024 sc1\n"
    "v_mfma_f32_16x16x32_bf16 v[16:19], a[80:83], a[12:15], v[16:19]\n"
    "v_mfma_f32_16x16x32_bf16 v[20:23], a[68:71], a[4:7], 0\n"
    "v_mfma_f32_16x16x32_bf16 v[20:23], a[84:87], a[12:15], v[20:23]\n"
    "v_mfma_f32_16x16x32_bf16 v[24:27], a[72:75], a[4:7], 0\n"
    "s_add_u32 s70, s25, 4096\n"
    "buffer_load_dwordx4 a[44:47], v190, s[76:79], s70 offen offset:1024 sc1\n"
    "v_mfma_f32_16x16x32_bf16 v[24:27], a[88:91], a[12:15], v[24:27]\n"
    "v_mfma_f32_16x16x32_bf16 v[28:31], a[76:79], a[4:7], 0\n"
    "v_mfma_f32_16x16x32_bf16 v[28:31], a[92:95], a[12:15], v[28:31]\n"
    "s_waitcnt vmcnt(4)\n"
    "v_mfma_f32_16x16x32_bf16 v[0:3], a[96:99], a[16:19], v[0:3]\n"
    "s_add_u32 s70, s24, 4096\n"
    "buffer_load_dwordx4 a[48:51], v190, s[76:79], s70 offen offset:2048 sc1\n"
    "v_mfma_f32_16x16x32_bf16 v[0:3], a[112:115], a[24:27], v[0:3]\n"
    "s_lshl_b32 s5, s56, 5\n"
    "s_add_u32 s5, s5, 0x5800000\n"
    "buffer_store_dword v182, v180, s[76:79], s5 offen sc1\n"
    "v_mfma_f32_16x16x32_bf16 v[4:7], a[100:103], a[16:19], v[4:7]\n"
    "v_mfma_f32_16x16x32_bf16 v[4:7], a[116:119], a[24:27], v[4:7]\n"
    "v_mfma_f32_16x16x32_bf16 v[8:11], a[104:107], a[16:19], v[8:11]\n"
    "s_add_u32 s70, s25, 4096\n"
    "buffer_load_dwordx4 a[52:55], v190, s[76:79], s70 offen offset:2048 sc1\n"
    "v_mfma_f32_16x16x32_bf16 v[8:11], a[120:123], a[24:27], v[8:11]\n"
    "v_mfma_f32_16x16x32_bf16 v[12:15], a[108:111], a[16:19], v[12:15]\n"
    "v_mfma_f32_16x16x32_bf16 v[12:15], a[124:127], a[24:27], v[12:15]\n"
    "v_mfma_f32_16x16x32_bf16 v[16:19], a[96:99], a[20:23], v[16:19]\n"
    "s_add_u32 s70, s24, 4096\n"
    "buffer_load_dwordx4 a[56:59], v190, s[76:79], s70 offen offset:3072 sc1\n"
    "v_mfma_f32_16x16x32_bf16 v[16:19], a[112:115], a[28:31], v[16:19]\n"
    "v_mfma_f32_16x16x32_bf16 v[20:23], a[100:103], a[20:23], v[20:23]\n"
    "v_mfma_f32_16x16x32_bf16 v[20:23], a[116:119], a[28:31], v[20:23]\n"
    "v_mfma_f32_16x16x32_bf16 v[24:27], a[104:107], a[20:23], v[24:27]\n"
    "s_add_u32 s70, s25, 4096\n"
    "buffer_load_dwordx4 a[60:63], v190, s[76:79], s70 offen offset:3072 sc1\n"
    "v_mfma_f32_16x16x32_bf16 v[24:27], a[120:123], a[28:31], v[24:27]\n"
    "v_mfma_f32_16x16x32_bf16 v[28:31], a[108:111], a[20:23], v[28:31]\n"
    "v_mfma_f32_16x16x32_bf16 v[28:31], a[124:127], a[28:31], v[28:31]\n"
    "s_waitcnt vmcnt(5)\n"
    "v_mfma_f32_16x16x32_bf16 v[0:3], a[192:195], a[32:35], v[0:3]\n"
    "s_add_u32 s70, s24, 8192\n"
    "buffer_load_dwordx4 a[128:131], v190, s[76:79], s70 offen offset:0 sc1\n"
    "v_mfma_f32_16x16x32_bf16 v[0:3], a[208:211], a[40:43], v[0:3]\n"
    "v_mfma_f32_16x16x32_bf16 v[4:7], a[196:199], a[32:35], v[4:7]\n"
    "v_mfma_f32_16x16x32_bf16 v[4:7], a[212:215], a[40:43], v[4:7]\n"
    "v_mfma_f32_16x16x32_bf16 v[8:11], a[200:203], a[32:35], v[8:11]\n"
    "s_add_u32 s70, s25, 8192\n"
    "buffer_load_dwordx4 a[132:135], v190, s[76:79], s70 offen offset:0 sc1\n"
    "v_mfma_f32_16x16x32_bf16 v[8:11], a[216:219], a[40:43], v[8:11]\n"
    ".p2align 12\n"
    "L_pad14:\n"
    "s_cbranch_execz L_walked\n"
    "v_mfma_f32_16x16x32_bf16 v[12:15], a[204:207], a[32:35], v[12:15]\n"
    "v_mfma_f32_16x16x32_bf16 v[12:15], a[220:223], a[40:43], v[12:15]\n"
    "v_mfma_f32_16x16x32_bf16 v[16:19], a[192:195], a[36:39], v[16:19]\n"
    "s_add_u32 s70, s24, 8192\n"
    "buffer_load_dwordx4 a[136:139], v190, s[76:79], s70 offen offset:1024 sc1\n"
    "v_mfma_f32_16x16x32_bf16 v[16:19], a[208:211], a[44:47], v[16:19]\n"
    "v_mfma_f32_16x16x32_bf16 v[20:23], a[196:199], a[36:39], v[20:23]\n"
    "s_add_u32 s68, s83, 53248\n"
    "buffer_load_dwordx4 a[176:179], v97, s[76:79], s68 offen offset:0 sc1\n"
    "v_mfma_f32_16x16x32_bf16 v[20:23], a[212:215], a[44:47], v[20:23]\n"
    "v_mfma_f32_16x16x32_bf16 v[24:27], a[200:203], a[36:39], v[24:27]\n"
    "s_add_u32 s70, s25, 8192\n"
    "buffer_load_dwordx4 a[140:143], v190, s[76:79], s70 offen offset:1024 sc1\n"
    "v_mfma_f32_16x16x32_bf16 v[24:27], a[216:219], a[44:47], v[24:27]\n"
    "v_mfma_f32_16x16x32_bf16 v[28:31], a[204:207], a[36:39], v[28:31]\n"
    "buffer_load_dwordx4 a[180:183], v97, s[76:79], s68 offen offset:1024 sc1\n"
    "v_mfma_f32_16x16x32_bf16 v[28:31], a[220:223], a[44:47], v[28:31]\n"
    "s_waitcnt vmcnt(6)\n"
    "v_mfma_f32_16x16x32_bf16 v[0:3], a[224:227], a[48:51], v[0:3]\n"
    "s_add_u32 s70, s24, 8192\n"
    "buffer_load_dwordx4 a[144:147], v190, s[76:79], s70 offen offset:2048 sc1\n"
    "v_mfma_f32_16x16x32_bf16 v[0:3], a[240:243], a[56:59], v[0:3]\n"
    "v_mfma_f32_16x16x32_bf16 v[4:7], a[228:231], a[48:51], v[4:7]\n"
    "buffer_load_dwordx4 a[184:187], v98, s[76:79], s68 offen offset:0 sc1\n"
    "v_mfma_f32_16x16x32_bf16 v[4:7], a[244:247], a[56:59], v[4:7]\n"
    "v_mfma_f32_16x16x32_bf16 v[8:11], a[232:235], a[48:51], v[8:11]\n"
    "s_add_u32 s70, s25, 8192\n"
    "buffer_load_dwordx4 a[148:151], v190, s[76:79], s70 offen offset:2048 sc1\n"
    "v_mfma_f32_16x16x32_bf16 v[8:11], a[248:251], a[56:59], v[8:11]\n"
    "v_mfma_f32_16x16x32_bf16 v[12:15], a[236:239], a[48:51], v[12:15]\n"
    "buffer_load_dwordx4 a[188:191], v98, s[76:79], s68 offen offset:1024 sc1\n"
    "v_mfma_f32_16x16x32_bf16 v[12:15], a[252:255], a[56:59], v[12:15]\n"
    "v_mfma_f32_16x16x32_bf16 v[16:19], a[224:227], a[52:55], v[16:19]\n"
    "s_add_u32 s70, s24, 8192\n"
    "buffer_load_dwordx4 a[152:155], v190, s[76:79], s70 offen offset:3072 sc1\n"
    "v_mfma_f32_16x16x32_bf16 v[16:19], a[240:243], a[60:63], v[16:19]\n"
    "v_mfma_f32_16x16x32_bf16 v[20:23], a[228:231], a[52:55], v[20:23]\n"
    "v_mfma_f32_16x16x32_bf16 v[20:23], a[244:247], a[60:63], v[20:23]\n"
    "v_mfma_f32_16x16x32_bf16 v[24:27], a[232:235], a[52:55], v[24:27]\n"
    "s_add_u32 s70, s25, 8192\n"
    "buffer_load_dwordx4 a[156:159], v190, s[76:79], s70 offen offset:3072 sc1\n"
    "v_mfma_f32_16x16x32_bf16 v[24:27], a[248:251], a[60:63], v[24:27]\n"
    "v_mfma_f32_16x16x32_bf16 v[28:31], a[236:239], a[52:55], v[28:31]\n"
    "v_mfma_f32_16x16x32_bf16 v[28:31], a[252:255], a[60:63], v[28:31]\n"
    "s_waitcnt vmcnt(7)\n"
    "v_mfma_f32_16x16x32_bf16 v[0:3], v[112:115], a[128:131], v[0:3]\n"
    "s_add_u32 s70, s24, 12288\n"
    "buffer_load_dwordx4 a[160:163], v190, s[76:79], s70 offen offset:0 sc1\n"
    "v_mfma_f32_16x16x32_bf16 v[0:3], v[128:131], a[136:139], v[0:3]\n"
    "v_mfma_f32_16x16x32_bf16 v[4:7], v[116:119], a[128:131], v[4:7]\n"
    "v_mfma_f32_16x16x32_bf16 v[4:7], v[132:135], a[136:139], v[4:7]\n"
    "v_mfma_f32_16x16x32_bf16 v[8:11], v[120:123], a[128:131], v[8:11]\n"
    "s_add_u32 s70, s25, 12288\n"
    "buffer_load_dwordx4 a[164:167], v190, s[76:79], s70 offen offset:0 sc1\n"
    "v_mfma_f32_16x16x32_bf16 v[8:11], v[136:139], a[136:139], v[8:11]\n"
    "v_mfma_f32_16x16x32_bf16 v[12:15], v[124:127], a[128:131], v[12:15]\n"
    "v_mfma_f32_16x16x32_bf16 v[12:15], v[140:143], a[136:139], v[12:15]\n"
    "v_mfma_f32_16x16x32_bf16 v[16:19], v[112:115], a[132:135], v[16:19]\n"
    "s_add_u32 s70, s24, 12288\n"
    "buffer_load_dwordx4 a[168:171], v190, s[76:79], s70 offen offset:1024 sc1\n"
    "v_mfma_f32_16x16x32_bf16 v[16:19], v[128:131], a[140:143], v[16:19]\n"
    "v_mfma_f32_16x16x32_bf16 v[20:23], v[116:119], a[132:135], v[20:23]\n"
    "v_mfma_f32_16x16x32_bf16 v[20:23], v[132:135], a[140:143], v[20:23]\n"
    "v_mfma_f32_16x16x32_bf16 v[24:27], v[120:123], a[132:135], v[24:27]\n"
    "s_add_u32 s70, s25, 12288\n"
    "buffer_load_dwordx4 a[172:175], v190, s[76:79], s70 offen offset:1024 sc1\n"
    "v_mfma_f32_16x16x32_bf16 v[24:27], v[136:139], a[140:143], v[24:27]\n"
    "v_mfma_f32_16x16x32_bf16 v[28:31], v[124:127], a[132:135], v[28:31]\n"
    "v_mfma_f32_16x16x32_bf16 v[28:31], v[140:143], a[140:143], v[28:31]\n"
    "s_waitcnt vmcnt(4)\n"
    "v_mfma_f32_16x16x32_bf16 v[0:3], v[144:147], a[144:147], v[0:3]\n"
    "v_mfma_f32_16x16x32_bf16 v[0:3], v[160:163], a[152:155], v[0:3]\n"
    "v_mfma_f32_16x16x32_bf16 v[4:7], v[148:151], a[144:147], v[4:7]\n"
    "buffer_load_dwordx4 v[192:195], v108, s[76:79], s69 offen offset:0 sc1\n"
    "v_mfma_f32_16x16x32_bf16 v[4:7], v[164:167], a[152:155], v[4:7]\n"
    "v_mfma_f32_16x16x32_bf16 v[8:11], v[152:155], a[144:147], v[8:11]\n"
    "v_mfma_f32_16x16x32_bf16 v[8:11], v[168:171], a[152:155], v[8:11]\n"
    "v_mfma_f32_16x16x32_bf16 v[12:15], v[156:159], a[144:147], v[12:15]\n"
    "buffer_load_dwordx4 v[196:199], v108, s[76:79], s69 offen offset:64 sc1\n"
    "v_mfma_f32_16x16x32_bf16 v[12:15], v[172:175], a[152:155], v[12:15]\n"
    "v_mfma_f32_16x16x32_bf16 v[16:19], v[144:147], a[148:151], v[16:19]\n"
    "v_mfma_f32_16x16x32_bf16 v[16:19], v[160:163], a[156:159], v[16:19]\n"
    "v_mfma_f32_16x16x32_bf16 v[20:23], v[148:151], a[148:151], v[20:23]\n"
    "s_lshl_b32 s4, s26, 5\n"
    "s_add_u32 s4, s4, 0x5800000\n"
    "buffer_load_dword v110, v178, s[76:79], s4 offen sc1\n"
    "v_mfma_f32_16x16x32_bf16 v[20:23], v[164:167], a[156:159], v[20:23]\n"
    "v_mfma_f32_16x16x32_bf16 v[24:27], v[152:155], a[148:151], v[24:27]\n"
    "v_mfma_f32_16x16x32_bf16 v[24:27], v[168:171], a[156:159], v[24:27]\n"
    "s_mov_b32 s44, s54\n"
    "v_mfma_f32_16x16x32_bf16 v[28:31], v[156:159], a[148:151], v[28:31]\n"
    "s_and_b32 s45, s55, 0xffff\n"
    "v_mfma_f32_16x16x32_bf16 v[28:31], v[172:175], a[156:159], v[28:31]\n"
    "s_waitcnt vmcnt(3)\n"
    "v_mfma_f32_16x16x32_bf16 v[0:3], v[208:211], a[160:163], v[0:3]\n"
    "v_mfma_f32_16x16x32_bf16 v[0:3], a[176:179], a[168:171], v[0:3]\n"
    "v_mfma_f32_16x16x32_bf16 v[4:7], v[212:215], a[160:163], v[4:7]\n"
    "v_mfma_f32_16x16x32_bf16 v[4:7], a[180:183], a[168:171], v[4:7]\n"
    "v_mfma_f32_16x16x32_bf16 v[8:11], v[216:219], a[160:163], v[8:11]\n"
    "v_mfma_f32_16x16x32_bf16 v[8:11], a[184:187], a[168:171], v[8:11]\n"
    "v_mfma_f32_16x16x32_bf16 v[12:15], v[220:223], a[160:163], v[12:15]\n"
    "v_mfma_f32_16x16x32_bf16 v[12:15], a[188:191], a[168:171], v[12:15]\n"
    "v_mfma_f32_16x16x32_bf16 v[16:19], v[208:211], a[164:167], v[16:19]\n"
    "v_mfma_f32_16x16x32_bf16 v[16:19], a[176:179], a[172:175], v[16:19]\n"
    "v_mfma_f32_16x16x32_bf16 v[20:23], v[212:215], a[164:167], v[20:23]\n"
    "v_mfma_f32_16x16x32_bf16 v[20:23], a[180:183], a[172:175], v[20:23]\n"
    "v_mfma_f32_16x16x32_bf16 v[24:27], v[216:219], a[164:167], v[24:27]\n"
    "v_mfma_f32_16x16x32_bf16 v[24:27], a[184:187], a[172:175], v[24:27]\n"
    "v_mfma_f32_16x16x32_bf16 v[28:31], v[220:223], a[164:167], v[28:31]\n"
    "v_mfma_f32_16x16x32_bf16 v[28:31], a[188:191], a[172:175], v[28:31]\n"
    "s_nop 3\n"
    "ds_write_b128 v104, v[8:11] offset:0\n"
    "ds_write_b128 v104, v[12:15] offset:1024\n"
    "ds_write_b128 v104, v[16:19] offset:2048\n"
    "ds_write_b128 v104, v[20:23] offset:3072\n"
    "ds_write_b128 v104, v[24:27] offset:16384\n"
    "ds_write_b128 v104, v[28:31] offset:17408\n"
    "s_waitcnt vmcnt(0)\n"
    "v_cmp_ne_u32 vcc, 1, v110\n"
    "s_cbranch_vccz L_ab_ok\n"
    "s_mov_b32 s14, 0x40000\n"
    "L_ab_spin:\n"
    "buffer_load_dword v110, v178, s[76:79], s4 offen sc1\n"
    "s_sub_u32 s14, s14, 1\n"
    "s_waitcnt vmcnt(0)\n"
    "s_cbranch_scc1 L_ab_ok\n"
    "v_cmp_ne_u32 vcc, 1, v110\n"
    "s_cbranch_vccz L_ab_ok\n"
    "s_sleep 3\n"
    "s_branch L_ab_spin\n"
    "L_ab_ok:\n"
    "s_add_u32 s70, s24, 114688\n"
    "buffer_load_dwordx4 a[0:3], v190, s[76:79], s70 offen offset:0 sc1\n"
    "s_add_u32 s70, s25, 114688\n"
    "buffer_load_dwordx4 a[4:7], v190, s[76:79], s70 offen offset:0 sc1\n"
    "s_add_u32 s70, s24, 114688\n"
    "buffer_load_dwordx4 a[8:11], v190, s[76:79], s70 offen offset:1024 sc1\n"
    "s_add_u32 s70, s25, 114688\n"
    "buffer_load_dwordx4 a[12:15], v190, s[76:79], s70 offen offset:1024 sc1\n"
    "s_mov_b32 s69, 0x3f317218\n"
    "v_mul_f32 v192, s69, v192\n"
    "v_mul_f32 v193, s69, v193\n"
    "s_add_u32 s70, s24, 114688\n"
    "buffer_load_dwordx4 a[16:19], v190, s[76:79], s70 offen offset:2048 sc1\n"
    "v_mul_f32 v194, s69, v194\n"
    "v_mul_f32 v195, s69, v195\n"
    "v_mul_f32 v196, s69, v196\n"
    "v_mul_f32 v197, s69, v197\n"
    "s_add_u32 s70, s25, 114688\n"
    "buffer_load_dwordx4 a[20:23], v190, s[76:79], s70 offen offset:2048 sc1\n"
    "v_mul_f32 v198, s69, v198\n"
    "v_mul_f32 v199, s69, v199\n"
    "s_waitcnt lgkmcnt(0)\n"
    "s_barrier\n"
    "s_add_u32 s70, s24, 114688\n"
    "buffer_load_dwordx4 a[24:27], v190, s[76:79], s70 offen offset:3072 sc1\n"
    "ds_read_b128 v[8:11], v105 offset:0\n"
    "ds_read_b128 v[12:15], v105 offset:1024\n"
    "ds_read_b128 v[16:19], v106 offset:2048\n"
    "ds_read_b128 v[20:23], v106 offset:3072\n"
    "s_add_u32 s70, s25, 114688\n"
    "buffer_load_dwordx4 a[28:31], v190, s[76:79], s70 offen offset:3072 sc1\n"
    "ds_read_b128 v[24:27], v107 offset:16384\n"
    "ds_read_b128 v[28:31], v107 offset:17408\n"
    "s_waitcnt lgkmcnt(4)\n"
    "v_pk_add_f32 v[0:1], v[0:1], v[8:9]\n"
    "s_add_u32 s70, s24, 118784\n"
    "buffer_load_dwordx4 a[32:35], v190, s[76:79], s70 offen offset:0 sc1\n"
    "v_pk_add_f32 v[2:3], v[2:3], v[10:11]\n"
    "v_pk_add_f32 v[4:5], v[4:5], v[12:13]\n"
    "v_pk_add_f32 v[6:7], v[6:7], v[14:15]\n"
    "s_waitcnt lgkmcnt(2)\n"
    "s_add_u32 s70, s25, 118784\n"
    "buffer_load_dwordx4 a[36:39], v190, s[76:79], s70 offen offset:0 sc1\n"
    "v_pk_add_f32 v[0:1], v[0:1], v[16:17]\n"
    "v_pk_add_f32 v[2:3], v[2:3], v[18:19]\n"
    "v_pk_add_f32 v[4:5], v[4:5], v[20:21]\n"
    "v_pk_add_f32 v[6:7], v[6:7], v[22:23]\n"
    "s_add_u32 s70, s24, 118784\n"
    "buffer_load_dwordx4 a[40:43], v190, s[76:79], s70 offen offset:1024 sc1\n"
    "s_waitcnt lgkmcnt(0)\n"
    "v_pk_add_f32 v[0:1], v[0:1], v[24:25]\n"
    "v_pk_add_f32 v[2:3], v[2:3], v[26:27]\n"
    "v_pk_add_f32 v[4:5], v[4:5], v[28:29]\n"
    "s_add_u32 s70, s25, 118784\n"
    "buffer_load_dwordx4 a[44:47], v190, s[76:79], s70 offen offset:1024 sc1\n"
    "v_pk_add_f32 v[6:7], v[6:7], v[30:31]\n"
    "v_pk_fma_f32 v[0:1], v[0:1], v[192:193], v[224:225]\n"
    "v_pk_fma_f32 v[2:3], v[2:3], v[194:195], v[226:227]\n"
    "v_pk_fma_f32 v[4:5], v[4:5], v[196:197], v[228:229]\n"
    "s_add_u32 s70, s24, 118784\n"
    "buffer_load_dwordx4 a[48:51], v190, s[76:79], s70 offen offset:2048 sc1\n"
    "v_pk_fma_f32 v[6:7], v[6:7], v[198:199], v[230:231]\n"
    "v_cvt_pk_bf16_f32 v8, v0, v1\n"
    "v_cvt_pk_bf16_f32 v9, v2, v3\n"
    "v_cvt_pk_bf16_f32 v10, v4, v5\n"
    "s_add_u32 s70, s25, 118784\n"
    "buffer_load_dwordx4 a[52:55], v190, s[76:79], s70 offen offset:2048 sc1\n"
    "v_cvt_pk_bf16_f32 v11, v6, v7\n"
    "s_add_u32 s69, s16, 0x0\n"
    "s_nop 0\n"
    "v_permlane16_swap_b32 v8, v10\n"
    "v_permlane16_swap_b32 v9, v11\n"
    "buffer_store_dwordx4 v[8:11], v111, s[44:47], s69 offen\n"
    "s_waitcnt vmcnt(11)\n"
    "v_mfma_f32_16x16x32_bf16 v[32:35], a[64:67], a[0:3], 0\n"
    "s_add_u32 s70, s24, 118784\n"
    "buffer_load_dwordx4 a[56:59], v190, s[76:79], s70 offen offset:3072 sc1\n"
    "v_mfma_f32_16x16x32_bf16 v[32:35], a[80:83], a[8:11], v[32:35]\n"
    "v_mfma_f32_16x16x32_bf16 v[36:39], a[68:71], a[0:3], 0\n"
    "v_mfma_f32_16x16x32_bf16 v[36:39], a[84:87], a[8:11], v[36:39]\n"
    "v_mfma_f32_16x16x32_bf16 v[40:43], a[72:75], a[0:3], 0\n"
    "s_add_u32 s70, s25, 118784\n"
    "buffer_load_dwordx4 a[60:63], v190, s[76:79], s70 offen offset:3072 sc1\n"
    "v_mfma_f32_16x16x32_bf16 v[40:43], a[88:91], a[8:11], v[40:43]\n"
    "v_mfma_f32_16x16x32_bf16 v[44:47], a[76:79], a[0:3], 0\n"
    "v_mfma_f32_16x16x32_bf16 v[44:47], a[92:95], a[8:11], v[44:47]\n"
    "v_mfma_f32_16x16x32_bf16 v[48:51], a[64:67], a[4:7], 0\n"
    "s_add_u32 s70, s24, 122880\n"
    "buffer_load_dwordx4 a[128:131], v190, s[76:79], s70 offen offset:0 sc1\n"
    "v_mfma_f32_16x16x32_bf16 v[48:51], a[80:83], a[12:15], v[48:51]\n"
    "v_mfma_f32_16x16x32_bf16 v[52:55], a[68:71], a[4:7], 0\n"
    "v_mfma_f32_16x16x32_bf16 v[52:55], a[84:87], a[12:15], v[52:55]\n"
    "v_mfma_f32_16x16x32_bf16 v[56:59], a[72:75], a[4:7], 0\n"
    "s_add_u32 s70, s25, 122880\n"
    "buffer_load_dwordx4 a[132:135], v190, s[76:79], s70 offen offset:0 sc1\n"
    "v_mfma_f32_16x16x32_bf16 v[56:59], a[88:91], a[12:15], v[56:59]\n"
    "v_mfma_f32_16x16x32_bf16 v[60:63], a[76:79], a[4:7], 0\n"
    "v_mfma_f32_16x16x32_bf16 v[60:63], a[92:95], a[12:15], v[60:63]\n"
    "s_waitcnt vmcnt(11)\n"
    "v_mfma_f32_16x16x32_bf16 v[32:35], a[96:99], a[16:19], v[32:35]\n"
    "s_add_u32 s70, s24, 122880\n"
    "buffer_load_dwordx4 a[136:139], v190, s[76:79], s70 offen offset:1024 sc1\n"
    "v_mfma_f32_16x16x32_bf16 v[32:35], a[112:115], a[24:27], v[32:35]\n"
    "v_mfma_f32_16x16x32_bf16 v[36:39], a[100:103], a[16:19], v[36:39]\n"
    "v_mfma_f32_16x16x32_bf16 v[36:39], a[116:119], a[24:27], v[36:39]\n"
    "v_mfma_f32_16x16x32_bf16 v[40:43], a[104:107], a[16:19], v[40:43]\n"
    "s_add_u32 s70, s25, 122880\n"
    "buffer_load_dwordx4 a[140:143], v190, s[76:79], s70 offen offset:1024 sc1\n"
    "v_mfma_f32_16x16x32_bf16 v[40:43], a[120:123], a[24:27], v[40:43]\n"
    "v_mfma_f32_16x16x32_bf16 v[44:47], a[108:111], a[16:19], v[44:47]\n"
    "v_mfma_f32_16x16x32_bf16 v[44:47], a[124:127], a[24:27], v[44:47]\n"
    "v_mfma_f32_16x16x32_bf16 v[48:51], a[96:99], a[20:23], v[48:51]\n"
    "s_add_u32 s70, s24, 122880\n"
    "buffer_load_dwordx4 a[144:147], v190, s[76:79], s70 offen offset:2048 sc1\n"
    "v_mfma_f32_16x16x32_bf16 v[48:51], a[112:115], a[28:31], v[48:51]\n"
    "v_mfma_f32_16x16x32_bf16 v[52:55], a[100:103], a[20:23], v[52:55]\n"
    "v_mfma_f32_16x16x32_bf16 v[52:55], a[116:119], a[28:31], v[52:55]\n"
    "v_mfma_f32_16x16x32_bf16 v[56:59], a[104:107], a[20:23], v[56:59]\n"
    "s_add_u32 s70, s25, 122880\n"
    "buffer_load_dwordx4 a[148:151], v190, s[76:79], s70 offen offset:2048 sc1\n"
    "v_mfma_f32_16x16x32_bf16 v[56:59], a[120:123], a[28:31], v[56:59]\n"
    "v_mfma_f32_16x16x32_bf16 v[60:63], a[108:111], a[20:23], v[60:63]\n"
    "v_mfma_f32_16x16x32_bf16 v[60:63], a[124:127], a[28:31], v[60:63]\n"
    "s_waitcnt vmcnt(11)\n"
    "v_mfma_f32_16x16x32_bf16 v[32:35], a[192:195], a[32:35], v[32:35]\n"
    "s_add_u32 s70, s24, 122880\n"
    "buffer_load_dwordx4 a[152:155], v190, s[76:79], s70 offen offset:3072 sc1\n"
    "v_mfma_f32_16x16x32_bf16 v[32:35], a[208:211], a[40:43], v[32:35]\n"
    "v_mfma_f32_16x16x32_bf16 v[36:39], a[196:199], a[32:35], v[36:39]\n"
    "v_mfma_f32_16x16x32_bf16 v[36:39], a[212:215], a[40:43], v[36:39]\n"
    "v_mfma_f32_16x16x32_bf16 v[40:43], a[200:203], a[32:35], v[40:43]\n"
    "s_add_u32 s70, s25, 122880\n"
    "buffer_load_dwordx4 a[156:159], v190, s[76:79], s70 offen offset:3072 sc1\n"
    "v_mfma_f32_16x16x32_bf16 v[40:43], a[216:219], a[40:43], v[40:43]\n"
    "v_mfma_f32_16x16x32_bf16 v[44:47], a[204:207], a[32:35], v[44:47]\n"
    "v_mfma_f32_16x16x32_bf16 v[44:47], a[220:223], a[40:43], v[44:47]\n"
    "v_mfma_f32_16x16x32_bf16 v[48:51], a[192:195], a[36:39], v[48:51]\n"
    "s_add_u32 s70, s24, 126976\n"
    "buffer_load_dwordx4 a[160:163], v190, s[76:79], s70 offen offset:0 sc1\n"
    "v_mfma_f32_16x16x32_bf16 v[48:51], a[208:211], a[44:47], v[48:51]\n"
    "v_mfma_f32_16x16x32_bf16 v[52:55], a[196:199], a[36:39], v[52:55]\n"
    "v_mfma_f32_16x16x32_bf16 v[52:55], a[212:215], a[44:47], v[52:55]\n"
    "v_mfma_f32_16x16x32_bf16 v[56:59], a[200:203], a[36:39], v[56:59]\n"
    "s_add_u32 s70, s25, 126976\n"
    "buffer_load_dwordx4 a[164:167], v190, s[76:79], s70 offen offset:0 sc1\n"
    "v_mfma_f32_16x16x32_bf16 v[56:59], a[216:219], a[44:47], v[56:59]\n"
    "v_mfma_f32_16x16x32_bf16 v[60:63], a[204:207], a[36:39], v[60:63]\n"
    "v_mfma_f32_16x16x32_bf16 v[60:63], a[220:223], a[44:47], v[60:63]\n"
    "s_waitcnt vmcnt(10)\n"
    "v_mfma_f32_16x16x32_bf16 v[32:35], a[224:227], a[48:51], v[32:35]\n"
    "s_add_u32 s70, s24, 126976\n"
    "buffer_load_dwordx4 a[168:171], v190, s[76:79], s70 offen offset:1024 sc1\n"
    "v_mfma_f32_16x16x32_bf16 v[32:35], a[240:243], a[56:59], v[32:35]\n"
    "v_mfma_f32_16x16x32_bf16 v[36:39], a[228:231], a[48:51], v[36:39]\n"
    "v_mfma_f32_16x16x32_bf16 v[36:39], a[244:247], a[56:59], v[36:39]\n"
    "v_mfma_f32_16x16x32_bf16 v[40:43], a[232:235], a[48:51], v[40:43]\n"
    "s_add_u32 s70, s25, 126976\n"
    "buffer_load_dwordx4 a[172:175], v190, s[76:79], s70 offen offset:1024 sc1\n"
    "v_mfma_f32_16x16x32_bf16 v[40:43], a[248:251], a[56:59], v[40:43]\n"
    "v_mfma_f32_16x16x32_bf16 v[44:47], a[236:239], a[48:51], v[44:47]\n"
    "v_mfma_f32_16x16x32_bf16 v[44:47], a[252:255], a[56:59], v[44:47]\n"
    "v_mfma_f32_16x16x32_bf16 v[48:51], a[224:227], a[52:55], v[48:51]\n"
    "v_mfma_f32_16x16x32_bf16 v[48:51], a[240:243], a[60:63], v[48:51]\n"
    "v_mfma_f32_16x16x32_bf16 v[52:55], a[228:231], a[52:55], v[52:55]\n"
    "v_mfma_f32_16x16x32_bf16 v[52:55], a[244:247], a[60:63], v[52:55]\n"
    "v_mfma_f32_16x16x32_bf16 v[56:59], a[232:235], a[52:55], v[56:59]\n"
    "v_mfma_f32_16x16x32_bf16 v[56:59], a[248:251], a[60:63], v[56:59]\n"
    "v_mfma_f32_16x16x32_bf16 v[60:63], a[236:239], a[52:55], v[60:63]\n"
    "v_mfma_f32_16x16x32_bf16 v[60:63], a[252:255], a[60:63], v[60:63]\n"
    "s_waitcnt vmcnt(8)\n"
    "v_mfma_f32_16x16x32_bf16 v[32:35], v[112:115], a[128:131], v[32:35]\n"
    "v_mfma_f32_16x16x32_bf16 v[32:35], v[128:131], a[136:139], v[32:35]\n"
    "v_mfma_f32_16x16x32_bf16 v[36:39], v[116:119], a[128:131], v[36:39]\n"
    "v_mfma_f32_16x16x32_bf16 v[36:39], v[132:135], a[136:139], v[36:39]\n"
    "v_mfma_f32_16x16x32_bf16 v[40:43], v[120:123], a[128:131], v[40:43]\n"
    "v_mfma_f32_16x16x32_bf16 v[40:43], v[136:139], a[136:139], v[40:43]\n"
    "v_mfma_f32_16x16x32_bf16 v[44:47], v[124:127], a[128:131], v[44:47]\n"
    "v_mfma_f32_16x16x32_bf16 v[44:47], v[140:143], a[136:139], v[44:47]\n"
    "v_mfma_f32_16x16x32_bf16 v[48:51], v[112:115], a[132:135], v[48:51]\n"
    "v_mfma_f32_16x16x32_bf16 v[48:51], v[128:131], a[140:143], v[48:51]\n"
    "v_mfma_f32_16x16x32_bf16 v[52:55], v[116:119], a[132:135], v[52:55]\n"
    "v_mfma_f32_16x16x32_bf16 v[52:55], v[132:135], a[140:143], v[52:55]\n"
    "v_mfma_f32_16x16x32_bf16 v[56:59], v[120:123], a[132:135], v[56:59]\n"
    "v_mfma_f32_16x16x32_bf16 v[56:59], v[136:139], a[140:143], v[56:59]\n"
    "v_mfma_f32_16x16x32_bf16 v[60:63], v[124:127], a[132:135], v[60:63]\n"
    "v_mfma_f32_16x16x32_bf16 v[60:63], v[140:143], a[140:143], v[60:63]\n"
    "s_waitcnt vmcnt(4)\n"
    "v_mfma_f32_16x16x32_bf16 v[32:35], v[144:147], a[144:147], v[32:35]\n"
    "v_mfma_f32_16x16x32_bf16 v[32:35], v[160:163], a[152:155], v[32:35]\n"
    "v_mfma_f32_16x16x32_bf16 v[36:39], v[148:151], a[144:147], v[36:39]\n"
    "v_mfma_f32_16x16x32_bf16 v[36:39], v[164:167], a[152:155], v[36:39]\n"
    "v_mfma_f32_16x16x32_bf16 v[40:43], v[152:155], a[144:147], v[40:43]\n"
    "v_mfma_f32_16x16x32_bf16 v[40:43], v[168:171], a[152:155], v[40:43]\n"
    "v_mfma_f32_16x16x32_bf16 v[44:47], v[156:159], a[144:147], v[44:47]\n"
    "v_mfma_f32_16x16x32_bf16 v[44:47], v[172:175], a[152:155], v[44:47]\n"
    "v_mfma_f32_16x16x32_bf16 v[48:51], v[144:147], a[148:151], v[48:51]\n"
    "v_mfma_f32_16x16x32_bf16 v[48:51], v[160:163], a[156:159], v[48:51]\n"
    "v_mfma_f32_16x16x32_bf16 v[52:55], v[148:151], a[148:151], v[52:55]\n"
    "v_mfma_f32_16x16x32_bf16 v[52:55], v[164:167], a[156:159], v[52:55]\n"
    "v_mfma_f32_16x16x32_bf16 v[56:59], v[152:155], a[148:151], v[56:59]\n"
    "v_mfma_f32_16x16x32_bf16 v[56:59], v[168:171], a[156:159], v[56:59]\n"
    "v_mfma_f32_16x16x32_bf16 v[60:63], v[156:159], a[148:151], v[60:63]\n"
    "v_mfma_f32_16x16x32_bf16 v[60:63], v[172:175], a[156:159], v[60:63]\n"
    "s_waitcnt vmcnt(0)\n"
    "v_mfma_f32_16x16x32_bf16 v[32:35], v[208:211], a[160:163], v[32:35]\n"
    "v_mfma_f32_16x16x32_bf16 v[32:35], a[176:179], a[168:171], v[32:35]\n"
    "v_mfma_f32_16x16x32_bf16 v[36:39], v[212:215], a[160:163], v[36:39]\n"
    "v_mfma_f32_16x16x32_bf16 v[36:39], a[180:183], a[168:171], v[36:39]\n"
    "v_mfma_f32_16x16x32_bf16 v[40:43], v[216:219], a[160:163], v[40:43]\n"
    "v_mfma_f32_16x16x32_bf16 v[40:43], a[184:187], a[168:171], v[40:43]\n"
    "v_mfma_f32_16x16x32_bf16 v[44:47], v[220:223], a[160:163], v[44:47]\n"
    "v_mfma_f32_16x16x32_bf16 v[44:47], a[188:191], a[168:171], v[44:47]\n"
    "v_mfma_f32_16x16x32_bf16 v[48:51], v[208:211], a[164:167], v[48:51]\n"
    "v_mfma_f32_16x16x32_bf16 v[48:51], a[176:179], a[172:175], v[48:51]\n"
    "v_mfma_f32_16x16x32_bf16 v[52:55], v[212:215], a[164:167], v[52:55]\n"
    "v_mfma_f32_16x16x32_bf16 v[52:55], a[180:183], a[172:175], v[52:55]\n"
    "v_mfma_f32_16x16x32_bf16 v[56:59], v[216:219], a[164:167], v[56:59]\n"
    "v_mfma_f32_16x16x32_bf16 v[56:59], a[184:187], a[172:175], v[56:59]\n"
    "v_mfma_f32_16x16x32_bf16 v[60:63], v[220:223], a[164:167], v[60:63]\n"
    "v_mfma_f32_16x16x32_bf16 v[60:63], a[188:191], a[172:175], v[60:63]\n"
    "s_nop 3\n"
    "ds_write_b128 v104, v[40:43] offset:32768\n"
    "ds_write_b128 v104, v[44:47] offset:33792\n"
    "ds_write_b128 v104, v[48:51] offset:34816\n"
    "ds_write_b128 v104, v[52:55] offset:35840\n"
    "ds_write_b128 v104, v[56:59] offset:49152\n"
    "ds_write_b128 v104, v[60:63] offset:50176\n"
    "s_waitcnt lgkmcnt(0)\n"
    "s_barrier\n"
    "ds_read_b128 v[40:43], v105 offset:32768\n"
    "ds_read_b128 v[44:47], v105 offset:33792\n"
    "ds_read_b128 v[48:51], v106 offset:34816\n"
    "ds_read_b128 v[52:55], v106 offset:35840\n"
    "ds_read_b128 v[56:59], v107 offset:49152\n"
    "ds_read_b128 v[60:63], v107 offset:50176\n"
    "s_waitcnt lgkmcnt(4)\n"
    "v_pk_add_f32 v[32:33], v[32:33], v[40:41]\n"
    "v_pk_add_f32 v[34:35], v[34:35], v[42:43]\n"
    "v_pk_add_f32 v[36:37], v[36:37], v[44:45]\n"
    "v_pk_add_f32 v[38:39], v[38:39], v[46:47]\n"
    "s_waitcnt lgkmcnt(2)\n"
    "v_pk_add_f32 v[32:33], v[32:33], v[48:49]\n"
    "v_pk_add_f32 v[34:35], v[34:35], v[50:51]\n"
    "v_pk_add_f32 v[36:37], v[36:37], v[52:53]\n"
    "v_pk_add_f32 v[38:39], v[38:39], v[54:55]\n"
    "s_waitcnt lgkmcnt(0)\n"
    "v_pk_add_f32 v[32:33], v[32:33], v[56:57]\n"
    "v_pk_add_f32 v[34:35], v[34:35], v[58:59]\n"
    "v_pk_add_f32 v[36:37], v[36:37], v[60:61]\n"
    "v_pk_add_f32 v[38:39], v[38:39], v[62:63]\n"
    "v_pk_fma_f32 v[32:33], v[32:33], v[192:193], v[232:233]\n"
    "v_pk_fma_f32 v[34:35], v[34:35], v[194:195], v[234:235]\n"
    "v_pk_fma_f32 v[36:37], v[36:37], v[196:197], v[236:237]\n"
    "v_pk_fma_f32 v[38:39], v[38:39], v[198:199], v[238:239]\n"
    "v_cvt_pk_bf16_f32 v40, v32, v33\n"
    "v_cvt_pk_bf16_f32 v41, v34, v35\n"
    "v_cvt_pk_bf16_f32 v42, v36, v37\n"
    "v_cvt_pk_bf16_f32 v43, v38, v39\n"
    "s_add_u32 s69, s16, 0x100000\n"
    "s_nop 0\n"
    "v_permlane16_swap_b32 v40, v42\n"
    "v_permlane16_swap_b32 v41, v43\n"
    "buffer_store_dwordx4 v[40:43], v111, s[44:47], s69 offen\n"
    "s_branch L_end\n"
    "L_e8:\n"
    "s_barrier\n"
    "buffer_load_dwordx4 v[242:245], v230, s[76:79], s22 offen offset:1024 sc1\n"
    "s_branch L_e8_r\n"
    "L_es0:\n"
    "s_waitcnt vmcnt(4)\n"
    "v_mov_b64 v[232:233], v[242:243]\n"
    "v_mov_b64 v[234:235], v[244:245]\n"
    "buffer_store_dwordx4 v[242:245], v230, s[76:79], s22 offen offset:0 sc1\n"
    "s_branch L_es0_r\n"
    "L_el7:\n"
    "buffer_load_dwordx4 v[242:245], v230, s[76:79], s22 offen offset:1024 sc1\n"
    "s_branch L_el7_r\n"
    "L_es7:\n"
    "s_waitcnt vmcnt(8)\n"
    "v_cmp_ne_u64 vcc, v[242:243], v[232:233]\n"
    "v_cmp_ne_u64 s[4:5], v[244:245], v[234:235]\n"
    "s_or_b64 vcc, vcc, s[4:5]\n"
    "s_cbranch_vccz L_es7_r\n"
    "v_mov_b64 v[232:233], v[242:243]\n"
    "v_mov_b64 v[234:235], v[244:245]\n"
    "buffer_store_dwordx4 v[242:245], v230, s[76:79], s22 offen offset:0 sc1\n"
    "s_branch L_es7_r\n"
    "L_el1:\n"
    "buffer_load_dwordx4 v[242:245], v230, s[76:79], s22 offen offset:1024 sc1\n"
    "s_branch L_el1_r\n"
    "L_es1:\n"
    "s_waitcnt vmcnt(9)\n"
    "v_cmp_ne_u64 vcc, v[242:243], v[232:233]\n"
    "v_cmp_ne_u64 s[4:5], v[244:245], v[234:235]\n"
    "s_or_b64 vcc, vcc, s[4:5]\n"
    "s_cbranch_vccz L_es1_r\n"
    "v_mov_b64 v[232:233], v[242:243]\n"
    "v_mov_b64 v[234:235], v[244:245]\n"
    "buffer_store_dwordx4 v[242:245], v230, s[76:79], s22 offen offset:0 sc1\n"
    "s_branch L_es1_r\n"
    "L_el2:\n"
    "buffer_load_dwordx4 v[242:245], v230, s[76:79], s22 offen offset:1024 sc1\n"
    "s_branch L_el2_r\n"
    "L_es2:\n"
    "s_waitcnt vmcnt(10)\n"
    "v_cmp_ne_u64 vcc, v[242:243], v[232:233]\n"
    "v_cmp_ne_u64 s[4:5], v[244:245], v[234:235]\n"
    "s_or_b64 vcc, vcc, s[4:5]\n"
    "s_cbranch_vccz L_es2_r\n"
    "v_mov_b64 v[232:233], v[242:243]\n"
    "v_mov_b64 v[234:235], v[244:245]\n"
    "buffer_store_dwordx4 v[242:245], v230, s[76:79], s22 offen offset:0 sc1\n"
    "s_branch L_es2_r\n"
    "L_el3:\n"
    "buffer_load_dwordx4 v[242:245], v230, s[76:79], s22 offen offset:1024 sc1\n"
    "s_branch L_el3_r\n"
    "L_es3:\n"
    "s_waitcnt vmcnt(6)\n"
    "v_cmp_ne_u64 vcc, v[242:243], v[232:233]\n"
    "v_cmp_ne_u64 s[4:5], v[244:245], v[234:235]\n"
    "s_or_b64 vcc, vcc, s[4:5]\n"
    "s_cbranch_vccz L_es3_r\n"
    "v_mov_b64 v[232:233], v[242:243]\n"
    "v_mov_b64 v[234:235], v[244:245]\n"
    "buffer_store_dwordx4 v[242:245], v230, s[76:79], s22 offen offset:0 sc1\n"
    "s_branch L_es3_r\n"
    "L_el9:\n"
    "buffer_load_dwordx4 v[242:245], v190, s[76:79], s7 offen sc1\n"
    "s_branch L_el9_r\n"
    "L_eo:\n"
    "s_mov_b32 s14, 0x40000\n"
    "s_waitcnt vmcnt(11)\n"
    "L_eo1:\n"
    "v_xor_b32 v242, 1, v242\n"
    "v_xor_b32 v243, 1, v243\n"
    "v_xor_b32 v244, 1, v244\n"
    "v_xor_b32 v245, 1, v245\n"
    "v_or3_b32 v242, v242, v243, v244\n"
    "v_or_b32 v242, v242, v245\n"
    "v_cmp_ne_u32 vcc, 0, v242\n"
    "s_sub_u32 s14, s14, 1\n"
    "s_cbranch_vccz L_eo_r\n"
    "s_cbranch_scc1 L_eo_r\n"
    "buffer_load_dwordx4 v[242:245], v230, s[76:79], s22 offen offset:1024 sc1\n"
    "s_waitcnt vmcnt(0)\n"
    "buffer_store_dwordx4 v[242:245], v230, s[76:79], s22 offen offset:0 sc1\n"
    "s_nop 1\n"
    "buffer_load_dwordx4 v[242:245], v190, s[76:79], s7 offen sc1\n"
    "s_waitcnt vmcnt(0)\n"
    "s_branch L_eo1\n"
    "s_branch L_eo_r\n"
    "L_redo:\n"
    "s_waitcnt vmcnt(0) lgkmcnt(0)\n"
    "s_barrier\n"
    "s_cmp_lt_u32 s15, 0x40000000\n"
    "s_cselect_b32 s13, 3, 1\n"
    "s_load_dwordx4 s[8:11], %0, 64\n"
    "s_load_dwordx4 s[92:95], %0, 80\n"
    "s_load_dwordx8 s[48:55], %0, 0\n"
    "s_load_dwordx2 s[36:37], %0, 96\n"
    "s_load_dwordx8 s[24:31], %0, 32\n"
    "s_mov_b32 s71, %2\n"
    "s_mov_b32 s18, %1\n"
    "s_and_b32 s73, %1, 7\n"
    "s_lshl_b32 s73, s73, 2\n"
    "s_bfe_u32 s68, %1, 0x20003\n"
    "s_add_u32 s73, s73, s68\n"
    "s_lshr_b32 s72, %1, 5\n"
    "s_lshl_b32 s56, s71, 18\n"
    "s_lshl_b32 s68, s73, 8\n"
    "s_add_u32 s56, s56, s68\n"
    "s_lshl_b32 s57, s71, 12\n"
    "s_lshl_b32 s68, s72, 2\n"
    "s_add_u32 s68, s68, s71\n"
    "s_lshl_b32 s58, s68, 14\n"
    "s_lshl_b32 s60, s68, 16\n"
    "s_lshl_b32 s61, s71, 14\n"
    "s_add_u32 s61, s61, 0x10000\n"
    "s_add_u32 s4, %1, s72\n"
    "s_add_u32 s69, s4, 0\n"
    "s_and_b32 s69, s69, 3\n"
    "s_lshl_b32 s70, s69, 20\n"
    "s_add_u32 s80, s70, s56\n"
    "s_lshl_b32 s70, s69, 8\n"
    "s_add_u32 s84, s70, s60\n"
    "s_lshl_b32 s70, s69, 19\n"
    "s_add_u32 s88, s70, 0x100000\n"
    "v_mbcnt_lo_u32_b32 v127, -1, 0\n"
    "v_mbcnt_hi_u32_b32 v127, -1, v127\n"
    "v_lshlrev_b32 v190, 4, v127\n"
    "v_lshrrev_b32 v0, 3, v127\n"
    "v_bfe_u32 v1, v127, 4, 1\n"
    "v_bfe_u32 v2, v127, 1, 2\n"
    "v_xor_b32 v1, v1, v2\n"
    "v_lshlrev_b32 v1, 5, v1\n"
    "v_lshl_or_b32 v1, v0, 13, v1\n"
    "v_and_b32 v2, 1, v127\n"
    "v_lshl_or_b32 v184, v2, 4, v1\n"
    "v_xor_b32 v185, 64, v184\n"
    "v_lshrrev_b32 v1, 4, v127\n"
    "v_and_b32 v2, 7, v127\n"
    "v_xor_b32 v2, v2, v1\n"
    "v_lshlrev_b32 v2, 4, v2\n"
    "v_lshl_or_b32 v186, v0, 10, v2\n"
    "v_xor_b32 v187, 64, v186\n"
    "s_mov_b32 s78, 0x5a00000\n"
    "s_mov_b32 s79, 0x20000\n"
    "s_mov_b32 s38, 0x200000\n"
    "s_mov_b32 s39, 0x20000\n"
    "s_mov_b32 s42, 0x400000\n"
    "s_mov_b32 s43, 0x20000\n"
    "s_mov_b32 s46, 0x100000\n"
    "s_mov_b32 s47, 0x20000\n"
    "s_waitcnt lgkmcnt(0)\n"
    "s_mov_b32 s76, s10\n"
    "s_and_b32 s77, s11, 0xffff\n"
    "s_and_b32 s37, s37, 0xffff\n"
    "s_and_b32 s49, s49, 0xffff\n"
    "s_and_b32 s51, s51, 0xffff\n"
    "s_and_b32 s53, s53, 0xffff\n"
    "s_mov_b64 s[34:35], s[30:31]\n"
    "s_lshl_b32 s19, s71, 13\n"
    "s_add_u32 s19, s19, 0x20000\n"
    "s_add_u32 s69, s4, 1\n"
    "s_and_b32 s69, s69, 3\n"
    "s_lshl_b32 s70, s69, 20\n"
    "s_add_u32 s81, s70, s56\n"
    "s_lshl_b32 s70, s69, 8\n"
    "s_add_u32 s85, s70, s60\n"
    "s_lshl_b32 s70, s69, 19\n"
    "s_add_u32 s89, s70, 0x100000\n"
    "s_add_u32 s69, s4, 2\n"
    "s_and_b32 s69, s69, 3\n"
    "s_lshl_b32 s70, s69, 20\n"
    "s_add_u32 s82, s70, s56\n"
    "s_lshl_b32 s70, s69, 8\n"
    "s_add_u32 s86, s70, s60\n"
    "s_lshl_b32 s70, s69, 19\n"
    "s_add_u32 s90, s70, 0x100000\n"
    "s_add_u32 s69, s4, 3\n"
    "s_and_b32 s69, s69, 3\n"
    "s_lshl_b32 s70, s69, 20\n"
    "s_add_u32 s83, s70, s56\n"
    "s_lshl_b32 s70, s69, 8\n"
    "s_add_u32 s87, s70, s60\n"
    "s_lshl_b32 s70, s69, 19\n"
    "s_add_u32 s91, s70, 0x100000\n"
    "s_lshl_b32 s4, s72, 3\n"
    "s_add_u32 s69, s4, s71\n"
    "s_sub_u32 s4, 120, s4\n"
    "s_add_u32 s70, s4, s71\n"
    "s_lshl_b32 s64, s69, 12\n"
    "s_add_u32 s65, s64, 0x4000\n"
    "s_lshl_b32 s66, s70, 12\n"
    "s_add_u32 s67, s66, 0x4000\n"
    "s_lshl_b32 s70, s73, 14\n"
    "s_add_u32 s70, s70, s57\n"
    "s_add_u32 s70, s70, 0x2400000\n"
    "s_lshl_b32 s69, s72, 20\n"
    "s_add_u32 s62, s70, s69\n"
    "s_sub_u32 s69, 0xf00000, s69\n"
    "s_add_u32 s63, s70, s69\n"
    "s_lshr_b32 s15, %1, 3\n"
    "s_lshl_b32 s15, s15, 2\n"
    "s_add_u32 s15, s15, s71\n"
    "s_and_b32 s69, %1, 7\n"
    "s_lshl_b32 s30, s15, 3\n"
    "s_add_u32 s30, s30, s69\n"
    "s_lshl_b32 s69, s73, 20\n"
    "s_add_u32 s69, s69, 0x400000\n"
    "s_add_u32 s44, s10, s69\n"
    "s_addc_u32 s45, s11, 0\n"
    "s_and_b32 s45, s45, 0xffff\n"
    "s_lshl_b32 s12, s73, 7\n"
    "s_lshl_b32 s12, s12, 5\n"
    "s_add_u32 s12, s12, 0x5800000\n"
    "v_bfe_u32 v2, v127, 1, 3\n"
    "v_xor_b32 v2, v2, v1\n"
    "v_lshlrev_b32 v2, 4, v2\n"
    "v_and_b32 v3, 15, v127\n"
    "v_lshl_or_b32 v2, v3, 7, v2\n"
    "v_xor_b32 v3, 64, v2\n"
    "v_add_u32 v188, s61, v2\n"
    "v_add_u32 v189, s61, v3\n"
    "v_add_u32 v254, s19, v2\n"
    "v_add_u32 v255, s19, v3\n"
    "v_bfe_u32 v2, v127, 2, 2\n"
    "v_lshl_or_b32 v3, v1, 3, v2\n"
    "v_lshlrev_b32 v3, 7, v3\n"
    "v_and_b32 v4, 3, v127\n"
    "v_lshl_or_b32 v3, v4, 3, v3\n"
    "v_bfe_u32 v4, v127, 3, 2\n"
    "v_lshl_or_b32 v176, v4, 5, v3\n"
    "v_xor_b32 v177, 32, v176\n"
    "v_xor_b32 v178, 64, v176\n"
    "v_xor_b32 v179, 96, v176\n"
    "v_add_u32 v180, 0x10000, v176\n"
    "v_add_u32 v181, 0x10000, v177\n"
    "v_add_u32 v182, 0x10000, v178\n"
    "v_add_u32 v183, 0x10000, v179\n"
    "s_mov_b32 s58, 0x40000000\n"
    "v_mov_b32 v186, 0\n"
    "s_mov_b32 s74, 0x18000\n"
    "s_branch L_mid\n"
    "L_exact:\n"
    "v_mov_b32 v224, v188\n"
    "v_mov_b32 v225, v188\n"
    "v_mov_b32 v226, v188\n"
    "v_mov_b32 v227, v188\n"
    "s_mov_b32 s68, 0\n"
    "s_lshl_b32 s70, s71, 4\n"
    "L_ex1:\n"
    "s_lshl_b32 s69, s68, 12\n"
    "buffer_load_dwordx4 v[112:115], v190, s[44:47], s69 offen offset:0 sc1\n"
    "buffer_load_dwordx4 v[116:119], v190, s[44:47], s69 offen offset:1024 sc1\n"
    "buffer_load_dwordx4 v[120:123], v190, s[44:47], s69 offen offset:2048 sc1\n"
    "buffer_load_dwordx4 v[124:127], v190, s[44:47], s69 offen offset:3072 sc1\n"
    "s_sub_i32 s74, s64, s69\n"
    "s_ashr_i32 s74, s74, 8\n"
    "s_sub_i32 s74, s74, s70\n"
    "v_add_u32 v144, s74, v183\n"
    "s_sub_i32 s74, s65, s69\n"
    "s_ashr_i32 s74, s74, 8\n"
    "s_sub_i32 s74, s74, s70\n"
    "v_add_u32 v145, s74, v183\n"
    "s_sub_i32 s74, s66, s69\n"
    "s_ashr_i32 s74, s74, 8\n"
    "s_sub_i32 s74, s74, s70\n"
    "v_add_u32 v146, s74, v183\n"
    "s_sub_i32 s74, s67, s69\n"
    "s_ashr_i32 s74, s74, 8\n"
    "s_sub_i32 s74, s74, s70\n"
    "v_add_u32 v147, s74, v183\n"
    "s_waitcnt vmcnt(0)\n"
    "v_mfma_f32_16x16x32_bf16 v[128:131], v[112:115], v[0:3], 0\n"
    "v_mfma_f32_16x16x32_bf16 v[128:131], v[116:119], v[4:7], v[128:131]\n"
    "v_mfma_f32_16x16x32_bf16 v[128:131], v[120:123], v[8:11], v[128:131]\n"
    "v_mfma_f32_16x16x32_bf16 v[128:131], v[124:127], v[12:15], v[128:131]\n"
    "v_mfma_f32_16x16x32_bf16 v[132:135], v[112:115], v[16:19], 0\n"
    "v_mfma_f32_16x16x32_bf16 v[132:135], v[116:119], v[20:23], v[132:135]\n"
    "v_mfma_f32_16x16x32_bf16 v[132:135], v[120:123], v[24:27], v[132:135]\n"
    "v_mfma_f32_16x16x32_bf16 v[132:135], v[124:127], v[28:31], v[132:135]\n"
    "v_mfma_f32_16x16x32_bf16 v[136:139], v[112:115], v[32:35], 0\n"
    "v_mfma_f32_16x16x32_bf16 v[136:139], v[116:119], v[36:39], v[136:139]\n"
    "v_mfma_f32_16x16x32_bf16 v[136:139], v[120:123], v[40:43], v[136:139]\n"
    "v_mfma_f32_16x16x32_bf16 v[136:139], v[124:127], v[44:47], v[136:139]\n"
    "v_mfma_f32_16x16x32_bf16 v[140:143], v[112:115], v[48:51], 0\n"
    "v_mfma_f32_16x16x32_bf16 v[140:143], v[116:119], v[52:55], v[140:143]\n"
    "v_mfma_f32_16x16x32_bf16 v[140:143], v[120:123], v[56:59], v[140:143]\n"
    "v_mfma_f32_16x16x32_bf16 v[140:143], v[124:127], v[60:63], v[140:143]\n"
    "s_nop 7\n"
    "v_cmp_le_i32 vcc, 0, v144\n"
    "s_nop 1\n"
    "v_cndmask_b32 v148, v188, v128, vcc\n"
    "v_max_f32 v224, v224, v148\n"
    "v_cmp_le_i32 vcc, 1, v144\n"
    "s_nop 1\n"
    "v_cndmask_b32 v148, v188, v129, vcc\n"
    "v_max_f32 v224, v224, v148\n"
    "v_cmp_le_i32 vcc, 2, v144\n"
    "s_nop 1\n"
    "v_cndmask_b32 v148, v188, v130, vcc\n"
    "v_max_f32 v224, v224, v148\n"
    "v_cmp_le_i32 vcc, 3, v144\n"
    "s_nop 1\n"
    "v_cndmask_b32 v148, v188, v131, vcc\n"
    "v_max_f32 v224, v224, v148\n"
    "v_cmp_le_i32 vcc, 0, v145\n"
    "s_nop 1\n"
    "v_cndmask_b32 v148, v188, v132, vcc\n"
    "v_max_f32 v225, v225, v148\n"
    "v_cmp_le_i32 vcc, 1, v145\n"
    "s_nop 1\n"
    "v_cndmask_b32 v148, v188, v133, vcc\n"
    "v_max_f32 v225, v225, v148\n"
    "v_cmp_le_i32 vcc, 2, v145\n"
    "s_nop 1\n"
    "v_cndmask_b32 v148, v188, v134, vcc\n"
    "v_max_f32 v225, v225, v148\n"
    "v_cmp_le_i32 vcc, 3, v145\n"
    "s_nop 1\n"
    "v_cndmask_b32 v148, v188, v135, vcc\n"
    "v_max_f32 v225, v225, v148\n"
    "v_cmp_le_i32 vcc, 0, v146\n"
    "s_nop 1\n"
    "v_cndmask_b32 v148, v188, v136, vcc\n"
    "v_max_f32 v226, v226, v148\n"
    "v_cmp_le_i32 vcc, 1, v146\n"
    "s_nop 1\n"
    "v_cndmask_b32 v148, v188, v137, vcc\n"
    "v_max_f32 v226, v226, v148\n"
    "v_cmp_le_i32 vcc, 2, v146\n"
    "s_nop 1\n"
    "v_cndmask_b32 v148, v188, v138, vcc\n"
    "v_max_f32 v226, v226, v148\n"
    "v_cmp_le_i32 vcc, 3, v146\n"
    "s_nop 1\n"
    "v_cndmask_b32 v148, v188, v139, vcc\n"
    "v_max_f32 v226, v226, v148\n"
    "v_cmp_le_i32 vcc, 0, v147\n"
    "s_nop 1\n"
    "v_cndmask_b32 v148, v188, v140, vcc\n"
    "v_max_f32 v227, v227, v148\n"
    "v_cmp_le_i32 vcc, 1, v147\n"
    "s_nop 1\n"
    "v_cndmask_b32 v148, v188, v141, vcc\n"
    "v_max_f32 v227, v227, v148\n"
    "v_cmp_le_i32 vcc, 2, v147\n"
    "s_nop 1\n"
    "v_cndmask_b32 v148, v188, v142, vcc\n"
    "v_max_f32 v227, v227, v148\n"
    "v_cmp_le_i32 vcc, 3, v147\n"
    "s_nop 1\n"
    "v_cndmask_b32 v148, v188, v143, vcc\n"
    "v_max_f32 v227, v227, v148\n"
    "s_add_u32 s68, s68, 1\n"
    "s_cmp_lt_u32 s68, 128\n"
    "s_cbranch_scc1 L_ex1\n"
    "v_mov_b32 v228, v224\n"
    "v_mov_b32 v229, v225\n"
    "v_mov_b32 v230, v226\n"
    "v_mov_b32 v231, v227\n"
    "v_permlane32_swap_b32 v224, v228\n"
    "v_permlane32_swap_b32 v225, v229\n"
    "v_permlane32_swap_b32 v226, v230\n"
    "v_permlane32_swap_b32 v227, v231\n"
    "v_max_f32 v224, v224, v228\n"
    "v_max_f32 v225, v225, v229\n"
    "v_max_f32 v226, v226, v230\n"
    "v_max_f32 v227, v227, v231\n"
    "v_mov_b32 v228, v224\n"
    "v_mov_b32 v229, v225\n"
    "v_mov_b32 v230, v226\n"
    "v_mov_b32 v231, v227\n"
    "v_permlane16_swap_b32 v224, v228\n"
    "v_permlane16_swap_b32 v225, v229\n"
    "v_permlane16_swap_b32 v226, v230\n"
    "v_permlane16_swap_b32 v227, v231\n"
    "v_max_f32 v224, v224, v228\n"
    "v_max_f32 v225, v225, v229\n"
    "v_max_f32 v226, v226, v230\n"
    "v_max_f32 v227, v227, v231\n"
    "s_branch L_at_havemax\n"
    "L_end:\n"
   :: "s"(__builtin_amdgcn_kernarg_segment_ptr()), "s"(blockIdx.x), "s"(w) : "v0","v1","v2","v3","v4","v5","v6","v7","v8","v9","v10","v11","v12","v13","v14","v15","v16","v17","v18","v19","v20","v21","v22","v23","v24","v25","v26","v27","v28","v29","v30","v31","v32","v33","v34","v35","v36","v37","v38","v39","v40","v41","v42","v43","v44","v45","v46","v47","v48","v49","v50","v51","v52","v53","v54","v55","v56","v57","v58","v59","v60","v61","v62","v63","v64","v65","v66","v67","v68","v69","v70","v71","v72","v73","v74","v75","v76","v77","v78","v79","v80","v81","v82","v83","v84","v85","v86","v87","v88","v89","v90","v91","v92","v93","v94","v95","v96","v97","v98","v99","v100","v101","v102","v103","v104","v105","v106","v107","v108","v109","v110","v111","v112","v113","v114","v115","v116","v117","v118","v119","v120","v121","v122","v123","v124","v125","v126","v127","v128","v129","v130","v131","v132","v133","v134","v135","v136","v137","v138","v139","v140","v141","v142","v143","v144","v145","v146","v147","v148","v149","v150","v151","v152","v153","v154","v155","v156","v157","v158","v159","v160","v161","v162","v163","v164","v165","v166","v167","v168","v169","v170","v171","v172","v173","v174","v175","v176","v177","v178","v179","v180","v181","v182","v183","v184","v185","v186","v187","v188","v189","v190","v191","v192","v193","v194","v195","v196","v197","v198","v199","v200","v201","v202","v203","v204","v205","v206","v207","v208","v209","v210","v211","v212","v213","v214","v215","v216","v217","v218","v219","v220","v221","v222","v223","v224","v225","v226","v227","v228","v229","v230","v231","v232","v233","v234","v235","v236","v237","v238","v239","v240","v241","v242","v243","v244","v245","v246","v247","v248","v249","v250","v251","v252","v253","v254","v255","a0","a1","a2","a3","a4","a5","a6","a7","a8","a9","a10","a11","a12","a13","a14","a15","a16","a17","a18","a19","a20","a21","a22","a23","a24","a25","a26","a27","a28","a29","a30","a31","a32","a33","a34","a35","a36","a37","a38","a39","a40","a41","a42","a43","a44","a45","a46","a47","a48","a49","a50","a51","a52","a53","a54","a55","a56","a57","a58","a59","a60","a61","a62","a63","a64","a65","a66","a67","a68","a69","a70","a71","a72","a73","a74","a75","a76","a77","a78","a79","a80","a81","a82","a83","a84","a85","a86","a87","a88","a89","a90","a91","a92","a93","a94","a95","a96","a97","a98","a99","a100","a101","a102","a103","a104","a105","a106","a107","a108","a109","a110","a111","a112","a113","a114","a115","a116","a117","a118","a119","a120","a121","a122","a123","a124","a125","a126","a127","a128","a129","a130","a131","a132","a133","a134","a135","a136","a137","a138","a139","a140","a141","a142","a143","a144","a145","a146","a147","a148","a149","a150","a151","a152","a153","a154","a155","a156","a157","a158","a159","a160","a161","a162","a163","a164","a165","a166","a167","a168","a169","a170","a171","a172","a173","a174","a175","a176","a177","a178","a179","a180","a181","a182","a183","a184","a185","a186","a187","a188","a189","a190","a191","a192","a193","a194","a195","a196","a197","a198","a199","a200","a201","a202","a203","a204","a205","a206","a207","a208","a209","a210","a211","a212","a213","a214","a215","a216","a217","a218","a219","a220","a221","a222","a223","a224","a225","a226","a227","a228","a229","a230","a231","a232","a233","a234","a235","a236","a237","a238","a239","a240","a241","a242","a243","a244","a245","a246","a247","a248","a249","a250","a251","a252","a253","a254","a255","s4","s5","s6","s7","s8","s9","s10","s11","s12","s13","s14","s15","s16","s17","s18","s19","s20","s21","s22","s23","s24","s25","s26","s27","s28","s29","s30","s31","s33","s34","s35","s36","s37","s38","s39","s40","s41","s42","s43","s44","s45","s46","s47","s48","s49","s50","s51","s52","s53","s54","s55","s56","s57","s58","s59","s60","s61","s62","s63","s64","s65","s66","s67","s68","s69","s70","s71","s72","s73","s74","s75","s76","s77","s78","s79","s80","s81","s82","s83","s84","s85","s86","s87","s88","s89","s90","s91","s92","s93","s94","s95","s96","s97","s98","s99","vcc","scc","memory");
}

extern "C" void kernel_launch(void* const* d_in, const int* in_sizes, int n_in, void* d_out, int out_size, void* d_ws, size_t ws_size, hipStream_t stream){
  (void)hipFuncSetAttribute((const void*)k_mega, hipFuncAttributeMaxDynamicSharedMemorySize, 163840);
  k_mega<<<256,256,163840,stream>>>(d_in[3],d_in[4],d_in[2],d_out,d_in[5],d_in[7],d_in[9],d_in[0],d_in[8],d_ws,d_in[10],d_in[11],d_in[1]);
}
```
